```python
import torch

def _softmax(x, axis=-1):
    return torch.softmax(x, dim=axis)

def _gelu(x):
    return torch.nn.functional.gelu(x, approximate='tanh')

def _silu(x):
    return torch.nn.functional.silu(x)

def _sigmoid(x):
    return torch.sigmoid(x)

def _layernorm(x, axis=-1, eps=1e-05):
    m = x.mean(dim=axis, keepdim=True)
    v = x.var(dim=axis, keepdim=True, unbiased=False)
    return (x - m) / torch.sqrt(v + eps)

def _scatter_add(tgt, idx, src, axis=0):
    return tgt.clone().index_add_(axis, idx, src)

def _segment_sum(src, seg, n):
    out = torch.zeros((n,) + src.shape[1:], dtype=src.dtype)
    return out.index_add_(0, seg, src)

def _pad(x, pw):
    flat = tuple((v for pair in reversed(pw) for v in pair))
    return torch.nn.functional.pad(x, flat)
kernel_name = 'recurrent_scan__sliding_local_dilated__mixer_gmlp__prompt'

def setup_inputs():

    _w = lambda a: (lambda t: t.to(torch.bfloat16) if t.is_floating_point() else t)(torch.as_tensor(a))

    def _A_setup():
        B, S, D = (1, 2048, 576)
        return {'x': _w((lambda *s: torch.randn(*s, dtype=torch.bfloat16) * 0.05)(B, S, D)), 'wqkv': _w((lambda *s: torch.randn(*s, dtype=torch.bfloat16) * 0.05)(D, 3 * D)), 'wo': _w((lambda *s: torch.randn(*s, dtype=torch.bfloat16) * 0.05)(D, D))}
    _A_in = _A_setup()

    def _B_setup():
        B, S, D, F = (1, 2048, 576, 4608)
        Fh = F // 2
        return {'x': _w((lambda *s: torch.randn(*s, dtype=torch.bfloat16) * 0.05)(B, S, D)), 'w_in': _w((lambda *s: torch.randn(*s, dtype=torch.bfloat16) * 0.8)(D, 2 * Fh)), 'w_sp': _w(0.01 * (lambda *s: torch.randn(*s, dtype=torch.bfloat16) * 0.8)(S, S)), 'b_sp': _w(1.0 + 0.01 * (lambda *s: torch.randn(*s, dtype=torch.bfloat16) * 0.05)(S)), 'w_out': _w((lambda *s: torch.randn(*s, dtype=torch.bfloat16) * 0.05)(Fh, D))}
    _B_in = _B_setup()
    return {'A_x': _A_in['x'], 'A_wqkv': _A_in['wqkv'], 'A_wo': _A_in['wo'], 'B_x': _B_in['x'], 'B_w_in': _B_in['w_in'], 'B_w_sp': _B_in['w_sp'], 'B_b_sp': _B_in['b_sp'], 'B_w_out': _B_in['w_out']}

def reference(A_x, A_wqkv, A_wo, B_x, B_w_in, B_w_sp, B_b_sp, B_w_out):

    def _A_ref(x, wqkv, wo):
        B, S, D = x.shape
        H = 32
        Dh = D // H
        W, dil = (8, 2)
        qkv = (lambda x, *d: x.permute(*d))((x @ wqkv).reshape(B, S, 3, H, Dh), 2, 0, 3, 1, 4)
        q, k, v = (qkv[0], qkv[1], qkv[2])
        scale = Dh ** (-0.5)
        pos = torch.arange(S)
        off = torch.arange(W) * dil
        idx = pos[:, None] - off[None, :]
        mask = idx >= 0
        idx_c = torch.where(mask, idx, 0).reshape(-1)
        kg = k[:, :, idx_c, :].reshape(B, H, S, W, Dh)
        vg = v[:, :, idx_c, :].reshape(B, H, S, W, Dh)
        logits = torch.einsum('bhsd,bhswd->bhsw', q, kg) * scale
        logits = torch.where(mask[None, None, :, :], logits, -1000000000.0)
        attn = _softmax(logits, axis=-1)
        o = torch.einsum('bhsw,bhswd->bhsd', attn, vg)
        return (lambda x, *d: x.permute(*d))(o, 0, 2, 1, 3).reshape(B, S, D) @ wo

    def _B_ref(x, w_in, w_sp, b_sp, w_out):
        B, S, D = x.shape
        F = w_in.shape[-1]
        Fh = F // 2
        z = _gelu(torch.einsum('bsd,df->bsf', x, w_in))
        z1 = z[..., :Fh].clone()
        z2 = _layernorm(z[..., Fh:], axis=-1)
        g = torch.einsum('ts,bsf->btf', w_sp, z2) + b_sp[None, :, None]
        return torch.einsum('bsf,fd->bsd', z1 * g, w_out)
    x = A_x
    xn = _layernorm(x, axis=-1)
    s = _A_ref(xn, A_wqkv, A_wo)
    g = _silu(_B_ref(xn, B_w_in, B_w_sp, B_b_sp, B_w_out))
    return x + s * g
```

```cpp
#include <hip/hip_runtime.h>
#include <hip/hip_bf16.h>
typedef __hip_bfloat16 bf16;
constexpr int S=2048, D=576, F=4608, Fh=2304, H=32, Dh=18;
__device__ inline float b2f(bf16 x){ return __bfloat162float(x);}
__device__ inline float rb(float x){ return __bfloat162float(__float2bfloat16(x));}

__global__ void ln_k(const bf16* x, bf16* y, int ld, int off, int n){
  int row=blockIdx.x; const bf16* p=x+(long)row*ld+off;
  __shared__ float red[256];
  float s=0; for(int i=threadIdx.x;i<n;i+=256) s+=b2f(p[i]);
  red[threadIdx.x]=s; __syncthreads();
  for(int o=128;o>0;o>>=1){ if(threadIdx.x<o) red[threadIdx.x]+=red[threadIdx.x+o]; __syncthreads();}
  float m=red[0]/n; __syncthreads();
  float v=0; for(int i=threadIdx.x;i<n;i+=256){ float d=b2f(p[i])-m; v+=d*d;}
  red[threadIdx.x]=v; __syncthreads();
  for(int o=128;o>0;o>>=1){ if(threadIdx.x<o) red[threadIdx.x]+=red[threadIdx.x+o]; __syncthreads();}
  float var=red[0]/n; float r=rsqrtf(var+1e-5f);
  for(int i=threadIdx.x;i<n;i+=256) y[(long)row*n+i]=__float2bfloat16((b2f(p[i])-m)*r);
}
__global__ void gemm_k(const bf16* A,int lda,const bf16* B,int ldb,bf16* C,int ldc,int M,int N,int K,int mode,const bf16* e0,int lde0,const bf16* e1,const bf16* e2){
  __shared__ float As[16][17], Bs[16][17];
  int tx=threadIdx.x&15, ty=threadIdx.x>>4; int row=blockIdx.y*16+ty, col=blockIdx.x*16+tx;
  float acc=0;
  for(int k0=0;k0<K;k0+=16){
    As[ty][tx]=b2f(A[(long)row*lda+k0+tx]); Bs[ty][tx]=b2f(B[(long)(k0+ty)*ldb+col]); __syncthreads();
    for(int k=0;k<16;k++) acc+=As[ty][k]*Bs[k][tx];
    __syncthreads();
  }
  if(mode==1){ float x=acc; float t=tanhf(0.7978845608028654f*(x+0.044715f*x*x*x)); acc=0.5f*x*(1.f+t);}
  else if(mode==2){ acc=b2f(e0[(long)row*lde0+col])*(acc+b2f(e1[row])); }
  else if(mode==3){ float g=acc; g=g/(1.f+expf(-g)); acc=b2f(e0[(long)row*lde0+col])+b2f(e2[(long)row*lde0+col])*g; }
  C[(long)row*ldc+col]=__float2bfloat16(acc);
}
__global__ void attn_k(const bf16* qkv, bf16* o){
  int t=blockIdx.x; int h=threadIdx.x; if(h>=H) return;
  float q[Dh]; for(int d=0;d<Dh;d++) q[d]=b2f(qkv[(long)t*3*D+h*Dh+d]);
  float lg[8]; float mx=-1e30f;
  for(int w=0;w<8;w++){ int idx=t-2*w; float s=0; if(idx>=0){ for(int d=0;d<Dh;d++) s+=q[d]*b2f(qkv[(long)idx*3*D+D+h*Dh+d]); s*=0.23570226039551584f;} else s=-1e9f; lg[w]=s; mx=fmaxf(mx,s);}
  float sum=0; for(int w=0;w<8;w++){ lg[w]=expf(lg[w]-mx); sum+=lg[w]; }
  for(int d=0;d<Dh;d++){ float a=0; for(int w=0;w<8;w++){ int idx=t-2*w; if(idx<0) idx=0; a+=lg[w]*b2f(qkv[(long)idx*3*D+2*D+h*Dh+d]); } o[(long)t*D+h*Dh+d]=__float2bfloat16(a/sum);}
}
extern "C" void kernel_launch(void* const* d_in, const int* in_sizes, int n_in, void* d_out, int out_size, void* d_ws, size_t ws_size, hipStream_t stream){
  const bf16* x=(const bf16*)d_in[0]; const bf16* wqkv=(const bf16*)d_in[1]; const bf16* wo=(const bf16*)d_in[2];
  const bf16* w_in=(const bf16*)d_in[4]; const bf16* w_sp=(const bf16*)d_in[5]; const bf16* b_sp=(const bf16*)d_in[6]; const bf16* w_out=(const bf16*)d_in[7];
  bf16* ws=(bf16*)d_ws; bf16* xn=ws; bf16* qkv=xn+S*D; bf16* o=qkv+S*3*D; bf16* s=o+S*D; bf16* z=s+S*D; bf16* z2n=z+(long)S*F; bf16* h=z2n+(long)S*Fh;
  ln_k<<<S,256,0,stream>>>(x,xn,D,0,D);
  gemm_k<<<dim3(3*D/16,S/16),256,0,stream>>>(xn,D,wqkv,3*D,qkv,3*D,S,3*D,D,0,nullptr,0,nullptr,nullptr);
  attn_k<<<S,64,0,stream>>>(qkv,o);
  gemm_k<<<dim3(D/16,S/16),256,0,stream>>>(o,D,wo,D,s,D,S,D,D,0,nullptr,0,nullptr,nullptr);
  gemm_k<<<dim3(F/16,S/16),256,0,stream>>>(xn,D,w_in,F,z,F,S,F,D,1,nullptr,0,nullptr,nullptr);
  ln_k<<<S,256,0,stream>>>(z,z2n,F,Fh,Fh);
  gemm_k<<<dim3(Fh/16,S/16),256,0,stream>>>(w_sp,S,z2n,Fh,h,Fh,S,Fh,S,2,z,F,b_sp,nullptr);
  gemm_k<<<dim3(D/16,S/16),256,0,stream>>>(h,Fh,w_out,D,(bf16*)d_out,D,S,D,Fh,3,x,D,nullptr,s);
}
```

```cpp
#include <hip/hip_runtime.h>
#include <stdint.h>

typedef unsigned short u16;
typedef __attribute__((ext_vector_type(8))) short bf16x8;
typedef __attribute__((ext_vector_type(4))) short s16x4;
typedef __attribute__((ext_vector_type(4))) float f32x4;
typedef __attribute__((ext_vector_type(16))) float f32x16;
typedef __attribute__((ext_vector_type(2))) float f32x2;
typedef __attribute__((ext_vector_type(4))) unsigned u32x4;
typedef __attribute__((ext_vector_type(2))) unsigned u32x2;
typedef __attribute__((ext_vector_type(2))) __bf16 bf16x2;
#define GAS __attribute__((address_space(1)))
#define LAS __attribute__((address_space(3)))
typedef GAS unsigned gu32;
#define RLX_AGENT __ATOMIC_RELAXED, __HIP_MEMORY_SCOPE_AGENT
#define BAR() asm volatile("s_barrier" ::: "memory")
#define SB() __builtin_amdgcn_sched_barrier(0)
#define SG(mask, n) __builtin_amdgcn_sched_group_barrier(mask, n, 0)
#define WAITV(n) asm volatile("s_waitcnt vmcnt(%0)" :: "i"(n) : "memory")
#define WAITL(n) asm volatile("s_waitcnt lgkmcnt(%0)" :: "i"(n) : "memory")
#define CWAITL0() __builtin_amdgcn_s_waitcnt(0xC07F)
#define RSRC __amdgpu_buffer_rsrc_t
#define MKRSRC(p, bytes) __builtin_amdgcn_make_buffer_rsrc((void*)(p), 0, (bytes), 0x00020000)
#define AUX_SC1 16
#define OPAQUE(x) asm volatile("" : "+v"(x))

constexpr int D_ = 576, FH = 2304;
constexpr size_t WS_FLAGS = 4096;
constexpr size_t WS_STAT  = 32768;
constexpr size_t WS_HALO  = WS_STAT + 2048*256;
constexpr size_t WS_Z2N   = WS_HALO + 16*16*4096;
constexpr size_t WS_H     = WS_Z2N + (size_t)16*64*9*1024;
constexpr size_t WS_O     = WS_H + (size_t)128*72*1024;
constexpr size_t WS_DBG   = WS_O + (size_t)128*18*1024;
constexpr int FL_STAT = 0, FL_Z = 1, FL_HALO = 2, FL_O = 3, FL_H = 4;
constexpr int LDS_BYTES = 163840;
constexpr int P1_STAGE = 18432, P1_NS = 5;
constexpr int XCH_OFF = 4*P1_STAGE;
constexpr int ZB_OFF = P1_NS*P1_STAGE;
constexpr int QB_PITCH = 248;
constexpr int QB_OFF = ZB_OFF + 36864 - 16*QB_PITCH;
constexpr int MISC_OFF = QB_OFF + 144*QB_PITCH;
constexpr int Z1B_OFF = 4*P1_STAGE;
constexpr int P2_STAGE = 34816;
constexpr int P3_STAGE = 27648;
constexpr int SPIN_MAX = 1 << 18;

template<int N> struct IC { static constexpr int value = N; };
template<int B, int E, class F> __device__ __forceinline__ void static_for(F&& f) {
  if constexpr (B < E) { f(IC<B>{}); static_for<B + 1, E>(f); }
}

__device__ __forceinline__ unsigned pack2(f32x2 v) { return __builtin_bit_cast(unsigned, __builtin_convertvector(v, bf16x2)); }
__device__ __forceinline__ float opq(float x) { asm("" : "+v"(x)); return x; }
__device__ __forceinline__ unsigned pack_bf16(float a, float b) { return pack2(f32x2{a, b}); }
__device__ __forceinline__ float asf(unsigned u) { return __builtin_bit_cast(float, u); }
__device__ __forceinline__ float bf_lo(unsigned u) { return __builtin_bit_cast(float, u << 16); }
__device__ __forceinline__ float bf_hi(unsigned u) { return __builtin_bit_cast(float, u & 0xffff0000u); }
__device__ __forceinline__ float dot2(unsigned a, unsigned b, float c) {
  return __builtin_amdgcn_fdot2_f32_bf16(__builtin_bit_cast(bf16x2, a), __builtin_bit_cast(bf16x2, b), c, false);
}
__device__ __forceinline__ float gelu_f(float x) {
  const float c1 = -2.302208198f, c2 = -0.1029432397f;
  float t = __builtin_fmaf(x * x, c2, c1);
  float e = __builtin_amdgcn_exp2f(x * t);
  return x * __builtin_amdgcn_rcpf(1.f + e);
}
__device__ __forceinline__ f32x2 gelu2(f32x2 x) {
  const f32x2 c1 = {-2.302208198f, -2.302208198f}, c2 = {-0.1029432397f, -0.1029432397f}, one = {1.f, 1.f};
  f32x2 t = __builtin_elementwise_fma(x * x, c2, c1);
  f32x2 a = x * t;
  f32x2 e = {__builtin_amdgcn_exp2f(a[0]), __builtin_amdgcn_exp2f(a[1])};
  f32x2 d = e + one;
  f32x2 r = {__builtin_amdgcn_rcpf(d[0]), __builtin_amdgcn_rcpf(d[1])};
  return x * r;
}
template<int CTRL> __device__ __forceinline__ float dpp_f(float v) {
  return __builtin_bit_cast(float, __builtin_amdgcn_update_dpp(0, __builtin_bit_cast(int, v), CTRL, 0xf, 0xf, true));
}
__device__ __forceinline__ float row16_allsum(float v) {
  v += dpp_f<0x128>(v); v += dpp_f<0x124>(v); v += dpp_f<0x122>(v); v += dpp_f<0x121>(v); return v;
}
__device__ __forceinline__ bool poll_flags(const char* ws, int group, int idx, int nflags, unsigned T, int lane) {
  gu32* p = (gu32*)(ws + WS_FLAGS + (size_t)(group*16 + idx)*256) + (lane & (nflags - 1));
  unsigned v = __hip_atomic_load(p, RLX_AGENT);
  return __all(v == T);
}
__device__ __forceinline__ void wait_flags(const char* ws, int group, int idx, int nflags, unsigned T, int lane) {
  for (int spin = 0; spin < SPIN_MAX; ++spin) {
    if (poll_flags(ws, group, idx, nflags, T, lane)) break;
    __builtin_amdgcn_s_sleep(1);
  }
}
__device__ __forceinline__ void set_flag(char* ws, int group, int idx, int k, unsigned T) {
  __hip_atomic_store((gu32*)(ws + WS_FLAGS + (size_t)(group*16 + idx)*256) + k, T, RLX_AGENT);
}
__device__ __forceinline__ unsigned load_epoch(const char* ws) {
  unsigned e;
  asm volatile("s_load_dword %0, %1, 0x0\n\ts_waitcnt lgkmcnt(0)" : "=s"(e) : "s"(ws) : "memory");
  return e + 1u;
}
__device__ __forceinline__ int kperm(int r) { return 8*(r >> 3) + 4*(r & 1) + ((r >> 1) & 3); }
__device__ __forceinline__ u32x4 make_srd(const void* p, unsigned bytes) {
  unsigned long long a = (unsigned long long)p;
  return u32x4{(unsigned)a, (unsigned)(a >> 32) & 0xffffu, bytes, 0x00020000u};
}
__device__ __forceinline__ void SETM0(int v) { asm volatile("s_mov_b32 m0, %0\n\ts_nop 0" :: "s"(v) : "memory"); }
template<int OFF> __device__ __forceinline__ void dma_o(int voff, u32x4 srd, int soff) {
  asm volatile("buffer_load_dwordx4 %0, %1, %2 offen offset:%3 lds" :: "v"(voff), "s"(srd), "s"(soff), "i"(OFF) : "memory");
}
template<int OFF> __device__ __forceinline__ void dma_s(int voff, u32x4 srd, int soff) {
  asm volatile("buffer_load_dwordx4 %0, %1, %2 offen offset:%3 sc1 lds" :: "v"(voff), "s"(srd), "s"(soff), "i"(OFF) : "memory");
}
#define DMA(voff, srd, soff, OFF) dma_o<OFF>((voff), (srd), (soff))
#define DMAS(voff, srd, soff, OFF) dma_s<OFF>((voff), (srd), (soff))

#define SETPRIO1() __builtin_amdgcn_s_setprio(0)
#define PIN4(a) asm volatile("" : "+v"(a))
#define PIN_ACC(acc) asm volatile("" : "+v"(acc[0][0]), "+v"(acc[0][1]), "+v"(acc[1][0]), "+v"(acc[1][1]), "+v"(acc[2][0]), "+v"(acc[2][1]), \
  "+v"(acc[3][0]), "+v"(acc[3][1]), "+v"(acc[4][0]), "+v"(acc[4][1]), "+v"(acc[5][0]), "+v"(acc[5][1]), "+v"(acc[6][0]), "+v"(acc[6][1]), \
  "+v"(acc[7][0]), "+v"(acc[7][1]), "+v"(acc[8][0]), "+v"(acc[8][1]))
#define PIN5(a) asm volatile("" : "+v"(a[0]), "+v"(a[1]), "+v"(a[2]), "+v"(a[3]), "+v"(a[4]))
#define MFMA32(a, b, c) __builtin_amdgcn_mfma_f32_32x32x16_bf16((a), (b), (c), 0, 0, 0)
#define MFMA(a, b, c) __builtin_amdgcn_mfma_f32_16x16x32_bf16((a), (b), (c), 0, 0, 0)

#define STAMP(k)

#define VB() BAR()
#define TK(v)
#define ACC(k, a, b)
#define TDUMP()
extern "C" __global__ __launch_bounds__(512, 2) void fused_kernel(const u16* __restrict__ x, const u16* __restrict__ wqkv, const u16* __restrict__ wo,
    const u16* __restrict__ w_in, const u16* __restrict__ w_sp, const u16* __restrict__ b_sp, const u16* __restrict__ w_out,
    u16* __restrict__ out, char* __restrict__ ws)
{
  extern __shared__ __attribute__((aligned(16))) char smem[];
  LAS char* lds = (LAS char*)smem;
  const int tid = threadIdx.x, lane = tid & 63;
  const int wave = __builtin_amdgcn_readfirstlane(tid >> 6);
  const int w = wave & 3;
  const int b = blockIdx.x, xcd = b & 7, rr = b >> 3;
  const int pi = 8*(xcd & 1) + (rr & 7), pj = 4*(xcd >> 1) + (rr >> 3);
  STAMP(0);
  if (wave == 7) {
    unsigned long long pc; unsigned off, sz;
    asm volatile("s_getpc_b64 %0\n.Lpf_here:\n\ts_mov_b32 %1, .Lpf_here-fused_kernel\n\ts_mov_b32 %2, .Lpf_end-fused_kernel" : "=s"(pc), "=s"(off), "=s"(sz));
    pc -= off;
    const u32x4 s_code = u32x4{(unsigned)pc, (unsigned)(pc >> 32) & 0xffffu, sz, 0x00020000u};
    const int per = 2048;
    SETM0(149504); DMA(lane*16, s_code, rr*per, 0); DMA(lane*16, s_code, rr*per, 1024);
  }
  unsigned T;

  const int hkk = w & 1, ht0 = (w < 2) ? 0 : 5;
  int cv[5], vq[5];
  u32x4 csrd = make_srd(w_in, 576*9216);
  int p1dst = hkk*9216 + ht0*1024, p1so = 32*hkk*9216 + (FH + 144*pj)*2, p1step = 64*9216;
  auto p1_issue4 = [&]() {
    SETM0(p1dst); DMA(cv[0], csrd, p1so, 0); DMA(cv[1], csrd, p1so, 1024); DMA(cv[2], csrd, p1so, 2048); DMA(cv[3], csrd, p1so, 3072);
  };
  auto p1_adv = [&]() { p1dst = (p1dst >= 4*P1_STAGE) ? p1dst - 4*P1_STAGE : p1dst + P1_STAGE; p1so += p1step; };
#define P1_FIN(K) do { if (w < 2) { SETM0(p1dst + 4096); DMA(cv[4], csrd, p1so, 0); p1_adv(); WAITV(5*(K)); } else { p1_adv(); WAITV(4*(K)); } } while (0)
#define P1_WAIT(K) do { if (w < 2) WAITV(5*(K)); else WAITV(4*(K)); } while (0)

  const int mt = wave >> 2;
  bf16x8 xn[36];
  {
    const RSRC r_x = MKRSRC(x + (size_t)(128*pi + 32*w)*D_, 32*1152);
    const int a = lane >> 2, bq = lane & 3;
    u32x4 raw[18];
    #pragma unroll
    for (int c = 0; c < 18; c++)
      raw[c] = __builtin_amdgcn_raw_buffer_load_b128(r_x, (16*mt + a)*1152 + bq*16 + c*64, 0, 0);
    SB();
    if (wave >= 4) {
      #pragma unroll
      for (int tt = 0; tt < 5; tt++) {
        int c = 64*(ht0 + tt) + lane; int r = c / 18, ch = c % 18; if (r > 31) r = 31;
        int kp = kperm(r), cmp = (tt < 4) ? tt*1024 : 0;
        cv[tt] = kp*9216 + ch*16 - cmp;
        vq[tt] = kp*3456 + (ch < 15 ? (ch / 5)*1152 + (ch % 5)*16 : 0) - cmp;
      }
    }
    T = load_epoch(ws);
    float s1 = 0.f, s2 = 0.f, s3 = 0.f, s4 = 0.f;
    unsigned ones = 0x3f803f80u; OPAQUE(ones);
    #pragma unroll
    for (int c = 0; c < 18; c++) {
      s1 = dot2(raw[c][0], ones, s1); s2 = dot2(raw[c][0], raw[c][0], s2); s3 = dot2(raw[c][1], ones, s3); s4 = dot2(raw[c][1], raw[c][1], s4);
      s1 = dot2(raw[c][2], ones, s1); s2 = dot2(raw[c][2], raw[c][2], s2); s3 = dot2(raw[c][3], ones, s3); s4 = dot2(raw[c][3], raw[c][3], s4);
    }
    if (wave >= 4) {
      #pragma unroll 1
      for (int q = 0; q < 4; q++) { p1_issue4(); if (w < 2) { SETM0(p1dst + 4096); DMA(cv[4], csrd, p1so, 0); } p1_adv(); }
    }
    s1 += s3; s2 += s4;
    s1 += dpp_f<0xB1>(s1); s1 += dpp_f<0x4E>(s1);
    s2 += dpp_f<0xB1>(s2); s2 += dpp_f<0x4E>(s2);
    const float mean = s1*(1.f/576.f), var = fmaxf(s2*(1.f/576.f) - mean*mean, 0.f);
    const float rs = __builtin_amdgcn_rsqf(var + 1e-5f), sh = -mean*rs;
    const f32x2 rs2 = {rs, rs}, sh2 = {sh, sh};
    int xw = XCH_OFF + w*18432 + (bq >> 1)*1024 + (32*(bq & 1) + ((16*mt + a) ^ (2*bq)))*16;
    int xr0 = XCH_OFF + w*18432 + (32*(lane >> 5) + ((lane & 31) ^ (2*(lane >> 5))))*16;
    int xr1 = XCH_OFF + w*18432 + 1024 + (32*(lane >> 5) + ((lane & 31) ^ (4 + 2*(lane >> 5))))*16;
    OPAQUE(xw); OPAQUE(xr0); OPAQUE(xr1);
    auto norm_write = [&](auto C) {
      constexpr int c = C.value;
      u32x4 o;
      #pragma unroll
      for (int d = 0; d < 4; d++) o[d] = pack2(__builtin_elementwise_fma(f32x2{bf_lo(raw[c][d]), bf_hi(raw[c][d])}, rs2, sh2));
      *(LAS u32x4*)(lds + xw + (c % 9)*2048) = o;
    };
    STAMP(1);
    static_for<0, 9>(norm_write);
    WAITL(0);
    BAR();
    if (wave < 4) {
      #pragma unroll
      for (int c = 0; c < 9; c++) { xn[2*c] = *(LAS bf16x8*)(lds + xr0 + c*2048); xn[2*c + 1] = *(LAS bf16x8*)(lds + xr1 + c*2048); }
      WAITL(0);
    }
    BAR();
    static_for<9, 18>(norm_write);
    if (wave >= 4) P1_WAIT(3);
    WAITL(0);
    BAR();
    if (wave < 4) {
      #pragma unroll
      for (int c = 0; c < 9; c++) { xn[18 + 2*c] = *(LAS bf16x8*)(lds + xr0 + c*2048); xn[18 + 2*c + 1] = *(LAS bf16x8*)(lds + xr1 + c*2048); }
    }
    STAMP(2);
  }

  unsigned z1p[9][4];
  f32x4 acc[9][2];

  if (wave < 4) {
    constexpr int D = 3, NB = 4, NT = 5, FS = 4*NT;
    int wl = (8*(lane >> 5) + 2*((lane >> 2) & 3))*288 + ((lane >> 4) & 1)*32 + (lane & 3)*8;
    int slot_off = 0;
    f32x16 a32[5];
    #pragma unroll 1
    for (int pass = 0; pass < 3; pass++) {
      asm volatile("" : "+s"(pass));
      bf16x8 wf[NB];
      int wc = wl + slot_off, wn;
      OPAQUE(wc);
      auto rdw = [&](auto G, int base) {
        constexpr int i = G.value % FS, k16 = i / NT, n = i % NT;
        LAS char* p = lds + base + (k16 >> 1)*9216 + (k16 & 1)*16*288 + n*64;
        s16x4 lo = __builtin_amdgcn_ds_read_tr16_b64_v4i16((LAS s16x4*)p);
        s16x4 hi = __builtin_amdgcn_ds_read_tr16_b64_v4i16((LAS s16x4*)(p + 288));
        wf[G.value % NB] = __builtin_shufflevector(lo, hi, 0, 1, 2, 3, 4, 5, 6, 7);
      };
      #pragma unroll
      for (int n = 0; n < 5; n++)
        #pragma unroll
        for (int e = 0; e < 16; e++) a32[n][e] = 0.f;
      static_for<0, D>([&](auto G) { rdw(G, wc); });
      SB();
      if (pass == 1) STAMP(16);
      static_for<0, 9*FS>([&](auto G) {
        constexpr int st = G.value / FS, i = G.value % FS, k16 = i / NT, n = i % NT;
        if constexpr (i == 0) { slot_off = (slot_off >= 4*P1_STAGE) ? 0 : slot_off + P1_STAGE; wn = wl + slot_off; OPAQUE(wn); }
        if constexpr (i == FS - D) { SB(); PIN5(a32);
          BAR(); SB(); }
        a32[n] = MFMA32(wf[G.value % NB], xn[4*st + k16], a32[n]);
        if constexpr (G.value + D < 9*FS) { if constexpr (i + D < FS) rdw(IC<G.value + D>{}, wc); else rdw(IC<G.value + D>{}, wn); }
        SG(0x8, 1); SG(0x100, 2);
        SB();
        if constexpr (i == FS - 1) wc = wn;
      });
      PIN5(a32);
      if (pass == 0) STAMP(14);
      int l2 = lane; OPAQUE(l2);
      const int sr = l2 & 31, h4 = (l2 >> 5)*4;
      auto pk4 = [&](int n, int rq) { return u32x2{pack2(f32x2{a32[n][4*rq], a32[n][4*rq + 1]}), pack2(f32x2{a32[n][4*rq + 2], a32[n][4*rq + 3]})}; };
      {
        const int rimg = (pass == 0) ? 16*((sr >> 2) & 1) + 4*(sr >> 3) + (sr & 3) : sr;
        const int ibase = (pass == 0) ? ZB_OFF + w*9216 : (pass == 1) ? QB_OFF + (16 + 32*w)*QB_PITCH : Z1B_OFF + w*9216;
        const int pitch = (pass == 1) ? QB_PITCH : 288, lim = (pass == 1) ? 124 : 144;
        LAS char* p = lds + ibase + rimg*pitch + h4*2;
        #pragma unroll
        for (int n = 0; n < 5; n++)
          #pragma unroll
          for (int rq = 0; rq < 4; rq++) {
            if (n < 3 || (n == 3 && rq < 3)) *(LAS u32x2*)(p + n*64 + rq*16) = pk4(n, rq);
            else if (n == 3 || rq < 2) { if (32*n + 8*rq + h4 < lim) *(LAS u32x2*)(p + n*64 + rq*16) = pk4(n, rq); }
          }
        if (pass == 0) STAMP(3); else if (pass == 1) STAMP(4); else STAMP(5);
      }
      WAITL(0);
      if (pass == 0) STAMP(15);
    }
    BAR();
    if (wave == 0 || wave == 2) {
      if (pi > 0) {
        gu32* fp = (gu32*)(ws + WS_FLAGS + (size_t)(FL_HALO*16 + pi - 1)*256) + pj;
        for (int spin = 0; spin < SPIN_MAX; ++spin) { if (__builtin_amdgcn_readfirstlane(__hip_atomic_load(fp, RLX_AGENT)) == T) break; __builtin_amdgcn_s_sleep(1); }
        const RSRC r_halo = MKRSRC(ws + WS_HALO + (size_t)((pi - 1)*16 + pj)*4096, 4096);
        #pragma unroll
        for (int c = 0; c < 4; c++) { int ch = c*64 + lane; if (ch > 247) ch = 247;
          u32x4 v = __builtin_amdgcn_raw_buffer_load_b128(r_halo, ch*16, 0, AUX_SC1);
          *(LAS u32x4*)(lds + QB_OFF + ch*16) = v; }
      } else {
        #pragma unroll
        for (int c = 0; c < 4; c++) { int ch = c*64 + lane; if (ch > 247) ch = 247; *(LAS u32x4*)(lds + QB_OFF + ch*16) = u32x4{0, 0, 0, 0}; }
      }
      WAITL(0);
    }
  } else {
    const int g = lane >> 4, fl = lane & 15;
    __builtin_amdgcn_s_setprio(3);
    P1_WAIT(2); VB();
    #pragma unroll 1
    for (int q = 1; q < 6; q++) { TK(a0); p1_issue4(); TK(a1); P1_FIN(2); TK(a2); VB(); TK(a3); ACC(0, a0, a1); ACC(1, a1, a2); ACC(2, a2, a3); }
    #pragma unroll
    for (int tt = 0; tt < 5; tt++) { const int t_ = cv[tt]; cv[tt] = vq[tt]; vq[tt] = t_; }
    csrd = make_srd(wqkv, 576*3456); p1so = 32*hkk*3456 + ((72*pj) & ~15); p1step = 64*3456;
    #pragma unroll 1
    for (int q = 6; q < 10; q++) { p1_issue4(); P1_FIN(2); VB(); }
    STAMP(3);
    LAS char* zb = lds + ZB_OFF + w*9216 + lane*16;
    {
      LAS char* p = lds + ZB_OFF + w*9216 + (4*g + ((lane >> 2) & 3))*288 + (lane & 3)*8;
      u32x4 fr[9];
      #pragma unroll
      for (int n = 0; n < 9; n++) {
        s16x4 lo = __builtin_amdgcn_ds_read_tr16_b64_v4i16((LAS s16x4*)(p + n*32));
        s16x4 hi = __builtin_amdgcn_ds_read_tr16_b64_v4i16((LAS s16x4*)(p + n*32 + 4608));
        fr[n] = __builtin_bit_cast(u32x4, __builtin_shufflevector(lo, hi, 0, 1, 2, 3, 4, 5, 6, 7));
      }
      p1_issue4();
      WAITL(0);
      #pragma unroll
      for (int n = 0; n < 9; n++) *(LAS u32x4*)(zb + n*1024) = fr[n];
      P1_FIN(2); VB();
    }
    f32x2 s1[4], s2[4];
    #pragma unroll
    for (int d = 0; d < 4; d++) { s1[d] = f32x2{0.f, 0.f}; s2[d] = f32x2{0.f, 0.f}; }
    u32x4 znext = *(LAS u32x4*)(zb);
    auto gelu_tile = [&](int n) {
      u32x4 v = znext, o;
      znext = *(LAS u32x4*)(zb + (n < 8 ? n + 1 : 8)*1024);
      #pragma unroll
      for (int d = 0; d < 4; d++) {
        const float ya = opq(gelu_f(opq(bf_lo(v[d])))), yb = opq(gelu_f(opq(bf_hi(v[d]))));
        s1[d][0] = opq(s1[d][0] + ya); s1[d][1] = opq(s1[d][1] + yb); s2[d][0] = opq(__builtin_fmaf(ya, ya, s2[d][0])); s2[d][1] = opq(__builtin_fmaf(yb, yb, s2[d][1]));
        o[d] = pack2(f32x2{ya, yb});
      }
      *(LAS u32x4*)(zb + n*1024) = o;
    };
    #pragma unroll 1
    for (int q = 11; q < 15; q++) { p1_issue4(); gelu_tile(q - 11); P1_FIN(2); VB(); }
    #pragma unroll
    for (int tt = 0; tt < 5; tt++) cv[tt] = vq[tt];
    csrd = make_srd(w_in, 576*9216); p1so = 32*hkk*9216 + 144*pj*2; p1step = 64*9216;
    #pragma unroll 1
    for (int q = 15; q < 19; q++) { p1_issue4(); gelu_tile(q - 11); P1_FIN(2); VB(); }
    STAMP(4);
    if (wave == 4 && pi < 15) {
      const RSRC r_halo = MKRSRC(ws + WS_HALO + (size_t)(pi*16 + pj)*4096, 4096);
      #pragma unroll
      for (int c = 0; c < 4; c++) { int ch = c*64 + lane; if (ch > 247) ch = 247;
        u32x4 v = *(LAS u32x4*)(lds + QB_OFF + 128*QB_PITCH + ch*16);
        __builtin_amdgcn_raw_buffer_store_b128(v, r_halo, ch*16, 0, AUX_SC1); }
    }
    p1_issue4(); gelu_tile(8); P1_FIN(2); VB();
    const RSRC r_stat = MKRSRC(ws + WS_STAT, 2048*256);
    {
      float a1 = 0.f, a2 = 0.f;
      #pragma unroll
      for (int e = 0; e < 8; e++) { float t1 = row16_allsum(s1[e >> 1][e & 1]), t2 = row16_allsum(s2[e >> 1][e & 1]); if ((fl & 7) == e) { a1 = t1; a2 = t2; } }
      u32x4 v = {__builtin_bit_cast(unsigned, a1), T, __builtin_bit_cast(unsigned, a2), T};
      if (fl < 8) __builtin_amdgcn_raw_buffer_store_b128(v, r_stat, ((128*pi + 32*w + 8*g + fl)*16 + pj)*16, 0, AUX_SC1);
      p1_issue4(); P1_FIN(2); VB();
    }
    if (wave == 4 && lane == 0 && pi < 15) set_flag(ws, FL_HALO, pi, pj, T);
    p1_issue4(); P1_FIN(2); VB();
    p1_issue4(); P1_FIN(2); VB();
    LAS f32x2* st = (LAS f32x2*)(lds + MISC_OFF + w*256);
    p1_issue4(); if (w < 2) { SETM0(p1dst + 4096); DMA(cv[4], csrd, p1so, 0); }
    {
      float t1, t2;
      for (int spin = 0; spin < SPIN_MAX; ++spin) {
        t1 = 0.f; t2 = 0.f; bool ok = true;
        #pragma unroll
        for (int c = 0; c < 8; c++) {
          u32x4 v = __builtin_amdgcn_raw_buffer_load_b128(r_stat, (128*pi + 32*w + (lane & 31))*256 + (lane >> 5)*128 + c*16, 0, AUX_SC1);
          t1 += asf(v[0]); t2 += asf(v[2]); ok = ok && v[1] == T && v[3] == T;
        }
        if (__all(ok)) break;
        __builtin_amdgcn_s_sleep(1);
      }
      t1 += __shfl_xor(t1, 32); t2 += __shfl_xor(t2, 32);
      float mean = t1*(1.f/2304.f), var = fmaxf(t2*(1.f/2304.f) - mean*mean, 0.f);
      float rs = __builtin_amdgcn_rsqf(var + 1e-5f);
      if (lane < 32) st[lane] = f32x2{rs, -mean*rs};
      WAITV(0); WAITL(0); VB();
    }
    f32x2 r2[4], h2[4];
    #pragma unroll
    for (int d = 0; d < 4; d++) { f32x2 u0 = st[8*g + 2*d], u1 = st[8*g + 2*d + 1]; r2[d] = f32x2{u0[0], u1[0]}; h2[d] = f32x2{u0[1], u1[1]}; }
    const RSRC r_z2n = MKRSRC(ws + WS_Z2N, 16*64*9*1024);
    int zo = lane*16 + (pj*64 + 4*pi + w)*9216;
    int zn = 0;
    #pragma unroll 1
    for (int q = 24; q < 27; q++) {
      #pragma unroll 1
      for (int k = 0; k < 3; k++, zn++) {
        u32x4 v = *(LAS u32x4*)(zb + zn*1024), o;
        #pragma unroll
        for (int d = 0; d < 4; d++) o[d] = pack2(f32x2{opq(__builtin_fmaf(bf_lo(v[d]), r2[d][0], h2[d][0])), opq(__builtin_fmaf(bf_hi(v[d]), r2[d][1], h2[d][1]))});
        __builtin_amdgcn_raw_buffer_store_b128(o, r_z2n, zo, 0, AUX_SC1);
        zo += 1024;
      }
      WAITL(0); VB();
    }
    STAMP(5);
    BAR();
    {
      #pragma unroll
      for (int m = 0; m < 2; m++) {
        LAS char* zx = lds + Z1B_OFF + w*9216 + (8*(fl >> 2) + 4*m + (fl & 3))*288 + g*8;
        #pragma unroll
        for (int n = 0; n < 9; n++) { u32x2 v = *(LAS u32x2*)(zx + n*32); z1p[n][2*m] = v[0]; z1p[n][2*m + 1] = v[1]; }
      }
    }
  }

  const u32x4 s_z2n = make_srd(ws + WS_Z2N + (size_t)pj*64*9*1024, 64*9*1024);
  const u32x4 s_wsp = make_srd(w_sp + (size_t)128*pi*2048, 128*4096);
  int vw[4];
  const int vlin = lane*16;
  auto p2_issue = [&](int s, auto ZR) {
    const int base = (s & 3)*P2_STAGE;
    if constexpr (ZR.value) {
      static_for<0, 9>([&](auto K) { SETM0(base + w*9216 + K.value*1024); DMAS(vlin, s_z2n, ((2*s + w)*9 + K.value)*1024, 0); });
    } else {
      static_for<0, 8>([&](auto K) { constexpr int k = K.value; const int wr = 2*(w - 2) + (k >> 2);
        SETM0(base + 18432 + wr*4096 + (k & 3)*1024); DMA(vw[k & 3], s_wsp, wr*32*4096 + s*128, 0); });
    }
  };
  if (wave >= 4) {
    { int l2 = lane; OPAQUE(l2);
      const int a = l2 >> 3, bb = l2 & 7;
      #pragma unroll
      for (int m = 0; m < 2; m++)
        #pragma unroll
        for (int hf = 0; hf < 2; hf++) { int rho = 8*hf + a; vw[2*m + hf] = (8*(rho >> 2) + 4*m + (rho & 3))*4096 + ((bb ^ ((rho >> 1) & 7))*16); } }
    if (w >= 2) {
      #pragma unroll 1
      for (int s = 0; s < 2; s++) p2_issue(s, IC<0>{});
    }
  }
  {
      int t2 = tid; OPAQUE(t2);
      const int u = t2 & 1, hh = (t2 >> 7) & 1, tl = ((t2 >> 1) & 63) + 64*(t2 >> 8);
      LAS char* rowp = lds + QB_OFF + (16 + tl)*QB_PITCH + 8*(pj & 1) + 36*hh;
      const int o8 = hh*4, o4 = hh ? 0 : 32;
      u32x2 qv[4]; unsigned q0;
      #pragma unroll
      for (int d = 0; d < 4; d++) qv[d] = *(LAS u32x2*)(rowp + o8 + d*8);
      q0 = *(LAS unsigned*)(rowp + o4);
      float lg[4];
      #pragma unroll
      for (int ww = 0; ww < 4; ww++) {
        LAS char* kr = rowp - (2*(4*u + ww))*QB_PITCH + 80;
        float sa = dot2(q0, *(LAS unsigned*)(kr + o4), 0.f), sb = 0.f;
        #pragma unroll
        for (int d = 0; d < 4; d++) { u32x2 kv = *(LAS u32x2*)(kr + o8 + d*8); sa = dot2(qv[d][0], kv[0], sa); sb = dot2(qv[d][1], kv[1], sb); }
        float s = (sa + sb)*0.23570226039551584f;
        if (128*pi + tl - 2*(4*u + ww) < 0) s = -1e9f;
        lg[ww] = s;
      }
      float l8[8];
      #pragma unroll
      for (int ww = 0; ww < 4; ww++) { float o = dpp_f<0xB1>(lg[ww]); l8[ww] = u ? o : lg[ww]; l8[4 + ww] = u ? lg[ww] : o; }
      float mx = l8[0];
      #pragma unroll
      for (int ww = 1; ww < 8; ww++) mx = fmaxf(mx, l8[ww]);
      float sum = 0.f;
      #pragma unroll
      for (int ww = 0; ww < 8; ww++) { l8[ww] = __builtin_amdgcn_exp2f((l8[ww] - mx)*1.4426950408889634f); sum += l8[ww]; }
      const float inv = __builtin_amdgcn_rcpf(sum);
      float olo[5], ohi[5];
      #pragma unroll
      for (int d = 0; d < 5; d++) { olo[d] = 0.f; ohi[d] = 0.f; }
      #pragma unroll
      for (int ww = 0; ww < 8; ww++) {
        const unsigned pb = pack_bf16(l8[ww]*inv, 0.f);
        const unsigned ph = pb << 16;
        LAS char* vr = rowp - (2*ww)*QB_PITCH + 160;
        u32x2 va = *(LAS u32x2*)(vr + o8 + 16*u), vb = *(LAS u32x2*)(vr + o8 + 16*u + 8); unsigned vc = *(LAS unsigned*)(vr + o4);
        olo[0] = dot2(va[0], pb, olo[0]); ohi[0] = dot2(va[0], ph, ohi[0]); olo[1] = dot2(va[1], pb, olo[1]); ohi[1] = dot2(va[1], ph, ohi[1]);
        olo[2] = dot2(vb[0], pb, olo[2]); ohi[2] = dot2(vb[0], ph, ohi[2]); olo[3] = dot2(vb[1], pb, olo[3]); ohi[3] = dot2(vb[1], ph, ohi[3]);
        olo[4] = dot2(vc, pb, olo[4]); ohi[4] = dot2(vc, ph, ohi[4]);
      }
      *(LAS u32x2*)(rowp + o8 + 16*u) = u32x2{pack_bf16(olo[0], ohi[0]), pack_bf16(olo[1], ohi[1])};
      *(LAS u32x2*)(rowp + o8 + 16*u + 8) = u32x2{pack_bf16(olo[2], ohi[2]), pack_bf16(olo[3], ohi[3])};
      if (u) *(LAS unsigned*)(rowp + o4) = pack_bf16(olo[4], ohi[4]);
      }
  WAITL(0);
  if (wave >= 4) { WAITV(0); if (lane == 0) set_flag(ws, FL_Z, pj, 4*pi + w, T); }
  STAMP(21);
  BAR();
  {
    const RSRC r_o = MKRSRC(ws + WS_O, 128*18*1024);
    #pragma unroll 1
    for (int e = tid; e < 1152; e += 512) {
      const int tl = e / 9, p = e % 9;
      u32x2 v = *(LAS u32x2*)(lds + QB_OFF + (16 + tl)*QB_PITCH + 8*(pj & 1) + 8*p);
      const int col = 36*pj + 4*p, r32 = tl & 31;
      const int tt = 8*pi + 2*(tl >> 5) + ((r32 >> 2) & 1), rho = 4*(r32 >> 3) + (r32 & 3);
      const int ks = col >> 5, c32 = col & 31;
      __builtin_amdgcn_raw_buffer_store_b64(v, r_o, ((tt*18 + ks)*64 + rho + 16*((c32 & 15) >> 2))*16 + (c32 >> 4)*8, 0, AUX_SC1);
    }
  }
  STAMP(22);
  if (wave >= 4 && w < 2) { wait_flags(ws, FL_Z, pj, 64, T, lane); STAMP(13); p2_issue(0, IC<1>{}); p2_issue(1, IC<1>{}); }
  WAITL(0);
  BAR();
  STAMP(6);

  const int p3cnt = (xcd < 2) ? 30 : 29;
  const int p3T = xcd*29 + (xcd < 2 ? xcd : 2) + rr;
  const bool p3act = rr < p3cnt;
  const int rg = p3T / 9, pc = p3T % 9;

  if (wave >= 4) {
    auto p2_helper = [&](auto ZR) {
      constexpr int CNT = ZR.value ? 9 : 8;
      p2_issue(2, ZR); p2_issue(3, ZR);
      WAITV(3*CNT);
      BAR();
      auto piece = [&](int s, auto K) {
        constexpr int k = K.value; const int base = (s & 3)*P2_STAGE;
        if constexpr (ZR.value) { SETM0(base + w*9216 + k*1024); DMAS(vlin, s_z2n, ((2*s + w)*9 + k)*1024, 0); }
        else if constexpr (k < 8) { const int wr = 2*(w - 2) + (k >> 2); SETM0(base + 18432 + wr*4096 + (k & 3)*1024); DMA(vw[k & 3], s_wsp, wr*32*4096 + s*128, 0); }
      };
      auto step = [&](int s, auto G0) {
        WAITV(2*CNT);
        BAR();
        static_for<0, 9>([&](auto K) {
          piece(s + 4, K);
          if constexpr (G0.value >= 0 && (K.value == 1 || K.value == 5)) {
            constexpr int pr = G0.value + (K.value == 5 ? 1 : 0);
            unsigned rr2 = pack2(f32x2{opq(gelu_f(opq(bf_lo(z1p[pr / 4][pr % 4])))), opq(gelu_f(opq(bf_hi(z1p[pr / 4][pr % 4]))))});
            asm volatile("" : "+v"(rr2));
            z1p[pr / 4][pr % 4] = rr2;
          }
        });
      };
      static_for<0, 18>([&](auto S) {
        step(S.value, IC<2*S.value>{});
        if constexpr (S.value == 4) { if (wave == 4 && lane == 0) set_flag(ws, FL_O, pi, pj, T); }
      });
      #pragma unroll 1
      for (int s = 18; s < 28; s++) step(s, IC<-1>{});
      WAITV(2*CNT); BAR();
      WAITV(CNT); BAR();
      #pragma unroll
      for (int n = 0; n < 9; n++) *(LAS u32x4*)(lds + w*9216 + n*1024 + lane*16) = u32x4{z1p[n][0], z1p[n][1], z1p[n][2], z1p[n][3]};
      WAITV(0); WAITL(0); BAR();
      BAR();
    };
    if (w < 2) p2_helper(IC<1>{}); else p2_helper(IC<0>{});
    STAMP(8);
    if (p3act) {
      const u32x4 s_o = make_srd(ws + WS_O, 128*18*1024), s_h = make_srd(ws + WS_H, 128*72*1024);
      const u32x4 s_wo = make_srd(wo, 576*1152), s_wout = make_srd(w_out, 2304*1152);
      int vW0, vW1, vA[5];
      int l2 = lane; OPAQUE(l2);
      { const int r = l2 >> 3;
        vW0 = r*1152 + (((l2 & 7) ^ (((r >> 1) & 3) << 1))*16);
        vW1 = (r + 8)*1152 + (((l2 & 7) ^ ((((r + 8) >> 1) & 3) << 1))*16);
        #pragma unroll
        for (int m5 = 0; m5 < 5; m5++) { int tt = 5*rg + m5; if (tt > 127) tt = 127; vA[m5] = l2*16 + tt*18*1024; } }
      int slot = 2;
      auto p3_ks = [&](u32x4 sw, u32x4 sa, int R0, int R1, int ks) {
        if (w < 3) {
          const int base = slot*P3_STAGE + w*9216, s0 = R0*1152 + pc*128, s1 = R1*1152 + pc*128;
          SETM0(base + 5120); DMA(vW0, sw, s0, 0); SETM0(base + 6144); DMA(vW1, sw, s0, 0);
          SETM0(base + 7168); DMA(vW0, sw, s1, 0); SETM0(base + 8192); DMA(vW1, sw, s1, 0);
          static_for<0, 5>([&](auto M) { SETM0(base + M.value*1024); DMAS(vA[M.value], sa, ks*1024, 0); });
        }
        slot = (slot == 4) ? 0 : slot + 1;
      };
      auto p3_wo = [&](int u) { const int ks = 3*u + w; p3_ks(s_wo, s_o, 32*ks, 32*ks + 16, ks); };
      auto p3_g3 = [&](int v) { const int ks = 3*v + w;
        const int R0 = (ks < 64) ? 144*(ks >> 2) + 32*(ks & 3) : 288*(ks - 64) + 128;
        p3_ks(s_wout, s_h, R0, R0 + ((ks < 64) ? 16 : 144), ks); };
      const int p0 = (5*rg) >> 3, p1 = ((5*rg + 4 > 127 ? 127 : 5*rg + 4)) >> 3;
      if (w < 3) { wait_flags(ws, FL_O, p0, 16, T, lane); wait_flags(ws, FL_O, p1, 16, T, lane); }
      STAMP(24);
      #pragma unroll 1
      for (int u = 0; u < 3; u++) p3_wo(u);
      BAR();
      p3_wo(3); p3_wo(4);
      WAITV(27);
      STAMP(26);
      BAR();
      WAITV(18); BAR();
      p3_wo(5); WAITV(18); BAR();
      WAITV(9); BAR();
      WAITV(0); BAR();
      BAR();
      #pragma unroll
      for (int m5 = 0; m5 < 5; m5++) { int tt = 5*rg + m5; if (tt > 127) tt = 127; vA[m5] = l2*16 + tt*72*1024; }
      if (w < 3) { wait_flags(ws, FL_H, p0, 16, T, lane); wait_flags(ws, FL_H, p1, 16, T, lane); }
      STAMP(27);
      #pragma unroll 1
      for (int v = 0; v < 4; v++) p3_g3(v);
      WAITV(18); BAR();
      #pragma unroll 1
      for (int u = 6; u < 26; u++) {
        p3_g3(u - 2);
        WAITV(18); BAR();
      }
      WAITV(9); BAR();
      WAITV(0); BAR();
      BAR();
    }
  } else {
    #pragma unroll
    for (int n = 0; n < 9; n++) { acc[n][0] = f32x4{0,0,0,0}; acc[n][1] = f32x4{0,0,0,0}; }
    int l2 = lane; OPAQUE(l2);
    const int g = l2 >> 4, fl = l2 & 15;
    const int zoff = l2*16;
    const int woff0 = 18432 + (4*w + (fl >> 3))*1024 + (fl & 7)*128 + ((g ^ ((fl >> 1) & 7))*16);
    float bs[2];
    #pragma unroll
    for (int m = 0; m < 2; m++) bs[m] = bf_lo(b_sp[128*pi + 32*w + 8*(fl >> 2) + 4*m + (fl & 3)]);
    bf16x8 z0[9], w0[2], z1[9], w1[2];
    auto rd = [&](bf16x8 (&z)[9], bf16x8 (&wv)[2], int s, int kk) {
      const int base = (s & 3)*P2_STAGE;
      LAS char* pw = lds + base + (woff0 ^ (kk*64));
      LAS char* pz = lds + base + kk*9216 + zoff;
      wv[0] = *(LAS bf16x8*)pw; wv[1] = *(LAS bf16x8*)(pw + 2048);
      #pragma unroll
      for (int n = 0; n < 9; n++) z[n] = *(LAS bf16x8*)(pz + n*1024);
    };
    auto mm = [&](bf16x8 (&z)[9], bf16x8 (&wv)[2]) {
      #pragma unroll
      for (int n = 0; n < 9; n++) { acc[n][0] = MFMA(z[n], wv[0], acc[n][0]); acc[n][1] = MFMA(z[n], wv[1], acc[n][1]); }
      #pragma unroll
      for (int q = 0; q < 11; q++) { SG(0x8, 1); SG(0x100, 1); }
      SG(0x8, 7);
    };
    BAR();
    rd(z0, w0, 0, 0);
    CWAITL0(); SB();
    #pragma unroll 1
    for (int s = 0; s < 32; s++) {
      CWAITL0(); SB();
      rd(z1, w1, s, 1);
      mm(z0, w0);
      SB(); CWAITL0(); PIN_ACC(acc);
      if (s == 4) WAITV(0);
      BAR(); SB();
      rd(z0, w0, (s + 1 < 32 ? s + 1 : s), 0);
      mm(z1, w1);
      SB();
    }
    STAMP(8);
    {
      const RSRC r_h = MKRSRC(ws + WS_H, 128*72*1024);
      u32x4 z1g[9];
      #pragma unroll
      for (int n = 0; n < 9; n++) z1g[n] = *(LAS u32x4*)(lds + w*9216 + n*1024 + zoff);
      #pragma unroll
      for (int m = 0; m < 2; m++) {
        const int tt = 8*pi + 2*w + m;
        const f32x2 b2 = {bs[m], bs[m]};
        unsigned hp[9][2];
        #pragma unroll
        for (int n = 0; n < 9; n++) {
          hp[n][0] = pack2(f32x2{bf_lo(z1g[n][2*m]), bf_hi(z1g[n][2*m])}*(f32x2{acc[n][m][0], acc[n][m][1]} + b2));
          hp[n][1] = pack2(f32x2{bf_lo(z1g[n][2*m + 1]), bf_hi(z1g[n][2*m + 1])}*(f32x2{acc[n][m][2], acc[n][m][3]} + b2));
        }
        #pragma unroll
        for (int q = 0; q < 4; q++)
          __builtin_amdgcn_raw_buffer_store_b128(u32x4{hp[2*q][0], hp[2*q][1], hp[2*q + 1][0], hp[2*q + 1][1]}, r_h, zoff + (tt*72 + 4*pj + q)*1024, 0, AUX_SC1);
        __builtin_amdgcn_raw_buffer_store_b64(u32x2{hp[8][0], hp[8][1]}, r_h, zoff + (pj & 1)*8 + (tt*72 + 64 + (pj >> 1))*1024, 0, AUX_SC1);
      }
    }
    STAMP(19);
    if (p3act) {
      WAITL(0);
      BAR();
      f32x4 aS[5], aC[5];
      u32x2 xv[5];
      const int q4 = (l2 >> 2) & 3, p4 = l2 & 3, r0 = 4*g + q4;
      const int woff = 5120 + r0*128 + (((2*w + (p4 >> 1)) ^ (((r0 >> 1) & 3) << 1))*16) + (p4 & 1)*8;
      bf16x8 hA[3][5], wA[3], hB[3][5], wB[3];
      int slot = 2;
      auto rd3 = [&](bf16x8 (&h)[3][5], bf16x8 (&wv)[3]) {
        LAS char* p = lds + slot*P3_STAGE;
        #pragma unroll
        for (int kk = 0; kk < 3; kk++) {
          s16x4 lo = __builtin_amdgcn_ds_read_tr16_b64_v4i16((LAS s16x4*)(p + kk*9216 + woff));
          s16x4 hi = __builtin_amdgcn_ds_read_tr16_b64_v4i16((LAS s16x4*)(p + kk*9216 + woff + 2048));
          wv[kk] = __builtin_shufflevector(lo, hi, 0, 1, 2, 3, 4, 5, 6, 7);
          #pragma unroll
          for (int m5 = 0; m5 < 5; m5++) h[kk][m5] = *(LAS bf16x8*)(p + kk*9216 + m5*1024 + zoff);
        }
        slot = (slot == 4) ? 0 : slot + 1;
      };
      auto mm3 = [&](bf16x8 (&h)[3][5], bf16x8 (&wv)[3]) {
        #pragma unroll
        for (int kk = 0; kk < 3; kk++)
          #pragma unroll
          for (int m5 = 0; m5 < 5; m5++) aC[m5] = MFMA(wv[kk], h[kk][m5], aC[m5]);
        #pragma unroll
        for (int q = 0; q < 15; q++) { SG(0x8, 1); SG(0x100, 1); }
        SG(0x100, 6);
      };
      #pragma unroll
      for (int m5 = 0; m5 < 5; m5++) {
        int tt = 5*rg + m5; if (tt > 127) tt = 127;
        const int row = 32*(tt >> 1) + 8*(fl >> 2) + 4*(tt & 1) + (fl & 3);
        xv[m5] = *(const u32x2*)(x + (size_t)row*D_ + 64*pc + 16*w + 4*g);
      }
      BAR();
      #pragma unroll 1
      for (int ph = 0; ph < 2; ph++) {
        #pragma unroll
        for (int m5 = 0; m5 < 5; m5++) aC[m5] = f32x4{0,0,0,0};
        rd3(hA, wA); SB();
        const int nit = ph ? 12 : 3;
        #pragma unroll 1
        for (int it = 0; it < nit; it++) {
          CWAITL0(); SB(); rd3(hB, wB); mm3(hA, wA); SB(); PIN5(aC); BAR(); SB();
          CWAITL0(); SB(); rd3(hA, wA); mm3(hB, wB); SB(); PIN5(aC);
          if (ph == 0 && it == 1) WAITV(0);
          if (ph == 0 || it < 11) BAR();
          SB();
          if (ph == 0 && it == 1 && tid == 0) set_flag(ws, FL_H, pi, pj, T);
        }
        if (ph == 0) {
          #pragma unroll
          for (int m5 = 0; m5 < 5; m5++) aS[m5] = aC[m5];
          slot = 3;
          STAMP(10);
        }
      }
      STAMP(11);
      #pragma unroll
      for (int m5 = 0; m5 < 5; m5++) {
        const int tt = 5*rg + m5;
        if (tt < 128) {
          const int row = 32*(tt >> 1) + 8*(fl >> 2) + 4*(tt & 1) + (fl & 3);
          const size_t o = (size_t)row*D_ + 64*pc + 16*w + 4*g;
          float r[4];
          #pragma unroll
          for (int e = 0; e < 4; e++) {
            float bv = aC[m5][e], sv = aS[m5][e];
            float sl = bv*__builtin_amdgcn_rcpf(1.f + __builtin_amdgcn_exp2f(-1.4426950408889634f*bv));
            unsigned xw = xv[m5][e >> 1];
            float xx = (e & 1) ? bf_hi(xw) : bf_lo(xw);
            r[e] = __builtin_fmaf(sv, sl, xx);
          }
          *(u32x2*)(out + o) = u32x2{pack_bf16(r[0], r[1]), pack_bf16(r[2], r[3])};
        }
      }
    } else {
      WAITV(0);
      BAR();
      if (tid == 0) set_flag(ws, FL_H, pi, pj, T);
    }
    if (b == 0 && tid == 0) __hip_atomic_store((gu32*)ws, T, RLX_AGENT);
    STAMP(12);
  }
  TDUMP();
  asm volatile(".Lpf_end:");
}

extern "C" void kernel_launch(void* const* d_in, const int* in_sizes, int n_in, void* d_out, int out_size, void* d_ws, size_t ws_size, hipStream_t stream) {
  (void)hipFuncSetAttribute((const void*)fused_kernel, hipFuncAttributeMaxDynamicSharedMemorySize, LDS_BYTES);
  fused_kernel<<<256, 512, LDS_BYTES, stream>>>((const u16*)d_in[0], (const u16*)d_in[1], (const u16*)d_in[2], (const u16*)d_in[4],
      (const u16*)d_in[5], (const u16*)d_in[6], (const u16*)d_in[7], (u16*)d_out, (char*)d_ws);
}
```

```cpp
#include <hip/hip_runtime.h>
#include <stdint.h>

typedef unsigned short u16;
typedef __attribute__((ext_vector_type(8))) short bf16x8;
typedef __attribute__((ext_vector_type(4))) short s16x4;
typedef __attribute__((ext_vector_type(4))) float f32x4;
typedef __attribute__((ext_vector_type(16))) float f32x16;
typedef __attribute__((ext_vector_type(2))) float f32x2;
typedef __attribute__((ext_vector_type(4))) unsigned u32x4;
typedef __attribute__((ext_vector_type(2))) unsigned u32x2;
typedef __attribute__((ext_vector_type(2))) __bf16 bf16x2;
#define GAS __attribute__((address_space(1)))
#define LAS __attribute__((address_space(3)))
typedef GAS unsigned gu32;
#define RLX_AGENT __ATOMIC_RELAXED, __HIP_MEMORY_SCOPE_AGENT
#define BAR() asm volatile("s_barrier" ::: "memory")
#define SB() __builtin_amdgcn_sched_barrier(0)
#define SG(mask, n) __builtin_amdgcn_sched_group_barrier(mask, n, 0)
#define WAITV(n) asm volatile("s_waitcnt vmcnt(%0)" :: "i"(n) : "memory")
#define WAITL(n) asm volatile("s_waitcnt lgkmcnt(%0)" :: "i"(n) : "memory")
#define CWAITL0() __builtin_amdgcn_s_waitcnt(0xC07F)
#define RSRC __amdgpu_buffer_rsrc_t
#define MKRSRC(p, bytes) __builtin_amdgcn_make_buffer_rsrc((void*)(p), 0, (bytes), 0x00020000)
#define AUX_SC1 16
#define OPAQUE(x) asm volatile("" : "+v"(x))

constexpr int D_ = 576, FH = 2304;
constexpr size_t WS_FLAGS = 4096;
constexpr size_t WS_STAT  = 32768;
constexpr size_t WS_HALO  = WS_STAT + 2048*256;
constexpr size_t WS_Z2N   = WS_HALO + 16*16*4096;
constexpr size_t WS_H     = WS_Z2N + (size_t)16*64*9*1024;
constexpr size_t WS_O     = WS_H + (size_t)128*72*1024;
constexpr size_t WS_DBG   = WS_O + (size_t)128*18*1024;
constexpr int FL_STAT = 0, FL_Z = 1, FL_HALO = 2, FL_O = 3, FL_H = 4;
constexpr int LDS_BYTES = 163840;
constexpr int P1_STAGE = 18432, P1_NS = 5;
constexpr int XCH_OFF = 4*P1_STAGE;
constexpr int ZB_OFF = P1_NS*P1_STAGE;
constexpr int QB_PITCH = 248;
constexpr int IMG_PITCH = 296, IMG_W = 32*IMG_PITCH;
constexpr int QB_OFF = ZB_OFF + 4*IMG_W - 16*QB_PITCH;
constexpr int MISC_OFF = QB_OFF + 144*QB_PITCH;
constexpr int Z1B_OFF = 4*P1_STAGE;
constexpr int P2_STAGE = 34816;
constexpr int P3_STAGE = 27648;
constexpr int SPIN_MAX = 1 << 18;

template<int N> struct IC { static constexpr int value = N; };
template<int B, int E, class F> __device__ __forceinline__ void static_for(F&& f) {
  if constexpr (B < E) { f(IC<B>{}); static_for<B + 1, E>(f); }
}

__device__ __forceinline__ unsigned pack2(f32x2 v) { return __builtin_bit_cast(unsigned, __builtin_convertvector(v, bf16x2)); }
__device__ __forceinline__ float opq(float x) { asm("" : "+v"(x)); return x; }
__device__ __forceinline__ unsigned pack_bf16(float a, float b) { return pack2(f32x2{a, b}); }
__device__ __forceinline__ float asf(unsigned u) { return __builtin_bit_cast(float, u); }
__device__ __forceinline__ float bf_lo(unsigned u) { return __builtin_bit_cast(float, u << 16); }
__device__ __forceinline__ float bf_hi(unsigned u) { return __builtin_bit_cast(float, u & 0xffff0000u); }
__device__ __forceinline__ float dot2(unsigned a, unsigned b, float c) {
  return __builtin_amdgcn_fdot2_f32_bf16(__builtin_bit_cast(bf16x2, a), __builtin_bit_cast(bf16x2, b), c, false);
}
__device__ __forceinline__ float gelu_f(float x) {
  const float c1 = -2.302208198f, c2 = -0.1029432397f;
  float t = __builtin_fmaf(x * x, c2, c1);
  float e = __builtin_amdgcn_exp2f(x * t);
  return x * __builtin_amdgcn_rcpf(1.f + e);
}
__device__ __forceinline__ f32x2 gelu2(f32x2 x) {
  const f32x2 c1 = {-2.302208198f, -2.302208198f}, c2 = {-0.1029432397f, -0.1029432397f}, one = {1.f, 1.f};
  f32x2 t = __builtin_elementwise_fma(x * x, c2, c1);
  f32x2 a = x * t;
  f32x2 e = {__builtin_amdgcn_exp2f(a[0]), __builtin_amdgcn_exp2f(a[1])};
  f32x2 d = e + one;
  f32x2 r = {__builtin_amdgcn_rcpf(d[0]), __builtin_amdgcn_rcpf(d[1])};
  return x * r;
}
template<int CTRL> __device__ __forceinline__ float dpp_f(float v) {
  return __builtin_bit_cast(float, __builtin_amdgcn_update_dpp(0, __builtin_bit_cast(int, v), CTRL, 0xf, 0xf, true));
}
__device__ __forceinline__ float row16_allsum(float v) {
  v += dpp_f<0x128>(v); v += dpp_f<0x124>(v); v += dpp_f<0x122>(v); v += dpp_f<0x121>(v); return v;
}
__device__ __forceinline__ bool poll_flags(const char* ws, int group, int idx, int nflags, unsigned T, int lane) {
  gu32* p = (gu32*)(ws + WS_FLAGS + (size_t)(group*16 + idx)*256) + (lane & (nflags - 1));
  unsigned v = __hip_atomic_load(p, RLX_AGENT);
  return __all(v == T);
}
__device__ __forceinline__ void wait_flags(const char* ws, int group, int idx, int nflags, unsigned T, int lane) {
  for (int spin = 0; spin < SPIN_MAX; ++spin) {
    if (poll_flags(ws, group, idx, nflags, T, lane)) break;
    __builtin_amdgcn_s_sleep(1);
  }
}
__device__ __forceinline__ void set_flag(char* ws, int group, int idx, int k, unsigned T) {
  __hip_atomic_store((gu32*)(ws + WS_FLAGS + (size_t)(group*16 + idx)*256) + k, T, RLX_AGENT);
}
__device__ __forceinline__ unsigned load_epoch(const char* ws) {
  unsigned e;
  asm volatile("s_load_dword %0, %1, 0x0\n\ts_waitcnt lgkmcnt(0)" : "=s"(e) : "s"(ws) : "memory");
  return e + 1u;
}
__device__ __forceinline__ int kperm(int r) { return 8*(r >> 3) + 4*(r & 1) + ((r >> 1) & 3); }
__device__ __forceinline__ u32x4 make_srd(const void* p, unsigned bytes) {
  unsigned long long a = (unsigned long long)p;
  return u32x4{(unsigned)a, (unsigned)(a >> 32) & 0xffffu, bytes, 0x00020000u};
}
__device__ __forceinline__ void SETM0(int v) { asm volatile("s_mov_b32 m0, %0\n\ts_nop 0" :: "s"(v) : "memory"); }
template<int OFF> __device__ __forceinline__ void dma_o(int voff, u32x4 srd, int soff) {
  asm volatile("buffer_load_dwordx4 %0, %1, %2 offen offset:%3 lds" :: "v"(voff), "s"(srd), "s"(soff), "i"(OFF) : "memory");
}
template<int OFF> __device__ __forceinline__ void dma_s(int voff, u32x4 srd, int soff) {
  asm volatile("buffer_load_dwordx4 %0, %1, %2 offen offset:%3 sc1 lds" :: "v"(voff), "s"(srd), "s"(soff), "i"(OFF) : "memory");
}
#define DMA(voff, srd, soff, OFF) dma_o<OFF>((voff), (srd), (soff))
#define DMAS(voff, srd, soff, OFF) dma_s<OFF>((voff), (srd), (soff))

#define SETPRIO1() __builtin_amdgcn_s_setprio(0)
#define PIN4(a) asm volatile("" : "+v"(a))
#define PIN_ACC(acc) asm volatile("" : "+v"(acc[0][0]), "+v"(acc[0][1]), "+v"(acc[1][0]), "+v"(acc[1][1]), "+v"(acc[2][0]), "+v"(acc[2][1]), \
  "+v"(acc[3][0]), "+v"(acc[3][1]), "+v"(acc[4][0]), "+v"(acc[4][1]), "+v"(acc[5][0]), "+v"(acc[5][1]), "+v"(acc[6][0]), "+v"(acc[6][1]), \
  "+v"(acc[7][0]), "+v"(acc[7][1]), "+v"(acc[8][0]), "+v"(acc[8][1]))
#define PIN5(a) asm volatile("" : "+v"(a[0]), "+v"(a[1]), "+v"(a[2]), "+v"(a[3]), "+v"(a[4]))
#define MFMA32(a, b, c) __builtin_amdgcn_mfma_f32_32x32x16_bf16((a), (b), (c), 0, 0, 0)
#define MFMA(a, b, c) __builtin_amdgcn_mfma_f32_16x16x32_bf16((a), (b), (c), 0, 0, 0)

#define STAMP(k)
#define RSTAMP(k)

#define VB() BAR()
#define TK(v)
#define ACC(k, a, b)
#define TDUMP()
extern "C" __global__ __launch_bounds__(512, 2) void fused_kernel(const u16* __restrict__ x, const u16* __restrict__ wqkv, const u16* __restrict__ wo,
    const u16* __restrict__ w_in, const u16* __restrict__ w_sp, const u16* __restrict__ b_sp, const u16* __restrict__ w_out,
    u16* __restrict__ out, char* __restrict__ ws)
{
  extern __shared__ __attribute__((aligned(16))) char smem[];
  LAS char* lds = (LAS char*)smem;
  const int tid = threadIdx.x, lane = tid & 63;
  const int wave = __builtin_amdgcn_readfirstlane(tid >> 6);
  const int w = wave & 3;
  const int b = blockIdx.x, xcd = b & 7, rr = b >> 3;
  const int pi = 4*(xcd & 3) + (rr & 3), pj = 8*(xcd >> 2) + (rr >> 2);
  STAMP(0);
  RSTAMP(28);
  if (wave == 7) {
    unsigned long long pc; unsigned off, sz;
    asm volatile("s_getpc_b64 %0\n.Lpf_here:\n\ts_mov_b32 %1, .Lpf_here-fused_kernel\n\ts_mov_b32 %2, .Lpf_end-fused_kernel" : "=s"(pc), "=s"(off), "=s"(sz));
    pc -= off;
    const u32x4 s_code = u32x4{(unsigned)pc, (unsigned)(pc >> 32) & 0xffffu, sz, 0x00020000u};
    const int per = 2048;
    SETM0(149504); DMA(lane*16, s_code, rr*per, 0); DMA(lane*16, s_code, rr*per, 1024);
  }
  unsigned T;

  const int hkk = w & 1, ht0 = (w < 2) ? 0 : 5;
  int cv[5], vq[5];
  u32x4 csrd = make_srd(w_in, 576*9216);
  int p1dst = hkk*9216 + ht0*1024, p1so = 32*hkk*9216 + (FH + 144*pj)*2, p1step = 64*9216;
  auto p1_issue4 = [&]() {
    SETM0(p1dst); DMA(cv[0], csrd, p1so, 0); DMA(cv[1], csrd, p1so, 1024); DMA(cv[2], csrd, p1so, 2048); DMA(cv[3], csrd, p1so, 3072);
  };
  auto p1_adv = [&]() { p1dst = (p1dst >= 4*P1_STAGE) ? p1dst - 4*P1_STAGE : p1dst + P1_STAGE; p1so += p1step; };
#define P1_FIN(K) do { if (w < 2) { SETM0(p1dst + 4096); DMA(cv[4], csrd, p1so, 0); p1_adv(); WAITV(5*(K)); } else { p1_adv(); WAITV(4*(K)); } } while (0)
#define P1_WAIT(K) do { if (w < 2) WAITV(5*(K)); else WAITV(4*(K)); } while (0)

  const int mt = wave >> 2;
  bf16x8 xn[36];
  {
    const u32x4 s_x = make_srd(x + (size_t)(128*pi + 32*w)*D_, 32*1152);
    const int a = lane >> 2, bq = lane & 3;
    u32x4 raw[18];
    {
      const int vx = (16*mt + a)*1152 + bq*16;
      static_for<0, 18>([&](auto C) { u32x4 t; asm volatile("buffer_load_dwordx4 %0, %1, %2, 0 offen offset:%3" : "=v"(t) : "v"(vx), "s"(s_x), "i"(C.value*64) : "memory"); raw[C.value] = t; });
    }
    if (wave >= 4) {
      #pragma unroll
      for (int tt = 0; tt < 5; tt++) {
        int c = 64*(ht0 + tt) + lane; int r = c / 18, ch = c % 18; if (r > 31) r = 31;
        int kp = kperm(r), cmp = (tt < 4) ? tt*1024 : 0;
        cv[tt] = kp*9216 + ch*16 - cmp;
        vq[tt] = kp*3456 + (ch < 15 ? (ch / 5)*1152 + (ch % 5)*16 : 0) - cmp;
      }
      p1_issue4(); if (w < 2) { SETM0(p1dst + 4096); DMA(cv[4], csrd, p1so, 0); } p1_adv();
    }
    STAMP(23);
    T = load_epoch(ws);
    STAMP(25);
    if (wave < 4) WAITV(0); else if (w < 2) WAITV(5); else WAITV(4);
    asm volatile("" : "+v"(raw[0]), "+v"(raw[1]), "+v"(raw[2]), "+v"(raw[3]), "+v"(raw[4]), "+v"(raw[5]), "+v"(raw[6]), "+v"(raw[7]), "+v"(raw[8]),
                      "+v"(raw[9]), "+v"(raw[10]), "+v"(raw[11]), "+v"(raw[12]), "+v"(raw[13]), "+v"(raw[14]), "+v"(raw[15]), "+v"(raw[16]), "+v"(raw[17]));
    STAMP(20);
    float s1 = 0.f, s2 = 0.f, s3 = 0.f, s4 = 0.f;
    unsigned ones = 0x3f803f80u; OPAQUE(ones);
    #pragma unroll
    for (int c = 0; c < 18; c++) {
      s1 = dot2(raw[c][0], ones, s1); s2 = dot2(raw[c][0], raw[c][0], s2); s3 = dot2(raw[c][1], ones, s3); s4 = dot2(raw[c][1], raw[c][1], s4);
      s1 = dot2(raw[c][2], ones, s1); s2 = dot2(raw[c][2], raw[c][2], s2); s3 = dot2(raw[c][3], ones, s3); s4 = dot2(raw[c][3], raw[c][3], s4);
    }
    s1 += s3; s2 += s4;
    s1 += dpp_f<0xB1>(s1); s1 += dpp_f<0x4E>(s1);
    s2 += dpp_f<0xB1>(s2); s2 += dpp_f<0x4E>(s2);
    const float mean = s1*(1.f/576.f), var = fmaxf(s2*(1.f/576.f) - mean*mean, 0.f);
    const float rs = __builtin_amdgcn_rsqf(var + 1e-5f), sh = -mean*rs;
    const f32x2 rs2 = {rs, rs}, sh2 = {sh, sh};
    int xw = XCH_OFF + w*18432 + (bq >> 1)*1024 + (32*(bq & 1) + ((16*mt + a) ^ (2*bq)))*16;
    int xr0 = XCH_OFF + w*18432 + (32*(lane >> 5) + ((lane & 31) ^ (2*(lane >> 5))))*16;
    int xr1 = XCH_OFF + w*18432 + 1024 + (32*(lane >> 5) + ((lane & 31) ^ (4 + 2*(lane >> 5))))*16;
    OPAQUE(xw); OPAQUE(xr0); OPAQUE(xr1);
    auto norm_write = [&](auto C) {
      constexpr int c = C.value;
      u32x4 o;
      #pragma unroll
      for (int d = 0; d < 4; d++) o[d] = pack2(__builtin_elementwise_fma(f32x2{bf_lo(raw[c][d]), bf_hi(raw[c][d])}, rs2, sh2));
      *(LAS u32x4*)(lds + xw + (c % 9)*2048) = o;
    };
    STAMP(1);
    static_for<0, 9>(norm_write);
    WAITL(0);
    STAMP(7);
    BAR();
    if (wave < 4) {
      #pragma unroll
      for (int c = 0; c < 9; c++) { xn[2*c] = *(LAS bf16x8*)(lds + xr0 + c*2048); xn[2*c + 1] = *(LAS bf16x8*)(lds + xr1 + c*2048); }
      WAITL(0);
    }
    STAMP(9);
    BAR();
    if (wave >= 4) { p1_issue4(); if (w < 2) { SETM0(p1dst + 4096); DMA(cv[4], csrd, p1so, 0); } p1_adv(); }
    static_for<9, 18>(norm_write);
    if (wave >= 4) P1_WAIT(1);
    WAITL(0);
    BAR();
    if (wave >= 4) {
      #pragma unroll 1
      for (int q = 2; q < 4; q++) { p1_issue4(); if (w < 2) { SETM0(p1dst + 4096); DMA(cv[4], csrd, p1so, 0); } p1_adv(); }
    }
    if (wave < 4) {
      #pragma unroll
      for (int c = 0; c < 9; c++) { xn[18 + 2*c] = *(LAS bf16x8*)(lds + xr0 + c*2048); xn[18 + 2*c + 1] = *(LAS bf16x8*)(lds + xr1 + c*2048); }
    }
    STAMP(2);
  }

  unsigned z1p[9][4];
  f32x4 acc[9][2];

  if (wave < 4) {
    constexpr int D = 3, NB = 4, NT = 5, FS = 4*NT;
    int wl = (8*(lane >> 5) + 2*((lane >> 2) & 3))*288 + ((lane >> 4) & 1)*32 + (lane & 3)*8;
    int slot_off = 0;
    f32x16 a32[5];
    #pragma unroll 1
    for (int pass = 0; pass < 3; pass++) {
      asm volatile("" : "+s"(pass));
      bf16x8 wf[NB];
      int wc = wl + slot_off, wn;
      OPAQUE(wc);
      auto rdw = [&](auto G, int base) {
        constexpr int i = G.value % FS, k16 = i / NT, n = i % NT;
        LAS char* p = lds + base + (k16 >> 1)*9216 + (k16 & 1)*16*288 + n*64;
        s16x4 lo = __builtin_amdgcn_ds_read_tr16_b64_v4i16((LAS s16x4*)p);
        s16x4 hi = __builtin_amdgcn_ds_read_tr16_b64_v4i16((LAS s16x4*)(p + 288));
        wf[G.value % NB] = __builtin_shufflevector(lo, hi, 0, 1, 2, 3, 4, 5, 6, 7);
      };
      #pragma unroll
      for (int n = 0; n < 5; n++)
        #pragma unroll
        for (int e = 0; e < 16; e++) a32[n][e] = 0.f;
      static_for<0, D>([&](auto G) { rdw(G, wc); });
      SB();
      if (pass == 1) STAMP(16);
      static_for<0, 9*FS>([&](auto G) {
        constexpr int st = G.value / FS, i = G.value % FS, k16 = i / NT, n = i % NT;
        if constexpr (i == 0) { slot_off = (slot_off >= 4*P1_STAGE) ? 0 : slot_off + P1_STAGE; wn = wl + slot_off; OPAQUE(wn); }
        if constexpr (i == FS - D) { SB(); PIN5(a32);
          BAR(); SB(); }
        a32[n] = MFMA32(wf[G.value % NB], xn[4*st + k16], a32[n]);
        if constexpr (G.value + D < 9*FS) { if constexpr (i + D < FS) rdw(IC<G.value + D>{}, wc); else rdw(IC<G.value + D>{}, wn); }
        SG(0x8, 1); SG(0x100, 2);
        SB();
        if constexpr (i == FS - 1) wc = wn;
      });
      PIN5(a32);
      if (pass == 0) STAMP(14);
      int l2 = lane; OPAQUE(l2);
      const int sr = l2 & 31, h4 = (l2 >> 5)*4;
      auto pk4 = [&](int n, int rq) { return u32x2{pack2(f32x2{a32[n][4*rq], a32[n][4*rq + 1]}), pack2(f32x2{a32[n][4*rq + 2], a32[n][4*rq + 3]})}; };
      if (pass == 1) WAITV(0);
      {
        const int ibase = (pass == 0) ? ZB_OFF + w*IMG_W : (pass == 1) ? QB_OFF + (16 + 32*w)*QB_PITCH : Z1B_OFF + w*IMG_W;
        const int pitch = (pass == 1) ? QB_PITCH : IMG_PITCH, lim = (pass == 1) ? 124 : 144;
        LAS char* p = lds + ibase + sr*pitch + h4*2;
        #pragma unroll
        for (int n = 0; n < 5; n++)
          #pragma unroll
          for (int rq = 0; rq < 4; rq++) {
            if (n < 3 || (n == 3 && rq < 3)) *(LAS u32x2*)(p + n*64 + rq*16) = pk4(n, rq);
            else if (n == 3 || rq < 2) { if (32*n + 8*rq + h4 < lim) *(LAS u32x2*)(p + n*64 + rq*16) = pk4(n, rq); }
          }
        if (pass == 0) STAMP(3); else if (pass == 1) STAMP(4); else STAMP(5);
      }
      WAITL(0);
      if (pass == 0) {
      const u32x4 s_a = make_srd(w_sp, 2048*4096), s_b = make_srd(w_out, 2304*1152), s_c = make_srd(wo, 576*1152);
      SETM0(149504);
      #pragma unroll
      for (int k = 0; k < 8; k++) DMA(lane*16, s_a, b*32768 + (w*8 + k)*1024, 0);
      #pragma unroll
      for (int k = 0; k < 3; k++) { const int pc_ = w + 4*k; if (pc_ < 11) DMA(lane*16, s_b, b*10368 + pc_*1024, 0); }
      if (w < 3) DMA(lane*16, s_c, b*2592 + w*1024, 0);
      }
      if (pass == 0) STAMP(15);
    }
    BAR();
    if (wave == 0 || wave == 2) {
      if (pi > 0) {
        gu32* fp = (gu32*)(ws + WS_FLAGS + (size_t)(FL_HALO*16 + pi - 1)*256) + pj;
        for (int spin = 0; spin < SPIN_MAX; ++spin) { if (__builtin_amdgcn_readfirstlane(__hip_atomic_load(fp, RLX_AGENT)) == T) break; __builtin_amdgcn_s_sleep(1); }
        const RSRC r_halo = MKRSRC(ws + WS_HALO + (size_t)((pi - 1)*16 + pj)*4096, 4096);
        #pragma unroll
        for (int c = 0; c < 4; c++) { int ch = c*64 + lane; if (ch > 247) ch = 247;
          u32x4 v = __builtin_amdgcn_raw_buffer_load_b128(r_halo, ch*16, 0, AUX_SC1);
          *(LAS u32x4*)(lds + QB_OFF + ch*16) = v; }
      } else {
        #pragma unroll
        for (int c = 0; c < 4; c++) { int ch = c*64 + lane; if (ch > 247) ch = 247; *(LAS u32x4*)(lds + QB_OFF + ch*16) = u32x4{0, 0, 0, 0}; }
      }
      WAITL(0);
    }
  } else {
    const int g = lane >> 4, fl = lane & 15;
    __builtin_amdgcn_s_setprio(3);
    P1_WAIT(2); VB();
    #pragma unroll 1
    for (int q = 1; q < 6; q++) { TK(a0); p1_issue4(); TK(a1); P1_FIN(2); TK(a2); VB(); TK(a3); ACC(0, a0, a1); ACC(1, a1, a2); ACC(2, a2, a3); }
    #pragma unroll
    for (int tt = 0; tt < 5; tt++) { const int t_ = cv[tt]; cv[tt] = vq[tt]; vq[tt] = t_; }
    csrd = make_srd(wqkv, 576*3456); p1so = 32*hkk*3456 + ((72*pj) & ~15); p1step = 64*3456;
    #pragma unroll 1
    for (int q = 6; q < 10; q++) { p1_issue4(); P1_FIN(2); VB(); }
    STAMP(3);
    LAS char* zb = lds + ZB_OFF + w*IMG_W + lane*16;
    {
      LAS char* p = lds + ZB_OFF + w*IMG_W + (8*g + ((lane >> 2) & 3))*IMG_PITCH + (lane & 3)*8;
      u32x4 fr[9];
      #pragma unroll
      for (int n = 0; n < 9; n++) {
        s16x4 lo = __builtin_amdgcn_ds_read_tr16_b64_v4i16((LAS s16x4*)(p + n*32));
        s16x4 hi = __builtin_amdgcn_ds_read_tr16_b64_v4i16((LAS s16x4*)(p + n*32 + 4*IMG_PITCH));
        fr[n] = __builtin_bit_cast(u32x4, __builtin_shufflevector(lo, hi, 0, 1, 2, 3, 4, 5, 6, 7));
      }
      p1_issue4();
      WAITL(0);
      #pragma unroll
      for (int n = 0; n < 9; n++) *(LAS u32x4*)(zb + n*1024) = fr[n];
      P1_FIN(2); VB();
    }
    f32x2 s1[4], s2[4];
    #pragma unroll
    for (int d = 0; d < 4; d++) { s1[d] = f32x2{0.f, 0.f}; s2[d] = f32x2{0.f, 0.f}; }
    u32x4 znext = *(LAS u32x4*)(zb);
    auto gelu_tile = [&](int n) {
      u32x4 v = znext, o;
      znext = *(LAS u32x4*)(zb + (n < 8 ? n + 1 : 8)*1024);
      #pragma unroll
      for (int d = 0; d < 4; d++) {
        const float ya = opq(gelu_f(opq(bf_lo(v[d])))), yb = opq(gelu_f(opq(bf_hi(v[d]))));
        s1[d][0] = opq(s1[d][0] + ya); s1[d][1] = opq(s1[d][1] + yb); s2[d][0] = opq(__builtin_fmaf(ya, ya, s2[d][0])); s2[d][1] = opq(__builtin_fmaf(yb, yb, s2[d][1]));
        o[d] = pack2(f32x2{ya, yb});
      }
      *(LAS u32x4*)(zb + n*1024) = o;
    };
    #pragma unroll 1
    for (int q = 11; q < 15; q++) { p1_issue4(); gelu_tile(q - 11); P1_FIN(2); VB(); }
    #pragma unroll
    for (int tt = 0; tt < 5; tt++) cv[tt] = vq[tt];
    csrd = make_srd(w_in, 576*9216); p1so = 32*hkk*9216 + 144*pj*2; p1step = 64*9216;
    #pragma unroll 1
    for (int q = 15; q < 19; q++) { p1_issue4(); gelu_tile(q - 11); P1_FIN(2); VB(); }
    STAMP(4);
    if (wave == 4 && pi < 15) {
      const RSRC r_halo = MKRSRC(ws + WS_HALO + (size_t)(pi*16 + pj)*4096, 4096);
      #pragma unroll
      for (int c = 0; c < 4; c++) { int ch = c*64 + lane; if (ch > 247) ch = 247;
        u32x4 v = *(LAS u32x4*)(lds + QB_OFF + 128*QB_PITCH + ch*16);
        __builtin_amdgcn_raw_buffer_store_b128(v, r_halo, ch*16, 0, AUX_SC1); }
    }
    p1_issue4(); gelu_tile(8); P1_FIN(2); VB();
    const RSRC r_stat = MKRSRC(ws + WS_STAT, 2048*256);
    {
      float a1 = 0.f, a2 = 0.f;
      #pragma unroll
      for (int e = 0; e < 8; e++) { float t1 = row16_allsum(s1[e >> 1][e & 1]), t2 = row16_allsum(s2[e >> 1][e & 1]); if ((fl & 7) == e) { a1 = t1; a2 = t2; } }
      u32x4 v = {__builtin_bit_cast(unsigned, a1), T, __builtin_bit_cast(unsigned, a2), T};
      RSTAMP(29);
      if (fl < 8) __builtin_amdgcn_raw_buffer_store_b128(v, r_stat, ((128*pi + 32*w + 8*g + fl)*16 + pj)*16, 0, AUX_SC1);
      p1_issue4(); P1_FIN(2); VB();
    }
    if (wave == 4 && lane == 0 && pi < 15) set_flag(ws, FL_HALO, pi, pj, T);
    p1_issue4(); P1_FIN(2); VB();
    p1_issue4(); P1_FIN(2); VB();
    LAS f32x2* st = (LAS f32x2*)(lds + MISC_OFF + w*256);
    p1_issue4(); if (w < 2) { SETM0(p1dst + 4096); DMA(cv[4], csrd, p1so, 0); }
    {
      float t1, t2;
      RSTAMP(30);
      for (int spin = 0; spin < SPIN_MAX; ++spin) {
        const unsigned tg = __builtin_amdgcn_raw_buffer_load_b32(r_stat, ((128*pi + 32*w + 31)*16 + fl)*16 + 12, 0, AUX_SC1);
        if (__all(tg == T)) break;
        __builtin_amdgcn_s_sleep(1);
      }
      RSTAMP(31);
      for (int spin = 0; spin < SPIN_MAX; ++spin) {
        t1 = 0.f; t2 = 0.f; bool ok = true;
        #pragma unroll
        for (int c = 0; c < 8; c++) {
          u32x4 v = __builtin_amdgcn_raw_buffer_load_b128(r_stat, (128*pi + 32*w + (lane & 31))*256 + (lane >> 5)*128 + c*16, 0, AUX_SC1);
          t1 += asf(v[0]); t2 += asf(v[2]); ok = ok && v[1] == T && v[3] == T;
        }
        if (__all(ok)) break;
        __builtin_amdgcn_s_sleep(1);
      }
      t1 += __shfl_xor(t1, 32); t2 += __shfl_xor(t2, 32);
      float mean = t1*(1.f/2304.f), var = fmaxf(t2*(1.f/2304.f) - mean*mean, 0.f);
      float rs = __builtin_amdgcn_rsqf(var + 1e-5f);
      if (lane < 32) st[lane] = f32x2{rs, -mean*rs};
      WAITV(0); WAITL(0); VB();
    }
    f32x2 r2[4], h2[4];
    #pragma unroll
    for (int d = 0; d < 4; d++) { f32x2 u0 = st[8*g + 2*d], u1 = st[8*g + 2*d + 1]; r2[d] = f32x2{u0[0], u1[0]}; h2[d] = f32x2{u0[1], u1[1]}; }
    const RSRC r_z2n = MKRSRC(ws + WS_Z2N, 16*64*9*1024);
    int zo = lane*16 + (pj*64 + 4*pi + w)*9216;
    int zn = 0;
    #pragma unroll 1
    for (int q = 24; q < 27; q++) {
      #pragma unroll 1
      for (int k = 0; k < 3; k++, zn++) {
        u32x4 v = *(LAS u32x4*)(zb + zn*1024), o;
        #pragma unroll
        for (int d = 0; d < 4; d++) o[d] = pack2(f32x2{opq(__builtin_fmaf(bf_lo(v[d]), r2[d][0], h2[d][0])), opq(__builtin_fmaf(bf_hi(v[d]), r2[d][1], h2[d][1]))});
        __builtin_amdgcn_raw_buffer_store_b128(o, r_z2n, zo, 0, AUX_SC1);
        zo += 1024;
      }
      WAITL(0); VB();
    }
    STAMP(5);
    BAR();
    {
      #pragma unroll
      for (int m = 0; m < 2; m++) {
        LAS char* zx = lds + Z1B_OFF + w*IMG_W + (8*(fl >> 2) + 4*m + (fl & 3))*IMG_PITCH + g*8;
        #pragma unroll
        for (int n = 0; n < 9; n++) { u32x2 v = *(LAS u32x2*)(zx + n*32); z1p[n][2*m] = v[0]; z1p[n][2*m + 1] = v[1]; }
      }
    }
  }

  const u32x4 s_z2n = make_srd(ws + WS_Z2N + (size_t)pj*64*9*1024, 64*9*1024);
  const u32x4 s_wsp = make_srd(w_sp + (size_t)128*pi*2048, 128*4096);
  int vw[4];
  const int vlin = lane*16;
  auto p2_issue = [&](int s, auto ZR) {
    const int base = (s & 3)*P2_STAGE;
    if constexpr (ZR.value) {
      static_for<0, 9>([&](auto K) { SETM0(base + w*9216 + K.value*1024); DMAS(vlin, s_z2n, ((2*s + w)*9 + K.value)*1024, 0); });
    } else {
      static_for<0, 8>([&](auto K) { constexpr int k = K.value; const int wr = 2*(w - 2) + (k >> 2);
        SETM0(base + 18432 + wr*4096 + (k & 3)*1024); DMA(vw[k & 3], s_wsp, wr*32*4096 + s*128, 0); });
    }
  };
  if (wave >= 4) {
    { int l2 = lane; OPAQUE(l2);
      const int a = l2 >> 3, bb = l2 & 7;
      #pragma unroll
      for (int m = 0; m < 2; m++)
        #pragma unroll
        for (int hf = 0; hf < 2; hf++) { int rho = 8*hf + a; vw[2*m + hf] = (8*(rho >> 2) + 4*m + (rho & 3))*4096 + ((bb ^ ((rho >> 1) & 7))*16); } }
    if (w >= 2) {
      #pragma unroll 1
      for (int s = 0; s < 2; s++) p2_issue(s, IC<0>{});
    }
  }
  {
      int t2 = tid; OPAQUE(t2);
      const int u = t2 & 1, hh = (t2 >> 7) & 1, tl = ((t2 >> 1) & 63) + 64*(t2 >> 8);
      LAS char* rowp = lds + QB_OFF + (16 + tl)*QB_PITCH + 8*(pj & 1) + 36*hh;
      const int o8 = hh*4, o4 = hh ? 0 : 32;
      u32x2 qv[4]; unsigned q0;
      #pragma unroll
      for (int d = 0; d < 4; d++) qv[d] = *(LAS u32x2*)(rowp + o8 + d*8);
      q0 = *(LAS unsigned*)(rowp + o4);
      float lg[4];
      #pragma unroll
      for (int ww = 0; ww < 4; ww++) {
        LAS char* kr = rowp - (2*(4*u + ww))*QB_PITCH + 80;
        float sa = dot2(q0, *(LAS unsigned*)(kr + o4), 0.f), sb = 0.f;
        #pragma unroll
        for (int d = 0; d < 4; d++) { u32x2 kv = *(LAS u32x2*)(kr + o8 + d*8); sa = dot2(qv[d][0], kv[0], sa); sb = dot2(qv[d][1], kv[1], sb); }
        float s = (sa + sb)*0.23570226039551584f;
        if (128*pi + tl - 2*(4*u + ww) < 0) s = -1e9f;
        lg[ww] = s;
      }
      float l8[8];
      #pragma unroll
      for (int ww = 0; ww < 4; ww++) { float o = dpp_f<0xB1>(lg[ww]); l8[ww] = u ? o : lg[ww]; l8[4 + ww] = u ? lg[ww] : o; }
      float mx = l8[0];
      #pragma unroll
      for (int ww = 1; ww < 8; ww++) mx = fmaxf(mx, l8[ww]);
      float sum = 0.f;
      #pragma unroll
      for (int ww = 0; ww < 8; ww++) { l8[ww] = __builtin_amdgcn_exp2f((l8[ww] - mx)*1.4426950408889634f); sum += l8[ww]; }
      const float inv = __builtin_amdgcn_rcpf(sum);
      float olo[5], ohi[5];
      #pragma unroll
      for (int d = 0; d < 5; d++) { olo[d] = 0.f; ohi[d] = 0.f; }
      #pragma unroll
      for (int ww = 0; ww < 8; ww++) {
        const unsigned pb = pack_bf16(l8[ww]*inv, 0.f);
        const unsigned ph = pb << 16;
        LAS char* vr = rowp - (2*ww)*QB_PITCH + 160;
        u32x2 va = *(LAS u32x2*)(vr + o8 + 16*u), vb = *(LAS u32x2*)(vr + o8 + 16*u + 8); unsigned vc = *(LAS unsigned*)(vr + o4);
        olo[0] = dot2(va[0], pb, olo[0]); ohi[0] = dot2(va[0], ph, ohi[0]); olo[1] = dot2(va[1], pb, olo[1]); ohi[1] = dot2(va[1], ph, ohi[1]);
        olo[2] = dot2(vb[0], pb, olo[2]); ohi[2] = dot2(vb[0], ph, ohi[2]); olo[3] = dot2(vb[1], pb, olo[3]); ohi[3] = dot2(vb[1], ph, ohi[3]);
        olo[4] = dot2(vc, pb, olo[4]); ohi[4] = dot2(vc, ph, ohi[4]);
      }
      *(LAS u32x2*)(rowp + o8 + 16*u) = u32x2{pack_bf16(olo[0], ohi[0]), pack_bf16(olo[1], ohi[1])};
      *(LAS u32x2*)(rowp + o8 + 16*u + 8) = u32x2{pack_bf16(olo[2], ohi[2]), pack_bf16(olo[3], ohi[3])};
      if (u) *(LAS unsigned*)(rowp + o4) = pack_bf16(olo[4], ohi[4]);
      }
  WAITL(0);
  if (wave >= 4) { WAITV(0); if (lane == 0) set_flag(ws, FL_Z, pj, 4*pi + w, T); }
  STAMP(21);
  BAR();
  {
    const RSRC r_o = MKRSRC(ws + WS_O, 128*18*1024);
    #pragma unroll 1
    for (int e = tid; e < 1152; e += 512) {
      const int tl = e / 9, p = e % 9;
      u32x2 v = *(LAS u32x2*)(lds + QB_OFF + (16 + tl)*QB_PITCH + 8*(pj & 1) + 8*p);
      const int col = 36*pj + 4*p, r32 = tl & 31;
      const int tt = 8*pi + 2*(tl >> 5) + ((r32 >> 2) & 1), rho = 4*(r32 >> 3) + (r32 & 3);
      const int ks = col >> 5, c32 = col & 31;
      __builtin_amdgcn_raw_buffer_store_b64(v, r_o, ((tt*18 + ks)*64 + rho + 16*((c32 & 15) >> 2))*16 + (c32 >> 4)*8, 0, AUX_SC1);
    }
  }
  STAMP(22);
  if (wave >= 4 && w < 2) { wait_flags(ws, FL_Z, pj, 64, T, lane); STAMP(13); p2_issue(0, IC<1>{}); p2_issue(1, IC<1>{}); }
  WAITL(0);
  BAR();
  STAMP(6);

  const int p3cnt = (xcd < 2) ? 30 : 29;
  const int p3T = xcd*29 + (xcd < 2 ? xcd : 2) + rr;
  const bool p3act = rr < p3cnt;
  const int rg = p3T / 9, pc = p3T % 9;

  if (wave >= 4) {
    auto p2_helper = [&](auto ZR) {
      constexpr int CNT = ZR.value ? 9 : 8;
      p2_issue(2, ZR); p2_issue(3, ZR);
      WAITV(3*CNT);
      BAR();
      auto piece = [&](int s, auto K) {
        constexpr int k = K.value; const int base = (s & 3)*P2_STAGE;
        if constexpr (ZR.value) { SETM0(base + w*9216 + k*1024); DMAS(vlin, s_z2n, ((2*s + w)*9 + k)*1024, 0); }
        else if constexpr (k < 8) { const int wr = 2*(w - 2) + (k >> 2); SETM0(base + 18432 + wr*4096 + (k & 3)*1024); DMA(vw[k & 3], s_wsp, wr*32*4096 + s*128, 0); }
      };
      auto step = [&](int s, auto G0) {
        WAITV(2*CNT);
        BAR();
        static_for<0, 9>([&](auto K) {
          piece(s + 4, K);
          if constexpr (G0.value >= 0 && (K.value == 1 || K.value == 5)) {
            constexpr int pr = G0.value + (K.value == 5 ? 1 : 0);
            unsigned rr2 = pack2(f32x2{opq(gelu_f(opq(bf_lo(z1p[pr / 4][pr % 4])))), opq(gelu_f(opq(bf_hi(z1p[pr / 4][pr % 4]))))});
            asm volatile("" : "+v"(rr2));
            z1p[pr / 4][pr % 4] = rr2;
          }
        });
      };
      static_for<0, 18>([&](auto S) {
        step(S.value, IC<2*S.value>{});
        if constexpr (S.value == 4) { if (wave == 4 && lane == 0) set_flag(ws, FL_O, pi, pj, T); }
      });
      #pragma unroll 1
      for (int s = 18; s < 28; s++) step(s, IC<-1>{});
      WAITV(2*CNT); BAR();
      WAITV(CNT); BAR();
      #pragma unroll
      for (int n = 0; n < 9; n++) *(LAS u32x4*)(lds + w*9216 + n*1024 + lane*16) = u32x4{z1p[n][0], z1p[n][1], z1p[n][2], z1p[n][3]};
      WAITV(0); WAITL(0); BAR();
      BAR();
    };
    if (w < 2) p2_helper(IC<1>{}); else p2_helper(IC<0>{});
    STAMP(8);
    if (p3act) {
      const u32x4 s_o = make_srd(ws + WS_O, 128*18*1024), s_h = make_srd(ws + WS_H, 128*72*1024);
      const u32x4 s_wo = make_srd(wo, 576*1152), s_wout = make_srd(w_out, 2304*1152);
      int vW0, vW1, vA[5];
      int l2 = lane; OPAQUE(l2);
      { const int r = l2 >> 3;
        vW0 = r*1152 + (((l2 & 7) ^ (((r >> 1) & 3) << 1))*16);
        vW1 = (r + 8)*1152 + (((l2 & 7) ^ ((((r + 8) >> 1) & 3) << 1))*16);
        #pragma unroll
        for (int m5 = 0; m5 < 5; m5++) { int tt = 5*rg + m5; if (tt > 127) tt = 127; vA[m5] = l2*16 + tt*18*1024; } }
      int slot = 2;
      auto p3_ks = [&](u32x4 sw, u32x4 sa, int R0, int R1, int ks) {
        if (w < 3) {
          const int base = slot*P3_STAGE + w*9216, s0 = R0*1152 + pc*128, s1 = R1*1152 + pc*128;
          SETM0(base + 5120); DMA(vW0, sw, s0, 0); SETM0(base + 6144); DMA(vW1, sw, s0, 0);
          SETM0(base + 7168); DMA(vW0, sw, s1, 0); SETM0(base + 8192); DMA(vW1, sw, s1, 0);
          static_for<0, 5>([&](auto M) { SETM0(base + M.value*1024); DMAS(vA[M.value], sa, ks*1024, 0); });
        }
        slot = (slot == 4) ? 0 : slot + 1;
      };
      auto p3_wo = [&](int u) { const int ks = 3*u + w; p3_ks(s_wo, s_o, 32*ks, 32*ks + 16, ks); };
      auto p3_g3 = [&](int v) { const int ks = 3*v + w;
        const int R0 = (ks < 64) ? 144*(ks >> 2) + 32*(ks & 3) : 288*(ks - 64) + 128;
        p3_ks(s_wout, s_h, R0, R0 + ((ks < 64) ? 16 : 144), ks); };
      const int p0 = (5*rg) >> 3, p1 = ((5*rg + 4 > 127 ? 127 : 5*rg + 4)) >> 3;
      if (w < 3) { wait_flags(ws, FL_O, p0, 16, T, lane); wait_flags(ws, FL_O, p1, 16, T, lane); }
      STAMP(24);
      #pragma unroll 1
      for (int u = 0; u < 3; u++) p3_wo(u);
      BAR();
      p3_wo(3); p3_wo(4);
      WAITV(27);
      STAMP(26);
      BAR();
      WAITV(18); BAR();
      p3_wo(5); WAITV(18); BAR();
      WAITV(9); BAR();
      WAITV(0); BAR();
      BAR();
      #pragma unroll
      for (int m5 = 0; m5 < 5; m5++) { int tt = 5*rg + m5; if (tt > 127) tt = 127; vA[m5] = l2*16 + tt*72*1024; }
      if (w < 3) { wait_flags(ws, FL_H, p0, 16, T, lane); wait_flags(ws, FL_H, p1, 16, T, lane); }
      STAMP(27);
      #pragma unroll 1
      for (int v = 0; v < 4; v++) p3_g3(v);
      WAITV(18); BAR();
      #pragma unroll 1
      for (int u = 6; u < 26; u++) {
        p3_g3(u - 2);
        WAITV(18); BAR();
      }
      WAITV(9); BAR();
      WAITV(0); BAR();
      BAR();
    }
  } else {
    #pragma unroll
    for (int n = 0; n < 9; n++) { acc[n][0] = f32x4{0,0,0,0}; acc[n][1] = f32x4{0,0,0,0}; }
    int l2 = lane; OPAQUE(l2);
    const int g = l2 >> 4, fl = l2 & 15;
    const int zoff = l2*16;
    const int woff0 = 18432 + (4*w + (fl >> 3))*1024 + (fl & 7)*128 + ((g ^ ((fl >> 1) & 7))*16);
    float bs[2];
    #pragma unroll
    for (int m = 0; m < 2; m++) bs[m] = bf_lo(b_sp[128*pi + 32*w + 8*(fl >> 2) + 4*m + (fl & 3)]);
    bf16x8 z0[9], w0[2], z1[9], w1[2];
    auto rd = [&](bf16x8 (&z)[9], bf16x8 (&wv)[2], int s, int kk) {
      const int base = (s & 3)*P2_STAGE;
      LAS char* pw = lds + base + (woff0 ^ (kk*64));
      LAS char* pz = lds + base + kk*9216 + zoff;
      wv[0] = *(LAS bf16x8*)pw; wv[1] = *(LAS bf16x8*)(pw + 2048);
      #pragma unroll
      for (int n = 0; n < 9; n++) z[n] = *(LAS bf16x8*)(pz + n*1024);
    };
    auto mm = [&](bf16x8 (&z)[9], bf16x8 (&wv)[2]) {
      #pragma unroll
      for (int n = 0; n < 9; n++) { acc[n][0] = MFMA(z[n], wv[0], acc[n][0]); acc[n][1] = MFMA(z[n], wv[1], acc[n][1]); }
      #pragma unroll
      for (int q = 0; q < 11; q++) { SG(0x8, 1); SG(0x100, 1); }
      SG(0x8, 7);
    };
    BAR();
    rd(z0, w0, 0, 0);
    CWAITL0(); SB();
    #pragma unroll 1
    for (int s = 0; s < 32; s++) {
      CWAITL0(); SB();
      rd(z1, w1, s, 1);
      mm(z0, w0);
      SB(); CWAITL0(); PIN_ACC(acc);
      if (s == 4) WAITV(0);
      BAR(); SB();
      rd(z0, w0, (s + 1 < 32 ? s + 1 : s), 0);
      mm(z1, w1);
      SB();
    }
    STAMP(8);
    {
      const RSRC r_h = MKRSRC(ws + WS_H, 128*72*1024);
      u32x4 z1g[9];
      #pragma unroll
      for (int n = 0; n < 9; n++) z1g[n] = *(LAS u32x4*)(lds + w*9216 + n*1024 + zoff);
      #pragma unroll
      for (int m = 0; m < 2; m++) {
        const int tt = 8*pi + 2*w + m;
        const f32x2 b2 = {bs[m], bs[m]};
        unsigned hp[9][2];
        #pragma unroll
        for (int n = 0; n < 9; n++) {
          hp[n][0] = pack2(f32x2{bf_lo(z1g[n][2*m]), bf_hi(z1g[n][2*m])}*(f32x2{acc[n][m][0], acc[n][m][1]} + b2));
          hp[n][1] = pack2(f32x2{bf_lo(z1g[n][2*m + 1]), bf_hi(z1g[n][2*m + 1])}*(f32x2{acc[n][m][2], acc[n][m][3]} + b2));
        }
        #pragma unroll
        for (int q = 0; q < 4; q++)
          __builtin_amdgcn_raw_buffer_store_b128(u32x4{hp[2*q][0], hp[2*q][1], hp[2*q + 1][0], hp[2*q + 1][1]}, r_h, zoff + (tt*72 + 4*pj + q)*1024, 0, AUX_SC1);
        __builtin_amdgcn_raw_buffer_store_b64(u32x2{hp[8][0], hp[8][1]}, r_h, zoff + (pj & 1)*8 + (tt*72 + 64 + (pj >> 1))*1024, 0, AUX_SC1);
      }
    }
    STAMP(19);
    if (p3act) {
      WAITL(0);
      BAR();
      f32x4 aS[5], aC[5];
      u32x2 xv[5];
      const int q4 = (l2 >> 2) & 3, p4 = l2 & 3, r0 = 4*g + q4;
      const int woff = 5120 + r0*128 + (((2*w + (p4 >> 1)) ^ (((r0 >> 1) & 3) << 1))*16) + (p4 & 1)*8;
      bf16x8 hA[3][5], wA[3], hB[3][5], wB[3];
      int slot = 2;
      auto rd3 = [&](bf16x8 (&h)[3][5], bf16x8 (&wv)[3]) {
        LAS char* p = lds + slot*P3_STAGE;
        #pragma unroll
        for (int kk = 0; kk < 3; kk++) {
          s16x4 lo = __builtin_amdgcn_ds_read_tr16_b64_v4i16((LAS s16x4*)(p + kk*9216 + woff));
          s16x4 hi = __builtin_amdgcn_ds_read_tr16_b64_v4i16((LAS s16x4*)(p + kk*9216 + woff + 2048));
          wv[kk] = __builtin_shufflevector(lo, hi, 0, 1, 2, 3, 4, 5, 6, 7);
          #pragma unroll
          for (int m5 = 0; m5 < 5; m5++) h[kk][m5] = *(LAS bf16x8*)(p + kk*9216 + m5*1024 + zoff);
        }
        slot = (slot == 4) ? 0 : slot + 1;
      };
      auto mm3 = [&](bf16x8 (&h)[3][5], bf16x8 (&wv)[3]) {
        #pragma unroll
        for (int kk = 0; kk < 3; kk++)
          #pragma unroll
          for (int m5 = 0; m5 < 5; m5++) aC[m5] = MFMA(wv[kk], h[kk][m5], aC[m5]);
        #pragma unroll
        for (int q = 0; q < 15; q++) { SG(0x8, 1); SG(0x100, 1); }
        SG(0x100, 6);
      };
      #pragma unroll
      for (int m5 = 0; m5 < 5; m5++) {
        int tt = 5*rg + m5; if (tt > 127) tt = 127;
        const int row = 32*(tt >> 1) + 8*(fl >> 2) + 4*(tt & 1) + (fl & 3);
        xv[m5] = *(const u32x2*)(x + (size_t)row*D_ + 64*pc + 16*w + 4*g);
      }
      BAR();
      #pragma unroll 1
      for (int ph = 0; ph < 2; ph++) {
        #pragma unroll
        for (int m5 = 0; m5 < 5; m5++) aC[m5] = f32x4{0,0,0,0};
        rd3(hA, wA); SB();
        const int nit = ph ? 12 : 3;
        #pragma unroll 1
        for (int it = 0; it < nit; it++) {
          CWAITL0(); SB(); rd3(hB, wB); mm3(hA, wA); SB(); PIN5(aC); BAR(); SB();
          CWAITL0(); SB(); rd3(hA, wA); mm3(hB, wB); SB(); PIN5(aC);
          if (ph == 0 && it == 1) WAITV(0);
          if (ph == 0 || it < 11) BAR();
          SB();
          if (ph == 0 && it == 1 && tid == 0) set_flag(ws, FL_H, pi, pj, T);
        }
        if (ph == 0) {
          #pragma unroll
          for (int m5 = 0; m5 < 5; m5++) aS[m5] = aC[m5];
          slot = 3;
          STAMP(10);
        }
      }
      STAMP(11);
      #pragma unroll
      for (int m5 = 0; m5 < 5; m5++) {
        const int tt = 5*rg + m5;
        if (tt < 128) {
          const int row = 32*(tt >> 1) + 8*(fl >> 2) + 4*(tt & 1) + (fl & 3);
          const size_t o = (size_t)row*D_ + 64*pc + 16*w + 4*g;
          float r[4];
          #pragma unroll
          for (int e = 0; e < 4; e++) {
            float bv = aC[m5][e], sv = aS[m5][e];
            float sl = bv*__builtin_amdgcn_rcpf(1.f + __builtin_amdgcn_exp2f(-1.4426950408889634f*bv));
            unsigned xw = xv[m5][e >> 1];
            float xx = (e & 1) ? bf_hi(xw) : bf_lo(xw);
            r[e] = __builtin_fmaf(sv, sl, xx);
          }
          *(u32x2*)(out + o) = u32x2{pack_bf16(r[0], r[1]), pack_bf16(r[2], r[3])};
        }
      }
    } else {
      WAITV(0);
      BAR();
      if (tid == 0) set_flag(ws, FL_H, pi, pj, T);
    }
    if (b == 0 && tid == 0) __hip_atomic_store((gu32*)ws, T, RLX_AGENT);
    STAMP(12);
  }
  TDUMP();
  asm volatile(".Lpf_end:");
}

extern "C" void kernel_launch(void* const* d_in, const int* in_sizes, int n_in, void* d_out, int out_size, void* d_ws, size_t ws_size, hipStream_t stream) {
  (void)hipFuncSetAttribute((const void*)fused_kernel, hipFuncAttributeMaxDynamicSharedMemorySize, LDS_BYTES);
  fused_kernel<<<256, 512, LDS_BYTES, stream>>>((const u16*)d_in[0], (const u16*)d_in[1], (const u16*)d_in[2], (const u16*)d_in[4],
      (const u16*)d_in[5], (const u16*)d_in[6], (const u16*)d_in[7], (u16*)d_out, (char*)d_ws);
}
```

```cpp
#include <hip/hip_runtime.h>
#include <stdint.h>

typedef unsigned short u16;
typedef __attribute__((ext_vector_type(8))) short bf16x8;
typedef __attribute__((ext_vector_type(4))) short s16x4;
typedef __attribute__((ext_vector_type(4))) float f32x4;
typedef __attribute__((ext_vector_type(16))) float f32x16;
typedef __attribute__((ext_vector_type(2))) float f32x2;
typedef __attribute__((ext_vector_type(4))) unsigned u32x4;
typedef __attribute__((ext_vector_type(2))) unsigned u32x2;
typedef __attribute__((ext_vector_type(2))) __bf16 bf16x2;
#define GAS __attribute__((address_space(1)))
#define LAS __attribute__((address_space(3)))
typedef GAS unsigned gu32;
#define RLX_AGENT __ATOMIC_RELAXED, __HIP_MEMORY_SCOPE_AGENT
#define BAR() asm volatile("s_barrier" ::: "memory")
#define SB() __builtin_amdgcn_sched_barrier(0)
#define SG(mask, n) __builtin_amdgcn_sched_group_barrier(mask, n, 0)
#define WAITV(n) asm volatile("s_waitcnt vmcnt(%0)" :: "i"(n) : "memory")
#define WAITL(n) asm volatile("s_waitcnt lgkmcnt(%0)" :: "i"(n) : "memory")
#define CWAITL0() __builtin_amdgcn_s_waitcnt(0xC07F)
#define RSRC __amdgpu_buffer_rsrc_t
#define MKRSRC(p, bytes) __builtin_amdgcn_make_buffer_rsrc((void*)(p), 0, (bytes), 0x00020000)
#define AUX_SC1 16
#define OPAQUE(x) asm volatile("" : "+v"(x))

constexpr int D_ = 576, FH = 2304;
constexpr size_t WS_FLAGS = 4096;
constexpr size_t WS_STAT  = 32768;
constexpr size_t WS_HALO  = WS_STAT + 2048*256;
constexpr size_t WS_Z2N   = WS_HALO + 16*16*4096;
constexpr size_t WS_H     = WS_Z2N + (size_t)16*64*9*1024;
constexpr size_t WS_O     = WS_H + (size_t)128*72*1024;
constexpr size_t WS_DBG   = WS_O + (size_t)128*18*1024;
constexpr int FL_STAT = 0, FL_Z = 1, FL_HALO = 2, FL_O = 3, FL_H = 4;
constexpr int LDS_BYTES = 163840;
constexpr int P1_STAGE = 18432, P1_NS = 5;
constexpr int XCH_OFF = 4*P1_STAGE;
constexpr int ZB_OFF = P1_NS*P1_STAGE;
constexpr int QB_PITCH = 248;
constexpr int IMG_PITCH = 296, IMG_W = 32*IMG_PITCH;
constexpr int QB_OFF = ZB_OFF + 4*IMG_W - 16*QB_PITCH;
constexpr int MISC_OFF = QB_OFF + 144*QB_PITCH;
constexpr int Z1B_OFF = 4*P1_STAGE;
constexpr int P2_STAGE = 34816;
constexpr int P3_STAGE = 27648;
constexpr int SPIN_MAX = 1 << 18;

template<int N> struct IC { static constexpr int value = N; };
template<int B, int E, class F> __device__ __forceinline__ void static_for(F&& f) {
  if constexpr (B < E) { f(IC<B>{}); static_for<B + 1, E>(f); }
}

__device__ __forceinline__ unsigned pack2(f32x2 v) { return __builtin_bit_cast(unsigned, __builtin_convertvector(v, bf16x2)); }
__device__ __forceinline__ float opq(float x) { asm("" : "+v"(x)); return x; }
__device__ __forceinline__ unsigned pack_bf16(float a, float b) { return pack2(f32x2{a, b}); }
__device__ __forceinline__ float asf(unsigned u) { return __builtin_bit_cast(float, u); }
__device__ __forceinline__ float bf_lo(unsigned u) { return __builtin_bit_cast(float, u << 16); }
__device__ __forceinline__ float bf_hi(unsigned u) { return __builtin_bit_cast(float, u & 0xffff0000u); }
__device__ __forceinline__ float dot2(unsigned a, unsigned b, float c) {
  return __builtin_amdgcn_fdot2_f32_bf16(__builtin_bit_cast(bf16x2, a), __builtin_bit_cast(bf16x2, b), c, false);
}
__device__ __forceinline__ float gelu_f(float x) {
  const float c1 = -2.302208198f, c2 = -0.1029432397f;
  float t = __builtin_fmaf(x * x, c2, c1);
  float e = __builtin_amdgcn_exp2f(x * t);
  return x * __builtin_amdgcn_rcpf(1.f + e);
}
__device__ __forceinline__ f32x2 gelu2(f32x2 x) {
  const f32x2 c1 = {-2.302208198f, -2.302208198f}, c2 = {-0.1029432397f, -0.1029432397f}, one = {1.f, 1.f};
  f32x2 t = __builtin_elementwise_fma(x * x, c2, c1);
  f32x2 a = x * t;
  f32x2 e = {__builtin_amdgcn_exp2f(a[0]), __builtin_amdgcn_exp2f(a[1])};
  f32x2 d = e + one;
  f32x2 r = {__builtin_amdgcn_rcpf(d[0]), __builtin_amdgcn_rcpf(d[1])};
  return x * r;
}
template<int CTRL> __device__ __forceinline__ float dpp_f(float v) {
  return __builtin_bit_cast(float, __builtin_amdgcn_update_dpp(0, __builtin_bit_cast(int, v), CTRL, 0xf, 0xf, true));
}
__device__ __forceinline__ float row16_allsum(float v) {
  v += dpp_f<0x128>(v); v += dpp_f<0x124>(v); v += dpp_f<0x122>(v); v += dpp_f<0x121>(v); return v;
}
__device__ __forceinline__ bool poll_flags(const char* ws, int group, int idx, int nflags, unsigned T, int lane) {
  gu32* p = (gu32*)(ws + WS_FLAGS + (size_t)(group*16 + idx)*256) + (lane & (nflags - 1));
  unsigned v = __hip_atomic_load(p, RLX_AGENT);
  return __all(v == T);
}
__device__ __forceinline__ void wait_flags(const char* ws, int group, int idx, int nflags, unsigned T, int lane) {
  for (int spin = 0; spin < SPIN_MAX; ++spin) {
    if (poll_flags(ws, group, idx, nflags, T, lane)) break;
    __builtin_amdgcn_s_sleep(1);
  }
}
__device__ __forceinline__ void set_flag(char* ws, int group, int idx, int k, unsigned T) {
  __hip_atomic_store((gu32*)(ws + WS_FLAGS + (size_t)(group*16 + idx)*256) + k, T, RLX_AGENT);
}
__device__ __forceinline__ unsigned load_epoch(const char* ws) {
  unsigned e;
  asm volatile("s_load_dword %0, %1, 0x0\n\ts_waitcnt lgkmcnt(0)" : "=s"(e) : "s"(ws) : "memory");
  return e + 1u;
}
__device__ __forceinline__ int kperm(int r) { return 8*(r >> 3) + 4*(r & 1) + ((r >> 1) & 3); }
__device__ __forceinline__ u32x4 make_srd(const void* p, unsigned bytes) {
  unsigned long long a = (unsigned long long)p;
  return u32x4{(unsigned)a, (unsigned)(a >> 32) & 0xffffu, bytes, 0x00020000u};
}
__device__ __forceinline__ void SETM0(int v) { asm volatile("s_mov_b32 m0, %0\n\ts_nop 0" :: "s"(v) : "memory"); }
template<int OFF> __device__ __forceinline__ void dma_o(int voff, u32x4 srd, int soff) {
  asm volatile("buffer_load_dwordx4 %0, %1, %2 offen offset:%3 lds" :: "v"(voff), "s"(srd), "s"(soff), "i"(OFF) : "memory");
}
template<int OFF> __device__ __forceinline__ void dma_s(int voff, u32x4 srd, int soff) {
  asm volatile("buffer_load_dwordx4 %0, %1, %2 offen offset:%3 sc1 lds" :: "v"(voff), "s"(srd), "s"(soff), "i"(OFF) : "memory");
}
#define DMA(voff, srd, soff, OFF) dma_o<OFF>((voff), (srd), (soff))
#define DMAS(voff, srd, soff, OFF) dma_s<OFF>((voff), (srd), (soff))

#define SETPRIO1() __builtin_amdgcn_s_setprio(0)
#define PIN4(a) asm volatile("" : "+v"(a))
#define PIN_ACC(acc) asm volatile("" : "+v"(acc[0][0]), "+v"(acc[0][1]), "+v"(acc[1][0]), "+v"(acc[1][1]), "+v"(acc[2][0]), "+v"(acc[2][1]), \
  "+v"(acc[3][0]), "+v"(acc[3][1]), "+v"(acc[4][0]), "+v"(acc[4][1]), "+v"(acc[5][0]), "+v"(acc[5][1]), "+v"(acc[6][0]), "+v"(acc[6][1]), \
  "+v"(acc[7][0]), "+v"(acc[7][1]), "+v"(acc[8][0]), "+v"(acc[8][1]))
#define PIN5(a) asm volatile("" : "+v"(a[0]), "+v"(a[1]), "+v"(a[2]), "+v"(a[3]), "+v"(a[4]))
#define MFMA32(a, b, c) __builtin_amdgcn_mfma_f32_32x32x16_bf16((a), (b), (c), 0, 0, 0)
#define MFMA(a, b, c) __builtin_amdgcn_mfma_f32_16x16x32_bf16((a), (b), (c), 0, 0, 0)

#define STAMP(k)
#define RSTAMP(k)

#define VB() BAR()
#define TK(v)
#define ACC(k, a, b)
#define TDUMP()
extern "C" __global__ __launch_bounds__(512, 2) void fused_kernel(const u16* __restrict__ x, const u16* __restrict__ wqkv, const u16* __restrict__ wo,
    const u16* __restrict__ w_in, const u16* __restrict__ w_sp, const u16* __restrict__ b_sp, const u16* __restrict__ w_out,
    u16* __restrict__ out, char* __restrict__ ws)
{
  extern __shared__ __attribute__((aligned(16))) char smem[];
  LAS char* lds = (LAS char*)smem;
  const int tid = threadIdx.x, lane = tid & 63;
  const int wave = __builtin_amdgcn_readfirstlane(tid >> 6);
  const int w = wave & 3;
  const int b = blockIdx.x, xcd = b & 7, rr = b >> 3;
  const int pi = 4*(xcd & 3) + (rr & 3), pj = 8*(xcd >> 2) + (rr >> 2);
  STAMP(0);
  RSTAMP(28);
  if (wave == 7) {
    unsigned long long pc; unsigned off, sz;
    asm volatile("s_getpc_b64 %0\n.Lpf_here:\n\ts_mov_b32 %1, .Lpf_here-fused_kernel\n\ts_mov_b32 %2, .Lpf_end-fused_kernel" : "=s"(pc), "=s"(off), "=s"(sz));
    pc -= off;
    const u32x4 s_code = u32x4{(unsigned)pc, (unsigned)(pc >> 32) & 0xffffu, sz, 0x00020000u};
    const int per = 2048;
    SETM0(149504); DMA(lane*16, s_code, rr*per, 0); DMA(lane*16, s_code, rr*per, 1024);
  }
  unsigned T;

  int cv[9];
  u32x4 csrd = make_srd(w_in, 576*9216);
  int p1dst = (w & 1)*9216, p1so = 32*(w & 1)*9216 + (FH + 144*pj)*2, p1step = 64*9216;
  int cq[9];
  auto p1_offsets = [&](auto QKV, int (&dst)[9]) {
    int l3 = lane; OPAQUE(l3);
    int r = l3 / 18, ch = l3 % 18;
    #pragma unroll
    for (int t = 0; t < 9; t++) {
      const int kp = kperm(r);
      dst[t] = (QKV.value ? kp*3456 + (ch < 15 ? (ch / 5)*1152 + (ch % 5)*16 : 0) : kp*9216 + ch*16) - (t & 3)*1024;
      r += 3; ch += 10; if (ch >= 18) { ch -= 18; r += 1; }
    }
  };
  auto p1_issue = [&]() {
    SETM0(p1dst); DMA(cv[0], csrd, p1so, 0); DMA(cv[1], csrd, p1so, 1024); DMA(cv[2], csrd, p1so, 2048); DMA(cv[3], csrd, p1so, 3072);
    SETM0(p1dst + 4096); DMA(cv[4], csrd, p1so, 0); DMA(cv[5], csrd, p1so, 1024); DMA(cv[6], csrd, p1so, 2048); DMA(cv[7], csrd, p1so, 3072);
    SETM0(p1dst + 8192); DMA(cv[8], csrd, p1so, 0);
    p1dst = (p1dst >= 4*P1_STAGE) ? p1dst - 4*P1_STAGE : p1dst + P1_STAGE; p1so += p1step;
  };
  const bool isD = (wave == 4 || wave == 5);

  const int mt = wave >> 2;
  bf16x8 xn[36];
  {
    const u32x4 s_x = make_srd(x + (size_t)(128*pi + 32*w)*D_, 32*1152);
    const int a = lane >> 2, bq = lane & 3;
    u32x4 raw[18];
    {
      const int vx = (16*mt + a)*1152 + bq*16;
      static_for<0, 18>([&](auto C) { u32x4 t; asm volatile("buffer_load_dwordx4 %0, %1, %2, 0 offen offset:%3" : "=v"(t) : "v"(vx), "s"(s_x), "i"(C.value*64) : "memory"); raw[C.value] = t; });
    }
    if (isD) { p1_offsets(IC<0>{}, cv); p1_issue(); }
    if (wave >= 6) {
      const u32x4 s_a = make_srd(w_in, 576*9216), s_b = make_srd(wqkv, 576*3456);
      SETM0(149504);
      const int vl = lane*16;
      #pragma unroll
      for (int k = 0; k < 11; k++) DMA(vl, s_a, b*20736 + ((w - 2)*11 + k)*1024, 0);
      #pragma unroll
      for (int k = 0; k < 4; k++) DMA(vl, s_b, b*7776 + ((w - 2)*4 + k)*1024, 0);
    }
    STAMP(23);
    T = load_epoch(ws);
    STAMP(25);
    if (isD) WAITV(9); else if (wave >= 6) WAITV(15); else WAITV(0);
    asm volatile("" : "+v"(raw[0]), "+v"(raw[1]), "+v"(raw[2]), "+v"(raw[3]), "+v"(raw[4]), "+v"(raw[5]), "+v"(raw[6]), "+v"(raw[7]), "+v"(raw[8]),
                      "+v"(raw[9]), "+v"(raw[10]), "+v"(raw[11]), "+v"(raw[12]), "+v"(raw[13]), "+v"(raw[14]), "+v"(raw[15]), "+v"(raw[16]), "+v"(raw[17]));
    STAMP(20);
    float s1 = 0.f, s2 = 0.f, s3 = 0.f, s4 = 0.f;
    unsigned ones = 0x3f803f80u; OPAQUE(ones);
    #pragma unroll
    for (int c = 0; c < 18; c++) {
      s1 = dot2(raw[c][0], ones, s1); s2 = dot2(raw[c][0], raw[c][0], s2); s3 = dot2(raw[c][1], ones, s3); s4 = dot2(raw[c][1], raw[c][1], s4);
      s1 = dot2(raw[c][2], ones, s1); s2 = dot2(raw[c][2], raw[c][2], s2); s3 = dot2(raw[c][3], ones, s3); s4 = dot2(raw[c][3], raw[c][3], s4);
    }
    s1 += s3; s2 += s4;
    s1 += dpp_f<0xB1>(s1); s1 += dpp_f<0x4E>(s1);
    s2 += dpp_f<0xB1>(s2); s2 += dpp_f<0x4E>(s2);
    const float mean = s1*(1.f/576.f), var = fmaxf(s2*(1.f/576.f) - mean*mean, 0.f);
    const float rs = __builtin_amdgcn_rsqf(var + 1e-5f), sh = -mean*rs;
    const f32x2 rs2 = {rs, rs}, sh2 = {sh, sh};
    int xw = XCH_OFF + w*18432 + (bq >> 1)*1024 + (32*(bq & 1) + ((16*mt + a) ^ (2*bq)))*16;
    int xr0 = XCH_OFF + w*18432 + (32*(lane >> 5) + ((lane & 31) ^ (2*(lane >> 5))))*16;
    int xr1 = XCH_OFF + w*18432 + 1024 + (32*(lane >> 5) + ((lane & 31) ^ (4 + 2*(lane >> 5))))*16;
    OPAQUE(xw); OPAQUE(xr0); OPAQUE(xr1);
    auto norm_write = [&](auto C) {
      constexpr int c = C.value;
      u32x4 o;
      #pragma unroll
      for (int d = 0; d < 4; d++) o[d] = pack2(__builtin_elementwise_fma(f32x2{bf_lo(raw[c][d]), bf_hi(raw[c][d])}, rs2, sh2));
      *(LAS u32x4*)(lds + xw + (c % 9)*2048) = o;
    };
    STAMP(1);
    static_for<0, 9>(norm_write);
    WAITL(0);
    STAMP(7);
    BAR();
    if (wave < 4) {
      #pragma unroll
      for (int c = 0; c < 9; c++) { xn[2*c] = *(LAS bf16x8*)(lds + xr0 + c*2048); xn[2*c + 1] = *(LAS bf16x8*)(lds + xr1 + c*2048); }
      WAITL(0);
    }
    STAMP(9);
    BAR();
    if (isD) p1_issue();
    static_for<9, 18>(norm_write);
    if (isD) WAITV(9);
    WAITL(0);
    BAR();
    if (isD) { p1_issue(); p1_issue(); p1_offsets(IC<1>{}, cq); }
    if (wave < 4) {
      #pragma unroll
      for (int c = 0; c < 9; c++) { xn[18 + 2*c] = *(LAS bf16x8*)(lds + xr0 + c*2048); xn[18 + 2*c + 1] = *(LAS bf16x8*)(lds + xr1 + c*2048); }
    }
    STAMP(2);
  }

  unsigned z1p[9][4];
  f32x4 acc[9][2];

  if (wave < 4) {
    constexpr int D = 3, NB = 4, NT = 5, FS = 4*NT;
    int wl = (8*(lane >> 5) + 2*((lane >> 2) & 3))*288 + ((lane >> 4) & 1)*32 + (lane & 3)*8;
    int slot_off = 0;
    f32x16 a32[5];
    #pragma unroll 1
    for (int pass = 0; pass < 3; pass++) {
      asm volatile("" : "+s"(pass));
      bf16x8 wf[NB];
      int wc = wl + slot_off, wn;
      OPAQUE(wc);
      auto rdw = [&](auto G, int base) {
        constexpr int i = G.value % FS, k16 = i / NT, n = i % NT;
        LAS char* p = lds + base + (k16 >> 1)*9216 + (k16 & 1)*16*288 + n*64;
        s16x4 lo = __builtin_amdgcn_ds_read_tr16_b64_v4i16((LAS s16x4*)p);
        s16x4 hi = __builtin_amdgcn_ds_read_tr16_b64_v4i16((LAS s16x4*)(p + 288));
        wf[G.value % NB] = __builtin_shufflevector(lo, hi, 0, 1, 2, 3, 4, 5, 6, 7);
      };
      #pragma unroll
      for (int n = 0; n < 5; n++)
        #pragma unroll
        for (int e = 0; e < 16; e++) a32[n][e] = 0.f;
      static_for<0, D>([&](auto G) { rdw(G, wc); });
      SB();
      if (pass == 1) STAMP(16);
      static_for<0, 9*FS>([&](auto G) {
        constexpr int st = G.value / FS, i = G.value % FS, k16 = i / NT, n = i % NT;
        if constexpr (i == 0) { slot_off = (slot_off >= 4*P1_STAGE) ? 0 : slot_off + P1_STAGE; wn = wl + slot_off; OPAQUE(wn); }
        if constexpr (i == FS - D) { SB(); PIN5(a32);
          BAR(); SB(); }
        a32[n] = MFMA32(wf[G.value % NB], xn[4*st + k16], a32[n]);
        if constexpr (G.value + D < 9*FS) { if constexpr (i + D < FS) rdw(IC<G.value + D>{}, wc); else rdw(IC<G.value + D>{}, wn); }
        SG(0x8, 1); SG(0x100, 2);
        SB();
        if constexpr (i == FS - 1) wc = wn;
      });
      PIN5(a32);
      if (pass == 0) STAMP(14);
      int l2 = lane; OPAQUE(l2);
      const int sr = l2 & 31, h4 = (l2 >> 5)*4;
      auto pk4 = [&](int n, int rq) { return u32x2{pack2(f32x2{a32[n][4*rq], a32[n][4*rq + 1]}), pack2(f32x2{a32[n][4*rq + 2], a32[n][4*rq + 3]})}; };
      if (pass == 0) {
        f32x2 q1 = {0.f, 0.f}, q2 = {0.f, 0.f};
        #pragma unroll
        for (int n = 0; n < 5; n++)
          #pragma unroll
          for (int e = 0; e < 16; e += 2) if (n < 4 || e < 8) {
            const f32x2 y = gelu2(f32x2{a32[n][e], a32[n][e + 1]});
            q1 += y; q2 = __builtin_elementwise_fma(y, y, q2);
            a32[n][e] = y[0]; a32[n][e + 1] = y[1];
          }
        float t1 = q1[0] + q1[1], t2 = q2[0] + q2[1];
        t1 += __shfl_xor(t1, 32); t2 += __shfl_xor(t2, 32);
        const u32x4 rec = {__builtin_bit_cast(unsigned, t1), T, __builtin_bit_cast(unsigned, t2), T};
        if (l2 < 32) __builtin_amdgcn_raw_buffer_store_b128(rec, MKRSRC(ws + WS_STAT, 2048*256), ((pi*16 + pj)*128 + 32*w + sr)*16, 0, AUX_SC1);
      }
      if (pass < 2) {
        const int ibase = (pass == 0) ? ZB_OFF + w*IMG_W : QB_OFF + (16 + 32*w)*QB_PITCH;
        const int pitch = (pass == 1) ? QB_PITCH : IMG_PITCH, lim = (pass == 1) ? 124 : 144;
        LAS char* p = lds + ibase + sr*pitch + h4*2;
        #pragma unroll
        for (int n = 0; n < 5; n++)
          #pragma unroll
          for (int rq = 0; rq < 4; rq++) {
            if (n < 3 || (n == 3 && rq < 3)) *(LAS u32x2*)(p + n*64 + rq*16) = pk4(n, rq);
            else if (n == 3 || rq < 2) { if (32*n + 8*rq + h4 < lim) *(LAS u32x2*)(p + n*64 + rq*16) = pk4(n, rq); }
          }
        if (pass == 0) STAMP(3); else STAMP(4);
        WAITL(0);
      }
    }
    {
      int l2 = lane; OPAQUE(l2);
      const int sr = l2 & 31, h4 = (l2 >> 5)*4;
      LAS char* p = lds + Z1B_OFF + w*IMG_W + sr*IMG_PITCH + h4*2;
      #pragma unroll
      for (int n = 0; n < 5; n++)
        #pragma unroll
        for (int rq = 0; rq < 4; rq++) if (n < 4 || rq < 2)
          *(LAS u32x2*)(p + n*64 + rq*16) = u32x2{pack2(f32x2{a32[n][4*rq], a32[n][4*rq + 1]}), pack2(f32x2{a32[n][4*rq + 2], a32[n][4*rq + 3]})};
      STAMP(5);
      WAITL(0);
    }
    BAR();
  } else {
    const int g = lane >> 4, fl = lane & 15;
    __builtin_amdgcn_s_setprio(3);
    if (isD) {
      WAITV(18); VB();
      #pragma unroll 1
      for (int q = 1; q < 6; q++) { p1_issue(); WAITV(18); VB(); }
      #pragma unroll
      for (int t = 0; t < 9; t++) { const int t_ = cv[t]; cv[t] = cq[t]; cq[t] = t_; }
      csrd = make_srd(wqkv, 576*3456); p1so = 32*(w & 1)*3456 + ((72*pj) & ~15); p1step = 64*3456;
      #pragma unroll 1
      for (int q = 6; q < 15; q++) { p1_issue(); WAITV(18); VB(); }
      #pragma unroll
      for (int t = 0; t < 9; t++) cv[t] = cq[t];
      csrd = make_srd(w_in, 576*9216); p1so = 32*(w & 1)*9216 + 144*pj*2; p1step = 64*9216;
      #pragma unroll 1
      for (int q = 15; q < 24; q++) {
        if (q == 19 && wave == 4 && pi < 15) {
          const RSRC r_halo = MKRSRC(ws + WS_HALO + (size_t)(pi*16 + pj)*4096, 4096);
          #pragma unroll
          for (int c = 0; c < 4; c++) { int ch = c*64 + lane; if (ch > 247) ch = 247;
            u32x4 v = *(LAS u32x4*)(lds + QB_OFF + 128*QB_PITCH + ch*16);
            __builtin_amdgcn_raw_buffer_store_b128(v, r_halo, ch*16, 0, AUX_SC1); }
        }
        p1_issue(); WAITV(18); VB();
        if (q == 20 && wave == 4 && lane == 0 && pi < 15) set_flag(ws, FL_HALO, pi, pj, T);
      }
      WAITV(9); VB();
      WAITV(0); VB();
      if (wave == 5) {
        if (pi > 0) {
          gu32* fp = (gu32*)(ws + WS_FLAGS + (size_t)(FL_HALO*16 + pi - 1)*256) + pj;
          for (int spin = 0; spin < SPIN_MAX; ++spin) { if (__builtin_amdgcn_readfirstlane(__hip_atomic_load(fp, RLX_AGENT)) == T) break; __builtin_amdgcn_s_sleep(1); }
          const RSRC r_halo = MKRSRC(ws + WS_HALO + (size_t)((pi - 1)*16 + pj)*4096, 4096);
          #pragma unroll
          for (int c = 0; c < 4; c++) { int ch = c*64 + lane; if (ch > 247) ch = 247;
            u32x4 v = __builtin_amdgcn_raw_buffer_load_b128(r_halo, ch*16, 0, AUX_SC1);
            *(LAS u32x4*)(lds + QB_OFF + ch*16) = v; }
        } else {
          #pragma unroll
          for (int c = 0; c < 4; c++) { int ch = c*64 + lane; if (ch > 247) ch = 247; *(LAS u32x4*)(lds + QB_OFF + ch*16) = u32x4{0, 0, 0, 0}; }
        }
        WAITL(0);
      }
      VB();
    } else {
      const int gA = 2*(w - 2);
      {
        const u32x4 s_a = make_srd(w_sp, 2048*4096), s_b = make_srd(w_out, 2304*1152), s_c = make_srd(wo, 576*1152);
        SETM0(149504);
        const int vl = lane*16;
        #pragma unroll 1
        for (int q = 0; q < 8; q++) VB();
        #pragma unroll 1
        for (int q = 8; q < 16; q++) {
          DMA(vl, s_a, b*32768 + ((w - 2)*16 + 2*(q - 8))*1024, 0); DMA(vl, s_a, b*32768 + ((w - 2)*16 + 2*(q - 8) + 1)*1024, 0);
          if (q < 14) DMA(vl, s_b, b*10368 + ((w - 2)*6 + (q - 8))*1024, 0);
          else DMA(vl, s_c, b*2592 + ((w - 2)*2 + (q - 14))*1024, 0);
          VB();
        }
      }
      LAS f32x2* st = (LAS f32x2*)(lds + MISC_OFF + (w - 2)*512);
      {
        const u32x4 s_st = make_srd(ws + WS_STAT, 2048*256);
        const int vo = (pi*16*128 + 32*gA + lane)*16;
        u32x4 rec[16];
        static_for<0, 16>([&](auto C) { u32x4 t; asm volatile("buffer_load_dwordx4 %0, %1, %2, %3 offen sc1" : "=v"(t) : "v"(vo), "s"(s_st), "s"(C.value*2048) : "memory"); rec[C.value] = t; });
        WAITV(16);
        VB();
        VB();
        asm volatile("s_waitcnt vmcnt(0)" : "+v"(rec[0]), "+v"(rec[1]), "+v"(rec[2]), "+v"(rec[3]), "+v"(rec[4]), "+v"(rec[5]), "+v"(rec[6]), "+v"(rec[7]),
                          "+v"(rec[8]), "+v"(rec[9]), "+v"(rec[10]), "+v"(rec[11]), "+v"(rec[12]), "+v"(rec[13]), "+v"(rec[14]), "+v"(rec[15]) :: "memory");
        float t1 = 0.f, t2 = 0.f; bool ok = true;
        #pragma unroll
        for (int c = 0; c < 16; c++) { t1 += asf(rec[c][0]); t2 += asf(rec[c][2]); ok = ok && rec[c][1] == T && rec[c][3] == T; }
        if (!__all(ok)) {
          const RSRC r_stat = MKRSRC(ws + WS_STAT, 2048*256);
          for (int spin = 0; spin < SPIN_MAX; ++spin) {
            t1 = 0.f; t2 = 0.f; ok = true;
            #pragma unroll
            for (int c = 0; c < 16; c++) {
              u32x4 v = __builtin_amdgcn_raw_buffer_load_b128(r_stat, vo + c*2048, 0, AUX_SC1);
              t1 += asf(v[0]); t2 += asf(v[2]); ok = ok && v[1] == T && v[3] == T;
            }
            if (__all(ok)) break;
            __builtin_amdgcn_s_sleep(1);
          }
        }
        const float mean = t1*(1.f/2304.f), var = fmaxf(t2*(1.f/2304.f) - mean*mean, 0.f);
        const float rs = __builtin_amdgcn_rsqf(var + 1e-5f);
        st[lane] = f32x2{rs, -mean*rs};
        WAITL(0);
        VB();
      }
      const RSRC r_z2n = MKRSRC(ws + WS_Z2N, 16*64*9*1024);
      #pragma unroll 1
      for (int k = 0; k < 2; k++) {
        f32x2 r2[4], h2[4];
        #pragma unroll
        for (int d = 0; d < 4; d++) { f32x2 u0 = st[32*k + 8*g + 2*d], u1 = st[32*k + 8*g + 2*d + 1]; r2[d] = f32x2{u0[0], u1[0]}; h2[d] = f32x2{u0[1], u1[1]}; }
        LAS char* p = lds + ZB_OFF + (gA + k)*IMG_W + (8*g + ((lane >> 2) & 3))*IMG_PITCH + (lane & 3)*8;
        int zo = lane*16 + (pj*64 + 4*pi + gA + k)*9216;
        #pragma unroll 1
        for (int j3 = 0; j3 < 3; j3++, p += 96, zo += 3072) {
          u32x4 v[3];
          #pragma unroll
          for (int u = 0; u < 3; u++) {
            s16x4 lo = __builtin_amdgcn_ds_read_tr16_b64_v4i16((LAS s16x4*)(p + u*32));
            s16x4 hi = __builtin_amdgcn_ds_read_tr16_b64_v4i16((LAS s16x4*)(p + u*32 + 4*IMG_PITCH));
            v[u] = __builtin_bit_cast(u32x4, __builtin_shufflevector(lo, hi, 0, 1, 2, 3, 4, 5, 6, 7));
          }
          #pragma unroll
          for (int u = 0; u < 3; u++) {
            u32x4 o;
            #pragma unroll
            for (int d = 0; d < 4; d++) o[d] = pack2(f32x2{opq(__builtin_fmaf(bf_lo(v[u][d]), r2[d][0], h2[d][0])), opq(__builtin_fmaf(bf_hi(v[u][d]), r2[d][1], h2[d][1]))});
            __builtin_amdgcn_raw_buffer_store_b128(o, r_z2n, zo + u*1024, 0, AUX_SC1);
          }
          VB();
        }
      }
      VB();
      VB();
    }
    STAMP(5);
    BAR();
    if (!isD) { WAITV(0); if (lane == 0) set_flag(ws, FL_Z, pj, 2*pi + (w - 2), T); }
    {
      #pragma unroll
      for (int m = 0; m < 2; m++) {
        LAS char* zx = lds + Z1B_OFF + w*IMG_W + (8*(fl >> 2) + 4*m + (fl & 3))*IMG_PITCH + g*8;
        #pragma unroll
        for (int n = 0; n < 9; n++) { u32x2 v = *(LAS u32x2*)(zx + n*32); z1p[n][2*m] = v[0]; z1p[n][2*m + 1] = v[1]; }
      }
    }
  }

  const u32x4 s_z2n = make_srd(ws + WS_Z2N + (size_t)pj*64*9*1024, 64*9*1024);
  const u32x4 s_wsp = make_srd(w_sp + (size_t)128*pi*2048, 128*4096);
  int vw[4];
  const int vlin = lane*16;
  auto p2_issue = [&](int s, auto ZR) {
    const int base = (s & 3)*P2_STAGE;
    if constexpr (ZR.value) {
      static_for<0, 9>([&](auto K) { SETM0(base + w*9216 + K.value*1024); DMAS(vlin, s_z2n, ((2*s + w)*9 + K.value)*1024, 0); });
    } else {
      static_for<0, 8>([&](auto K) { constexpr int k = K.value; const int wr = 2*(w - 2) + (k >> 2);
        SETM0(base + 18432 + wr*4096 + (k & 3)*1024); DMA(vw[k & 3], s_wsp, wr*32*4096 + s*128, 0); });
    }
  };
  if (wave >= 4) {
    { int l2 = lane; OPAQUE(l2);
      const int a = l2 >> 3, bb = l2 & 7;
      #pragma unroll
      for (int m = 0; m < 2; m++)
        #pragma unroll
        for (int hf = 0; hf < 2; hf++) { int rho = 8*hf + a; vw[2*m + hf] = (8*(rho >> 2) + 4*m + (rho & 3))*4096 + ((bb ^ ((rho >> 1) & 7))*16); } }
    if (w >= 2) {
      #pragma unroll 1
      for (int s = 0; s < 2; s++) p2_issue(s, IC<0>{});
    }
  }
  {
      int t2 = tid; OPAQUE(t2);
      const int u = t2 & 1, hh = (t2 >> 7) & 1, tl = ((t2 >> 1) & 63) + 64*(t2 >> 8);
      LAS char* rowp = lds + QB_OFF + (16 + tl)*QB_PITCH + 8*(pj & 1) + 36*hh;
      const int o8 = hh*4, o4 = hh ? 0 : 32;
      u32x2 qv[4]; unsigned q0;
      #pragma unroll
      for (int d = 0; d < 4; d++) qv[d] = *(LAS u32x2*)(rowp + o8 + d*8);
      q0 = *(LAS unsigned*)(rowp + o4);
      float lg[4];
      #pragma unroll
      for (int ww = 0; ww < 4; ww++) {
        LAS char* kr = rowp - (2*(4*u + ww))*QB_PITCH + 80;
        float sa = dot2(q0, *(LAS unsigned*)(kr + o4), 0.f), sb = 0.f;
        #pragma unroll
        for (int d = 0; d < 4; d++) { u32x2 kv = *(LAS u32x2*)(kr + o8 + d*8); sa = dot2(qv[d][0], kv[0], sa); sb = dot2(qv[d][1], kv[1], sb); }
        float s = (sa + sb)*0.23570226039551584f;
        if (128*pi + tl - 2*(4*u + ww) < 0) s = -1e9f;
        lg[ww] = s;
      }
      float l8[8];
      #pragma unroll
      for (int ww = 0; ww < 4; ww++) { float o = dpp_f<0xB1>(lg[ww]); l8[ww] = u ? o : lg[ww]; l8[4 + ww] = u ? lg[ww] : o; }
      float mx = l8[0];
      #pragma unroll
      for (int ww = 1; ww < 8; ww++) mx = fmaxf(mx, l8[ww]);
      float sum = 0.f;
      #pragma unroll
      for (int ww = 0; ww < 8; ww++) { l8[ww] = __builtin_amdgcn_exp2f((l8[ww] - mx)*1.4426950408889634f); sum += l8[ww]; }
      const float inv = __builtin_amdgcn_rcpf(sum);
      float olo[5], ohi[5];
      #pragma unroll
      for (int d = 0; d < 5; d++) { olo[d] = 0.f; ohi[d] = 0.f; }
      #pragma unroll
      for (int ww = 0; ww < 8; ww++) {
        const unsigned pb = pack_bf16(l8[ww]*inv, 0.f);
        const unsigned ph = pb << 16;
        LAS char* vr = rowp - (2*ww)*QB_PITCH + 160;
        u32x2 va = *(LAS u32x2*)(vr + o8 + 16*u), vb = *(LAS u32x2*)(vr + o8 + 16*u + 8); unsigned vc = *(LAS unsigned*)(vr + o4);
        olo[0] = dot2(va[0], pb, olo[0]); ohi[0] = dot2(va[0], ph, ohi[0]); olo[1] = dot2(va[1], pb, olo[1]); ohi[1] = dot2(va[1], ph, ohi[1]);
        olo[2] = dot2(vb[0], pb, olo[2]); ohi[2] = dot2(vb[0], ph, ohi[2]); olo[3] = dot2(vb[1], pb, olo[3]); ohi[3] = dot2(vb[1], ph, ohi[3]);
        olo[4] = dot2(vc, pb, olo[4]); ohi[4] = dot2(vc, ph, ohi[4]);
      }
      *(LAS u32x2*)(rowp + o8 + 16*u) = u32x2{pack_bf16(olo[0], ohi[0]), pack_bf16(olo[1], ohi[1])};
      *(LAS u32x2*)(rowp + o8 + 16*u + 8) = u32x2{pack_bf16(olo[2], ohi[2]), pack_bf16(olo[3], ohi[3])};
      if (u) *(LAS unsigned*)(rowp + o4) = pack_bf16(olo[4], ohi[4]);
      }
  WAITL(0);
  STAMP(21);
  BAR();
  STAMP(22);
  if (wave >= 4 && w < 2) { wait_flags(ws, FL_Z, pj, 32, T, lane); STAMP(13); p2_issue(0, IC<1>{}); p2_issue(1, IC<1>{}); }
  if (wave < 4) {
    const RSRC r_o = MKRSRC(ws + WS_O, 128*18*1024);
    #pragma unroll 1
    for (int e = tid; e < 1152; e += 256) {
      const int tl = e / 9, p = e % 9;
      u32x2 v = *(LAS u32x2*)(lds + QB_OFF + (16 + tl)*QB_PITCH + 8*(pj & 1) + 8*p);
      const int col = 36*pj + 4*p, r32 = tl & 31;
      const int tt = 8*pi + 2*(tl >> 5) + ((r32 >> 2) & 1), rho = 4*(r32 >> 3) + (r32 & 3);
      const int ks = col >> 5, c32 = col & 31;
      __builtin_amdgcn_raw_buffer_store_b64(v, r_o, ((tt*18 + ks)*64 + rho + 16*((c32 & 15) >> 2))*16 + (c32 >> 4)*8, 0, AUX_SC1);
    }
  }
  WAITL(0);
  BAR();
  STAMP(6);

  const int p3cnt = (xcd < 2) ? 30 : 29;
  const int p3T = xcd*29 + (xcd < 2 ? xcd : 2) + rr;
  const bool p3act = rr < p3cnt;
  const int rg = p3T / 9, pc = p3T % 9;

  if (wave >= 4) {
    auto p2_helper = [&](auto ZR) {
      constexpr int CNT = ZR.value ? 9 : 8;
      p2_issue(2, ZR); p2_issue(3, ZR);
      WAITV(3*CNT);
      VB();
      auto piece = [&](int s, auto K) {
        constexpr int k = K.value; const int base = (s & 3)*P2_STAGE;
        if constexpr (ZR.value) { SETM0(base + w*9216 + k*1024); DMAS(vlin, s_z2n, ((2*s + w)*9 + k)*1024, 0); }
        else if constexpr (k < 8) { const int wr = 2*(w - 2) + (k >> 2); SETM0(base + 18432 + wr*4096 + (k & 3)*1024); DMA(vw[k & 3], s_wsp, wr*32*4096 + s*128, 0); }
      };
      auto step = [&](int s, auto G0) {
        WAITV(2*CNT);
        VB();
        static_for<0, 9>([&](auto K) {
          piece(s + 4, K);
          if constexpr (G0.value >= 0 && (K.value == 1 || K.value == 5)) {
            constexpr int pr = G0.value + (K.value == 5 ? 1 : 0);
            unsigned rr2 = pack2(f32x2{opq(gelu_f(opq(bf_lo(z1p[pr / 4][pr % 4])))), opq(gelu_f(opq(bf_hi(z1p[pr / 4][pr % 4]))))});
            asm volatile("" : "+v"(rr2));
            z1p[pr / 4][pr % 4] = rr2;
          }
        });
      };
      static_for<0, 18>([&](auto S) {
        step(S.value, IC<2*S.value>{});
        if constexpr (S.value == 4) { if (wave == 4 && lane == 0) set_flag(ws, FL_O, pi, pj, T); }
      });
      #pragma unroll 1
      for (int s = 18; s < 28; s++) step(s, IC<-1>{});
      WAITV(2*CNT); VB();
      WAITV(CNT); VB();
      #pragma unroll
      for (int n = 0; n < 9; n++) *(LAS u32x4*)(lds + w*9216 + n*1024 + lane*16) = u32x4{z1p[n][0], z1p[n][1], z1p[n][2], z1p[n][3]};
      WAITV(0); WAITL(0); VB();
      VB();
    };
    if (w < 2) p2_helper(IC<1>{}); else p2_helper(IC<0>{});
    STAMP(8);
    if (p3act) {
      const u32x4 s_o = make_srd(ws + WS_O, 128*18*1024), s_h = make_srd(ws + WS_H, 128*72*1024);
      const u32x4 s_wo = make_srd(wo, 576*1152), s_wout = make_srd(w_out, 2304*1152);
      int vW0, vW1, vA[5];
      int l2 = lane; OPAQUE(l2);
      { const int r = l2 >> 3;
        vW0 = r*1152 + (((l2 & 7) ^ (((r >> 1) & 3) << 1))*16);
        vW1 = (r + 8)*1152 + (((l2 & 7) ^ ((((r + 8) >> 1) & 3) << 1))*16);
        #pragma unroll
        for (int m5 = 0; m5 < 5; m5++) { int tt = 5*rg + m5; if (tt > 127) tt = 127; vA[m5] = l2*16 + tt*18*1024; } }
      int slot = 2;
      auto p3_ks = [&](u32x4 sw, u32x4 sa, int R0, int R1, int ks) {
        if (w < 3) {
          const int base = slot*P3_STAGE + w*9216, s0 = R0*1152 + pc*128, s1 = R1*1152 + pc*128;
          SETM0(base + 5120); DMA(vW0, sw, s0, 0); SETM0(base + 6144); DMA(vW1, sw, s0, 0);
          SETM0(base + 7168); DMA(vW0, sw, s1, 0); SETM0(base + 8192); DMA(vW1, sw, s1, 0);
          static_for<0, 5>([&](auto M) { SETM0(base + M.value*1024); DMAS(vA[M.value], sa, ks*1024, 0); });
        }
        slot = (slot == 4) ? 0 : slot + 1;
      };
      auto p3_wo = [&](int u) { const int ks = 3*u + w; p3_ks(s_wo, s_o, 32*ks, 32*ks + 16, ks); };
      auto p3_g3 = [&](int v) { const int ks = 3*v + w;
        const int R0 = (ks < 64) ? 144*(ks >> 2) + 32*(ks & 3) : 288*(ks - 64) + 128;
        p3_ks(s_wout, s_h, R0, R0 + ((ks < 64) ? 16 : 144), ks); };
      const int p0 = (5*rg) >> 3, p1 = ((5*rg + 4 > 127 ? 127 : 5*rg + 4)) >> 3;
      if (w < 3) { wait_flags(ws, FL_O, p0, 16, T, lane); wait_flags(ws, FL_O, p1, 16, T, lane); }
      STAMP(24);
      #pragma unroll 1
      for (int u = 0; u < 3; u++) p3_wo(u);
      BAR();
      p3_wo(3); p3_wo(4);
      WAITV(27);
      STAMP(26);
      BAR();
      WAITV(18); BAR();
      p3_wo(5); WAITV(18); BAR();
      WAITV(9); BAR();
      WAITV(0); BAR();
      BAR();
      #pragma unroll
      for (int m5 = 0; m5 < 5; m5++) { int tt = 5*rg + m5; if (tt > 127) tt = 127; vA[m5] = l2*16 + tt*72*1024; }
      if (w < 3) { wait_flags(ws, FL_H, p0, 16, T, lane); wait_flags(ws, FL_H, p1, 16, T, lane); }
      STAMP(27);
      #pragma unroll 1
      for (int v = 0; v < 4; v++) p3_g3(v);
      WAITV(18); BAR();
      #pragma unroll 1
      for (int u = 6; u < 26; u++) {
        p3_g3(u - 2);
        WAITV(18); BAR();
      }
      WAITV(9); BAR();
      WAITV(0); BAR();
      BAR();
    }
  } else {
    #pragma unroll
    for (int n = 0; n < 9; n++) { acc[n][0] = f32x4{0,0,0,0}; acc[n][1] = f32x4{0,0,0,0}; }
    int l2 = lane; OPAQUE(l2);
    const int g = l2 >> 4, fl = l2 & 15;
    const int zoff = l2*16;
    const int woff0 = 18432 + (4*w + (fl >> 3))*1024 + (fl & 7)*128 + ((g ^ ((fl >> 1) & 7))*16);
    float bs[2];
    #pragma unroll
    for (int m = 0; m < 2; m++) bs[m] = bf_lo(b_sp[128*pi + 32*w + 8*(fl >> 2) + 4*m + (fl & 3)]);
    bf16x8 z0[9], w0[2], z1[9], w1[2];
    auto rd = [&](bf16x8 (&z)[9], bf16x8 (&wv)[2], int s, int kk) {
      const int base = (s & 3)*P2_STAGE;
      LAS char* pw = lds + base + (woff0 ^ (kk*64));
      LAS char* pz = lds + base + kk*9216 + zoff;
      wv[0] = *(LAS bf16x8*)pw; wv[1] = *(LAS bf16x8*)(pw + 2048);
      #pragma unroll
      for (int n = 0; n < 9; n++) z[n] = *(LAS bf16x8*)(pz + n*1024);
    };
    auto mm = [&](bf16x8 (&z)[9], bf16x8 (&wv)[2]) {
      #pragma unroll
      for (int n = 0; n < 9; n++) { acc[n][0] = MFMA(z[n], wv[0], acc[n][0]); acc[n][1] = MFMA(z[n], wv[1], acc[n][1]); }
      #pragma unroll
      for (int q = 0; q < 11; q++) { SG(0x8, 1); SG(0x100, 1); }
      SG(0x8, 7);
    };
    BAR();
    rd(z0, w0, 0, 0);
    CWAITL0(); SB();
    #pragma unroll 1
    for (int s = 0; s < 32; s++) {
      CWAITL0(); SB();
      rd(z1, w1, s, 1);
      mm(z0, w0);
      SB(); CWAITL0(); PIN_ACC(acc);
      if (s == 4) WAITV(0);
      BAR(); SB();
      rd(z0, w0, (s + 1 < 32 ? s + 1 : s), 0);
      mm(z1, w1);
      SB();
    }
    STAMP(8);
    {
      const RSRC r_h = MKRSRC(ws + WS_H, 128*72*1024);
      u32x4 z1g[9];
      #pragma unroll
      for (int n = 0; n < 9; n++) z1g[n] = *(LAS u32x4*)(lds + w*9216 + n*1024 + zoff);
      #pragma unroll
      for (int m = 0; m < 2; m++) {
        const int tt = 8*pi + 2*w + m;
        const f32x2 b2 = {bs[m], bs[m]};
        unsigned hp[9][2];
        #pragma unroll
        for (int n = 0; n < 9; n++) {
          hp[n][0] = pack2(f32x2{bf_lo(z1g[n][2*m]), bf_hi(z1g[n][2*m])}*(f32x2{acc[n][m][0], acc[n][m][1]} + b2));
          hp[n][1] = pack2(f32x2{bf_lo(z1g[n][2*m + 1]), bf_hi(z1g[n][2*m + 1])}*(f32x2{acc[n][m][2], acc[n][m][3]} + b2));
        }
        #pragma unroll
        for (int q = 0; q < 4; q++)
          __builtin_amdgcn_raw_buffer_store_b128(u32x4{hp[2*q][0], hp[2*q][1], hp[2*q + 1][0], hp[2*q + 1][1]}, r_h, zoff + (tt*72 + 4*pj + q)*1024, 0, AUX_SC1);
        __builtin_amdgcn_raw_buffer_store_b64(u32x2{hp[8][0], hp[8][1]}, r_h, zoff + (pj & 1)*8 + (tt*72 + 64 + (pj >> 1))*1024, 0, AUX_SC1);
      }
    }
    STAMP(19);
    if (p3act) {
      WAITL(0);
      BAR();
      f32x4 aS[5], aC[5];
      u32x2 xv[5];
      const int q4 = (l2 >> 2) & 3, p4 = l2 & 3, r0 = 4*g + q4;
      const int woff = 5120 + r0*128 + (((2*w + (p4 >> 1)) ^ (((r0 >> 1) & 3) << 1))*16) + (p4 & 1)*8;
      bf16x8 hA[3][5], wA[3], hB[3][5], wB[3];
      int slot = 2;
      auto rd3 = [&](bf16x8 (&h)[3][5], bf16x8 (&wv)[3]) {
        LAS char* p = lds + slot*P3_STAGE;
        #pragma unroll
        for (int kk = 0; kk < 3; kk++) {
          s16x4 lo = __builtin_amdgcn_ds_read_tr16_b64_v4i16((LAS s16x4*)(p + kk*9216 + woff));
          s16x4 hi = __builtin_amdgcn_ds_read_tr16_b64_v4i16((LAS s16x4*)(p + kk*9216 + woff + 2048));
          wv[kk] = __builtin_shufflevector(lo, hi, 0, 1, 2, 3, 4, 5, 6, 7);
          #pragma unroll
          for (int m5 = 0; m5 < 5; m5++) h[kk][m5] = *(LAS bf16x8*)(p + kk*9216 + m5*1024 + zoff);
        }
        slot = (slot == 4) ? 0 : slot + 1;
      };
      auto mm3 = [&](bf16x8 (&h)[3][5], bf16x8 (&wv)[3]) {
        #pragma unroll
        for (int kk = 0; kk < 3; kk++)
          #pragma unroll
          for (int m5 = 0; m5 < 5; m5++) aC[m5] = MFMA(wv[kk], h[kk][m5], aC[m5]);
        #pragma unroll
        for (int q = 0; q < 15; q++) { SG(0x8, 1); SG(0x100, 1); }
        SG(0x100, 6);
      };
      #pragma unroll
      for (int m5 = 0; m5 < 5; m5++) {
        int tt = 5*rg + m5; if (tt > 127) tt = 127;
        const int row = 32*(tt >> 1) + 8*(fl >> 2) + 4*(tt & 1) + (fl & 3);
        xv[m5] = *(const u32x2*)(x + (size_t)row*D_ + 64*pc + 16*w + 4*g);
      }
      BAR();
      #pragma unroll 1
      for (int ph = 0; ph < 2; ph++) {
        #pragma unroll
        for (int m5 = 0; m5 < 5; m5++) aC[m5] = f32x4{0,0,0,0};
        rd3(hA, wA); SB();
        const int nit = ph ? 12 : 3;
        #pragma unroll 1
        for (int it = 0; it < nit; it++) {
          CWAITL0(); SB(); rd3(hB, wB); mm3(hA, wA); SB(); PIN5(aC); BAR(); SB();
          CWAITL0(); SB(); rd3(hA, wA); mm3(hB, wB); SB(); PIN5(aC);
          if (ph == 0 && it == 1) WAITV(0);
          if (ph == 0 || it < 11) BAR();
          SB();
          if (ph == 0 && it == 1 && tid == 0) set_flag(ws, FL_H, pi, pj, T);
        }
        if (ph == 0) {
          #pragma unroll
          for (int m5 = 0; m5 < 5; m5++) aS[m5] = aC[m5];
          slot = 3;
          STAMP(10);
        }
      }
      STAMP(11);
      #pragma unroll
      for (int m5 = 0; m5 < 5; m5++) {
        const int tt = 5*rg + m5;
        if (tt < 128) {
          const int row = 32*(tt >> 1) + 8*(fl >> 2) + 4*(tt & 1) + (fl & 3);
          const size_t o = (size_t)row*D_ + 64*pc + 16*w + 4*g;
          float r[4];
          #pragma unroll
          for (int e = 0; e < 4; e++) {
            float bv = aC[m5][e], sv = aS[m5][e];
            float sl = bv*__builtin_amdgcn_rcpf(1.f + __builtin_amdgcn_exp2f(-1.4426950408889634f*bv));
            unsigned xw = xv[m5][e >> 1];
            float xx = (e & 1) ? bf_hi(xw) : bf_lo(xw);
            r[e] = __builtin_fmaf(sv, sl, xx);
          }
          *(u32x2*)(out + o) = u32x2{pack_bf16(r[0], r[1]), pack_bf16(r[2], r[3])};
        }
      }
    } else {
      WAITV(0);
      BAR();
      if (tid == 0) set_flag(ws, FL_H, pi, pj, T);
    }
    if (b == 0 && tid == 0) __hip_atomic_store((gu32*)ws, T, RLX_AGENT);
    STAMP(12);
  }
  TDUMP();
  asm volatile(".Lpf_end:");
}

extern "C" void kernel_launch(void* const* d_in, const int* in_sizes, int n_in, void* d_out, int out_size, void* d_ws, size_t ws_size, hipStream_t stream) {
  (void)hipFuncSetAttribute((const void*)fused_kernel, hipFuncAttributeMaxDynamicSharedMemorySize, LDS_BYTES);
  fused_kernel<<<256, 512, LDS_BYTES, stream>>>((const u16*)d_in[0], (const u16*)d_in[1], (const u16*)d_in[2], (const u16*)d_in[4],
      (const u16*)d_in[5], (const u16*)d_in[6], (const u16*)d_in[7], (u16*)d_out, (char*)d_ws);
}
```

```cpp
#include <hip/hip_runtime.h>
#include <stdint.h>

typedef unsigned short u16;
typedef __attribute__((ext_vector_type(8))) short bf16x8;
typedef __attribute__((ext_vector_type(4))) short s16x4;
typedef __attribute__((ext_vector_type(4))) float f32x4;
typedef __attribute__((ext_vector_type(16))) float f32x16;
typedef __attribute__((ext_vector_type(2))) float f32x2;
typedef __attribute__((ext_vector_type(4))) unsigned u32x4;
typedef __attribute__((ext_vector_type(2))) unsigned u32x2;
typedef __attribute__((ext_vector_type(2))) __bf16 bf16x2;
#define GAS __attribute__((address_space(1)))
#define LAS __attribute__((address_space(3)))
typedef GAS unsigned gu32;
#define RLX_AGENT __ATOMIC_RELAXED, __HIP_MEMORY_SCOPE_AGENT
#define BAR() asm volatile("s_barrier" ::: "memory")
#define SB() __builtin_amdgcn_sched_barrier(0)
#define SG(mask, n) __builtin_amdgcn_sched_group_barrier(mask, n, 0)
#define WAITV(n) asm volatile("s_waitcnt vmcnt(%0)" :: "i"(n) : "memory")
#define WAITL(n) asm volatile("s_waitcnt lgkmcnt(%0)" :: "i"(n) : "memory")
#define CWAITL0() __builtin_amdgcn_s_waitcnt(0xC07F)
#define RSRC __amdgpu_buffer_rsrc_t
#define MKRSRC(p, bytes) __builtin_amdgcn_make_buffer_rsrc((void*)(p), 0, (bytes), 0x00020000)
#define AUX_SC1 16
#define OPAQUE(x) asm volatile("" : "+v"(x))

constexpr int D_ = 576, FH = 2304;
constexpr size_t WS_FLAGS = 4096;
constexpr size_t WS_STAT  = 32768;
constexpr size_t WS_HALO  = WS_STAT + 2048*256;
constexpr size_t WS_Z2N   = WS_HALO + 16*16*4096;
constexpr size_t WS_H     = WS_Z2N + (size_t)16*64*9*1024;
constexpr size_t WS_O     = WS_H + (size_t)128*72*1024;
constexpr size_t WS_DBG   = WS_O + (size_t)128*18*1024;
constexpr int FL_STAT = 0, FL_Z = 1, FL_HALO = 2, FL_O = 3, FL_H = 4;
constexpr int LDS_BYTES = 163840;
constexpr int P1_STAGE = 18432, P1_NS = 5;
constexpr int XCH_OFF = 4*P1_STAGE;
constexpr int ZB_OFF = P1_NS*P1_STAGE;
constexpr int QB_PITCH = 248;
constexpr int IMG_PITCH = 296, IMG_W = 32*IMG_PITCH;
constexpr int QB_OFF = ZB_OFF + 4*IMG_W - 16*QB_PITCH;
constexpr int MISC_OFF = QB_OFF + 144*QB_PITCH;
constexpr int Z1B_OFF = 4*P1_STAGE;
constexpr int P2_STAGE = 34816;
constexpr int P3_STAGE = 27648;
constexpr int SPIN_MAX = 1 << 18;

template<int N> struct IC { static constexpr int value = N; };
template<int B, int E, class F> __device__ __forceinline__ void static_for(F&& f) {
  if constexpr (B < E) { f(IC<B>{}); static_for<B + 1, E>(f); }
}

__device__ __forceinline__ unsigned pack2(f32x2 v) { return __builtin_bit_cast(unsigned, __builtin_convertvector(v, bf16x2)); }
__device__ __forceinline__ float opq(float x) { asm("" : "+v"(x)); return x; }
__device__ __forceinline__ unsigned pack_bf16(float a, float b) { return pack2(f32x2{a, b}); }
__device__ __forceinline__ float asf(unsigned u) { return __builtin_bit_cast(float, u); }
__device__ __forceinline__ float bf_lo(unsigned u) { return __builtin_bit_cast(float, u << 16); }
__device__ __forceinline__ float bf_hi(unsigned u) { return __builtin_bit_cast(float, u & 0xffff0000u); }
__device__ __forceinline__ float dot2(unsigned a, unsigned b, float c) {
  return __builtin_amdgcn_fdot2_f32_bf16(__builtin_bit_cast(bf16x2, a), __builtin_bit_cast(bf16x2, b), c, false);
}
__device__ __forceinline__ float gelu_f(float x) {
  const float c1 = -2.302208198f, c2 = -0.1029432397f;
  float t = __builtin_fmaf(x * x, c2, c1);
  float e = __builtin_amdgcn_exp2f(x * t);
  return x * __builtin_amdgcn_rcpf(1.f + e);
}
__device__ __forceinline__ f32x2 gelu2(f32x2 x) {
  const f32x2 c1 = {-2.302208198f, -2.302208198f}, c2 = {-0.1029432397f, -0.1029432397f}, one = {1.f, 1.f};
  f32x2 t = __builtin_elementwise_fma(x * x, c2, c1);
  f32x2 a = x * t;
  f32x2 e = {__builtin_amdgcn_exp2f(a[0]), __builtin_amdgcn_exp2f(a[1])};
  f32x2 d = e + one;
  f32x2 r = {__builtin_amdgcn_rcpf(d[0]), __builtin_amdgcn_rcpf(d[1])};
  return x * r;
}
template<int CTRL> __device__ __forceinline__ float dpp_f(float v) {
  return __builtin_bit_cast(float, __builtin_amdgcn_update_dpp(0, __builtin_bit_cast(int, v), CTRL, 0xf, 0xf, true));
}
__device__ __forceinline__ float row16_allsum(float v) {
  v += dpp_f<0x128>(v); v += dpp_f<0x124>(v); v += dpp_f<0x122>(v); v += dpp_f<0x121>(v); return v;
}
__device__ __forceinline__ bool poll_flags(const char* ws, int group, int idx, int nflags, unsigned T, int lane) {
  gu32* p = (gu32*)(ws + WS_FLAGS + (size_t)(group*16 + idx)*256) + (lane & (nflags - 1));
  unsigned v = __hip_atomic_load(p, RLX_AGENT);
  return __all(v == T);
}
__device__ __forceinline__ void wait_flags(const char* ws, int group, int idx, int nflags, unsigned T, int lane) {
  for (int spin = 0; spin < SPIN_MAX; ++spin) {
    if (poll_flags(ws, group, idx, nflags, T, lane)) break;
    __builtin_amdgcn_s_sleep(1);
  }
}
__device__ __forceinline__ void set_flag(char* ws, int group, int idx, int k, unsigned T) {
  __hip_atomic_store((gu32*)(ws + WS_FLAGS + (size_t)(group*16 + idx)*256) + k, T, RLX_AGENT);
}
__device__ __forceinline__ unsigned load_epoch(const char* ws) {
  unsigned e;
  asm volatile("s_load_dword %0, %1, 0x0\n\ts_waitcnt lgkmcnt(0)" : "=s"(e) : "s"(ws) : "memory");
  return e + 1u;
}
__device__ __forceinline__ int kperm(int r) { return 8*(r >> 3) + 4*(r & 1) + ((r >> 1) & 3); }
__device__ __forceinline__ u32x4 make_srd(const void* p, unsigned bytes) {
  unsigned long long a = (unsigned long long)p;
  return u32x4{(unsigned)a, (unsigned)(a >> 32) & 0xffffu, bytes, 0x00020000u};
}
__device__ __forceinline__ void SETM0(int v) { asm volatile("s_mov_b32 m0, %0\n\ts_nop 0" :: "s"(v) : "memory"); }
template<int OFF> __device__ __forceinline__ void dma_o(int voff, u32x4 srd, int soff) {
  asm volatile("buffer_load_dwordx4 %0, %1, %2 offen offset:%3 lds" :: "v"(voff), "s"(srd), "s"(soff), "i"(OFF) : "memory");
}
template<int OFF> __device__ __forceinline__ void dma_s(int voff, u32x4 srd, int soff) {
  asm volatile("buffer_load_dwordx4 %0, %1, %2 offen offset:%3 sc1 lds" :: "v"(voff), "s"(srd), "s"(soff), "i"(OFF) : "memory");
}
#define DMA(voff, srd, soff, OFF) dma_o<OFF>((voff), (srd), (soff))
#define DMAS(voff, srd, soff, OFF) dma_s<OFF>((voff), (srd), (soff))

#define SETPRIO1() __builtin_amdgcn_s_setprio(0)
#define PIN4(a) asm volatile("" : "+v"(a))
#define PIN_ACC(acc) asm volatile("" : "+v"(acc[0][0]), "+v"(acc[0][1]), "+v"(acc[1][0]), "+v"(acc[1][1]), "+v"(acc[2][0]), "+v"(acc[2][1]), \
  "+v"(acc[3][0]), "+v"(acc[3][1]), "+v"(acc[4][0]), "+v"(acc[4][1]), "+v"(acc[5][0]), "+v"(acc[5][1]), "+v"(acc[6][0]), "+v"(acc[6][1]), \
  "+v"(acc[7][0]), "+v"(acc[7][1]), "+v"(acc[8][0]), "+v"(acc[8][1]))
#define PIN5(a) asm volatile("" : "+v"(a[0]), "+v"(a[1]), "+v"(a[2]), "+v"(a[3]), "+v"(a[4]))
#define MFMA32(a, b, c) __builtin_amdgcn_mfma_f32_32x32x16_bf16((a), (b), (c), 0, 0, 0)
#define MFMA(a, b, c) __builtin_amdgcn_mfma_f32_16x16x32_bf16((a), (b), (c), 0, 0, 0)

#define STAMP(k)
#define RSTAMP(k)

#define VB() BAR()
#define TK(v)
#define ACC(k, a, b)
#define TDUMP()
extern "C" __global__ __launch_bounds__(512, 2) void fused_kernel(const u16* __restrict__ x, const u16* __restrict__ wqkv, const u16* __restrict__ wo,
    const u16* __restrict__ w_in, const u16* __restrict__ w_sp, const u16* __restrict__ b_sp, const u16* __restrict__ w_out,
    u16* __restrict__ out, char* __restrict__ ws)
{
  extern __shared__ __attribute__((aligned(16))) char smem[];
  LAS char* lds = (LAS char*)smem;
  const int tid = threadIdx.x, lane = tid & 63;
  const int wave = __builtin_amdgcn_readfirstlane(tid >> 6);
  const int w = wave & 3;
  const int b = blockIdx.x, xcd = b & 7, rr = b >> 3;
  const int pi = 4*(xcd & 3) + (rr & 3), pj = 8*(xcd >> 2) + (rr >> 2);
  STAMP(0);
  RSTAMP(28);
  if (wave == 7) {
    unsigned long long pc; unsigned off, sz;
    asm volatile("s_getpc_b64 %0\n.Lpf_here:\n\ts_mov_b32 %1, .Lpf_here-fused_kernel\n\ts_mov_b32 %2, .Lpf_end-fused_kernel" : "=s"(pc), "=s"(off), "=s"(sz));
    pc -= off;
    const u32x4 s_code = u32x4{(unsigned)pc, (unsigned)(pc >> 32) & 0xffffu, sz, 0x00020000u};
    const int per = 2048;
    SETM0(149504); DMA(lane*16, s_code, rr*per, 0); DMA(lane*16, s_code, rr*per, 1024);
  }
  unsigned T;

  int cv[9];
  u32x4 csrd = make_srd(w_in, 576*9216);
  int p1dst = (w & 1)*9216, p1so = 32*(w & 1)*9216 + (FH + 144*pj)*2, p1step = 64*9216;
  int cq[9];
  auto p1_offsets = [&](auto QKV, int (&dst)[9]) {
    int l3 = lane; OPAQUE(l3);
    int r = l3 / 18, ch = l3 % 18;
    #pragma unroll
    for (int t = 0; t < 9; t++) {
      const int kp = kperm(r);
      dst[t] = (QKV.value ? kp*3456 + (ch < 15 ? (ch / 5)*1152 + (ch % 5)*16 : 0) : kp*9216 + ch*16) - (t & 3)*1024;
      r += 3; ch += 10; if (ch >= 18) { ch -= 18; r += 1; }
    }
  };
  auto p1_issue = [&]() {
    SETM0(p1dst); DMA(cv[0], csrd, p1so, 0); DMA(cv[1], csrd, p1so, 1024); DMA(cv[2], csrd, p1so, 2048); DMA(cv[3], csrd, p1so, 3072);
    SETM0(p1dst + 4096); DMA(cv[4], csrd, p1so, 0); DMA(cv[5], csrd, p1so, 1024); DMA(cv[6], csrd, p1so, 2048); DMA(cv[7], csrd, p1so, 3072);
    SETM0(p1dst + 8192); DMA(cv[8], csrd, p1so, 0);
    p1dst = (p1dst >= 4*P1_STAGE) ? p1dst - 4*P1_STAGE : p1dst + P1_STAGE; p1so += p1step;
  };
  const bool isD = (wave == 4 || wave == 5);

  const int mt = wave >> 2;
  bf16x8 xn[36];
  {
    const u32x4 s_x = make_srd(x + (size_t)(128*pi + 32*w)*D_, 32*1152);
    const int a = lane >> 2, bq = lane & 3;
    u32x4 raw[18];
    {
      const int vx = (16*mt + a)*1152 + bq*16;
      static_for<0, 18>([&](auto C) { u32x4 t; asm volatile("buffer_load_dwordx4 %0, %1, %2, 0 offen offset:%3" : "=v"(t) : "v"(vx), "s"(s_x), "i"(C.value*64) : "memory"); raw[C.value] = t; });
    }
    if (isD) p1_offsets(IC<0>{}, cv);
    STAMP(23);
    WAITV(0);
    asm volatile("" : "+v"(raw[0]), "+v"(raw[1]), "+v"(raw[2]), "+v"(raw[3]), "+v"(raw[4]), "+v"(raw[5]), "+v"(raw[6]), "+v"(raw[7]), "+v"(raw[8]),
                      "+v"(raw[9]), "+v"(raw[10]), "+v"(raw[11]), "+v"(raw[12]), "+v"(raw[13]), "+v"(raw[14]), "+v"(raw[15]), "+v"(raw[16]), "+v"(raw[17]));
    STAMP(20);
    float s1 = 0.f, s2 = 0.f, s3 = 0.f, s4 = 0.f;
    unsigned ones = 0x3f803f80u; OPAQUE(ones);
    #pragma unroll
    for (int c = 0; c < 18; c++) {
      s1 = dot2(raw[c][0], ones, s1); s2 = dot2(raw[c][0], raw[c][0], s2); s3 = dot2(raw[c][1], ones, s3); s4 = dot2(raw[c][1], raw[c][1], s4);
      s1 = dot2(raw[c][2], ones, s1); s2 = dot2(raw[c][2], raw[c][2], s2); s3 = dot2(raw[c][3], ones, s3); s4 = dot2(raw[c][3], raw[c][3], s4);
    }
    s1 += s3; s2 += s4;
    s1 += dpp_f<0xB1>(s1); s1 += dpp_f<0x4E>(s1);
    s2 += dpp_f<0xB1>(s2); s2 += dpp_f<0x4E>(s2);
    const float mean = s1*(1.f/576.f), var = fmaxf(s2*(1.f/576.f) - mean*mean, 0.f);
    const float rs = __builtin_amdgcn_rsqf(var + 1e-5f), sh = -mean*rs;
    const f32x2 rs2 = {rs, rs}, sh2 = {sh, sh};
    int xw = XCH_OFF + w*18432 + (bq >> 1)*1024 + (32*(bq & 1) + ((16*mt + a) ^ (2*bq)))*16;
    int xr0 = XCH_OFF + w*18432 + (32*(lane >> 5) + ((lane & 31) ^ (2*(lane >> 5))))*16;
    int xr1 = XCH_OFF + w*18432 + 1024 + (32*(lane >> 5) + ((lane & 31) ^ (4 + 2*(lane >> 5))))*16;
    OPAQUE(xw); OPAQUE(xr0); OPAQUE(xr1);
    auto norm_write = [&](auto C) {
      constexpr int c = C.value;
      u32x4 o;
      #pragma unroll
      for (int d = 0; d < 4; d++) o[d] = pack2(__builtin_elementwise_fma(f32x2{bf_lo(raw[c][d]), bf_hi(raw[c][d])}, rs2, sh2));
      *(LAS u32x4*)(lds + xw + (c % 9)*2048) = o;
    };
    T = load_epoch(ws);
    STAMP(1);
    static_for<0, 9>(norm_write);
    WAITL(0);
    STAMP(7);
    BAR();
    if (isD) p1_issue();
    if (wave < 4) {
      #pragma unroll
      for (int c = 0; c < 9; c++) { xn[2*c] = *(LAS bf16x8*)(lds + xr0 + c*2048); xn[2*c + 1] = *(LAS bf16x8*)(lds + xr1 + c*2048); }
      WAITL(0);
    }
    STAMP(9);
    BAR();
    if (isD) p1_issue();
    static_for<9, 18>(norm_write);
    if (isD) WAITV(9);
    WAITL(0);
    BAR();
    if (isD) { p1_issue(); p1_issue(); p1_offsets(IC<1>{}, cq); }
    if (wave >= 6) {
      const u32x4 s_a = make_srd(w_in, 576*9216), s_b = make_srd(wqkv, 576*3456);
      SETM0(149504);
      const int vl = lane*16;
      #pragma unroll
      for (int k = 0; k < 11; k++) DMA(vl, s_a, b*20736 + ((w - 2)*11 + k)*1024, 0);
      #pragma unroll
      for (int k = 0; k < 4; k++) DMA(vl, s_b, b*7776 + ((w - 2)*4 + k)*1024, 0);
    }
    if (wave < 4) {
      #pragma unroll
      for (int c = 0; c < 9; c++) { xn[18 + 2*c] = *(LAS bf16x8*)(lds + xr0 + c*2048); xn[18 + 2*c + 1] = *(LAS bf16x8*)(lds + xr1 + c*2048); }
    }
    STAMP(2);
  }

  unsigned z1p[9][4];
  f32x4 acc[9][2];

  if (wave < 4) {
    constexpr int D = 3, NB = 4, NT = 5, FS = 4*NT;
    int wl = (8*(lane >> 5) + 2*((lane >> 2) & 3))*288 + ((lane >> 4) & 1)*32 + (lane & 3)*8;
    int slot_off = 0;
    f32x16 a32[5];
    #pragma unroll 1
    for (int pass = 0; pass < 3; pass++) {
      asm volatile("" : "+s"(pass));
      bf16x8 wf[NB];
      int wc = wl + slot_off, wn;
      OPAQUE(wc);
      auto rdw = [&](auto G, int base) {
        constexpr int i = G.value % FS, k16 = i / NT, n = i % NT;
        LAS char* p = lds + base + (k16 >> 1)*9216 + (k16 & 1)*16*288 + n*64;
        s16x4 lo = __builtin_amdgcn_ds_read_tr16_b64_v4i16((LAS s16x4*)p);
        s16x4 hi = __builtin_amdgcn_ds_read_tr16_b64_v4i16((LAS s16x4*)(p + 288));
        wf[G.value % NB] = __builtin_shufflevector(lo, hi, 0, 1, 2, 3, 4, 5, 6, 7);
      };
      #pragma unroll
      for (int n = 0; n < 5; n++)
        #pragma unroll
        for (int e = 0; e < 16; e++) a32[n][e] = 0.f;
      static_for<0, D>([&](auto G) { rdw(G, wc); });
      SB();
      if (pass == 1) STAMP(16);
      static_for<0, 9*FS>([&](auto G) {
        constexpr int st = G.value / FS, i = G.value % FS, k16 = i / NT, n = i % NT;
        if constexpr (i == 0) { slot_off = (slot_off >= 4*P1_STAGE) ? 0 : slot_off + P1_STAGE; wn = wl + slot_off; OPAQUE(wn); }
        if constexpr (i == FS - D) { SB(); PIN5(a32);
          BAR(); SB(); }
        a32[n] = MFMA32(wf[G.value % NB], xn[4*st + k16], a32[n]);
        if constexpr (G.value + D < 9*FS) { if constexpr (i + D < FS) rdw(IC<G.value + D>{}, wc); else rdw(IC<G.value + D>{}, wn); }
        SG(0x8, 1); SG(0x100, 2);
        SB();
        if constexpr (i == FS - 1) wc = wn;
      });
      PIN5(a32);
      if (pass == 0) STAMP(14);
      int l2 = lane; OPAQUE(l2);
      const int sr = l2 & 31, h4 = (l2 >> 5)*4;
      auto pk4 = [&](int n, int rq) { return u32x2{pack2(f32x2{a32[n][4*rq], a32[n][4*rq + 1]}), pack2(f32x2{a32[n][4*rq + 2], a32[n][4*rq + 3]})}; };
      if (pass == 0) {
        f32x2 q1 = {0.f, 0.f}, q2 = {0.f, 0.f};
        LAS char* p = lds + ZB_OFF + w*IMG_W + sr*IMG_PITCH + h4*2;
        #pragma unroll
        for (int n = 0; n < 5; n++)
          #pragma unroll
          for (int rq = 0; rq < 4; rq++) if (n < 4 || rq < 2) {
            const f32x2 ya = gelu2(f32x2{a32[n][4*rq], a32[n][4*rq + 1]}), yb = gelu2(f32x2{a32[n][4*rq + 2], a32[n][4*rq + 3]});
            q1 += ya; q2 = __builtin_elementwise_fma(ya, ya, q2);
            q1 += yb; q2 = __builtin_elementwise_fma(yb, yb, q2);
            *(LAS u32x2*)(p + n*64 + rq*16) = u32x2{pack2(ya), pack2(yb)};
          }
        float t1 = q1[0] + q1[1], t2 = q2[0] + q2[1];
        t1 += __shfl_xor(t1, 32); t2 += __shfl_xor(t2, 32);
        const u32x4 rec = {__builtin_bit_cast(unsigned, t1), T, __builtin_bit_cast(unsigned, t2), T};
        if (l2 < 32) __builtin_amdgcn_raw_buffer_store_b128(rec, MKRSRC(ws + WS_STAT, 2048*256), ((pi*16 + pj)*128 + 32*w + sr)*16, 0, AUX_SC1);
        STAMP(3);
        WAITL(0);
      } else if (pass == 1) {
        LAS char* p = lds + QB_OFF + (16 + 32*w + sr)*QB_PITCH + h4*2;
        #pragma unroll
        for (int n = 0; n < 4; n++)
          #pragma unroll
          for (int rq = 0; rq < 4; rq++) if (n < 3 || rq < 3 || h4 == 0) *(LAS u32x2*)(p + n*64 + rq*16) = pk4(n, rq);
        STAMP(4);
        WAITL(0);
      }
    }
    {
      int l2 = lane; OPAQUE(l2);
      const int sr = l2 & 31, h4 = (l2 >> 5)*4;
      LAS char* p = lds + Z1B_OFF + w*IMG_W + sr*IMG_PITCH + h4*2;
      #pragma unroll
      for (int n = 0; n < 5; n++)
        #pragma unroll
        for (int rq = 0; rq < 4; rq++) if (n < 4 || rq < 2)
          *(LAS u32x2*)(p + n*64 + rq*16) = u32x2{pack2(f32x2{a32[n][4*rq], a32[n][4*rq + 1]}), pack2(f32x2{a32[n][4*rq + 2], a32[n][4*rq + 3]})};
      STAMP(5);
      WAITL(0);
    }
    BAR();
  } else {
    const int g = lane >> 4, fl = lane & 15;
    __builtin_amdgcn_s_setprio(3);
    if (isD) {
      WAITV(18); VB();
      #pragma unroll 1
      for (int q = 1; q < 6; q++) { p1_issue(); WAITV(18); VB(); }
      #pragma unroll
      for (int t = 0; t < 9; t++) { const int t_ = cv[t]; cv[t] = cq[t]; cq[t] = t_; }
      csrd = make_srd(wqkv, 576*3456); p1so = 32*(w & 1)*3456 + ((72*pj) & ~15); p1step = 64*3456;
      #pragma unroll 1
      for (int q = 6; q < 15; q++) { p1_issue(); WAITV(18); VB(); }
      #pragma unroll
      for (int t = 0; t < 9; t++) cv[t] = cq[t];
      csrd = make_srd(w_in, 576*9216); p1so = 32*(w & 1)*9216 + 144*pj*2; p1step = 64*9216;
      #pragma unroll 1
      for (int q = 15; q < 24; q++) {
        if (q == 19 && wave == 4 && pi < 15) {
          const RSRC r_halo = MKRSRC(ws + WS_HALO + (size_t)(pi*16 + pj)*4096, 4096);
          #pragma unroll
          for (int c = 0; c < 4; c++) { int ch = c*64 + lane; if (ch > 247) ch = 247;
            u32x4 v = *(LAS u32x4*)(lds + QB_OFF + 128*QB_PITCH + ch*16);
            __builtin_amdgcn_raw_buffer_store_b128(v, r_halo, ch*16, 0, AUX_SC1); }
        }
        p1_issue(); WAITV(18); VB();
        if (q == 20 && wave == 4 && lane == 0 && pi < 15) set_flag(ws, FL_HALO, pi, pj, T);
      }
      WAITV(9); VB();
      WAITV(0); VB();
      if (wave == 5) {
        if (pi > 0) {
          gu32* fp = (gu32*)(ws + WS_FLAGS + (size_t)(FL_HALO*16 + pi - 1)*256) + pj;
          for (int spin = 0; spin < SPIN_MAX; ++spin) { if (__builtin_amdgcn_readfirstlane(__hip_atomic_load(fp, RLX_AGENT)) == T) break; __builtin_amdgcn_s_sleep(1); }
          const RSRC r_halo = MKRSRC(ws + WS_HALO + (size_t)((pi - 1)*16 + pj)*4096, 4096);
          #pragma unroll
          for (int c = 0; c < 4; c++) { int ch = c*64 + lane; if (ch > 247) ch = 247;
            u32x4 v = __builtin_amdgcn_raw_buffer_load_b128(r_halo, ch*16, 0, AUX_SC1);
            *(LAS u32x4*)(lds + QB_OFF + ch*16) = v; }
        } else {
          #pragma unroll
          for (int c = 0; c < 4; c++) { int ch = c*64 + lane; if (ch > 247) ch = 247; *(LAS u32x4*)(lds + QB_OFF + ch*16) = u32x4{0, 0, 0, 0}; }
        }
        WAITL(0);
      }
      VB();
    } else {
      const int gA = 2*(w - 2);
      {
        const u32x4 s_a = make_srd(w_sp, 2048*4096), s_b = make_srd(w_out, 2304*1152), s_c = make_srd(wo, 576*1152);
        SETM0(149504);
        const int vl = lane*16;
        #pragma unroll 1
        for (int q = 0; q < 8; q++) VB();
        #pragma unroll 1
        for (int q = 8; q < 16; q++) {
          DMA(vl, s_a, b*32768 + ((w - 2)*16 + 2*(q - 8))*1024, 0); DMA(vl, s_a, b*32768 + ((w - 2)*16 + 2*(q - 8) + 1)*1024, 0);
          if (q < 14) DMA(vl, s_b, b*10368 + ((w - 2)*6 + (q - 8))*1024, 0);
          else DMA(vl, s_c, b*2592 + ((w - 2)*2 + (q - 14))*1024, 0);
          VB();
        }
      }
      LAS f32x2* st = (LAS f32x2*)(lds + MISC_OFF + (w - 2)*512);
      {
        const u32x4 s_st = make_srd(ws + WS_STAT, 2048*256);
        const int vo = (pi*16*128 + 32*gA + lane)*16;
        u32x4 rec[16];
        static_for<0, 16>([&](auto C) { u32x4 t; asm volatile("buffer_load_dwordx4 %0, %1, %2, %3 offen sc1" : "=v"(t) : "v"(vo), "s"(s_st), "s"(C.value*2048) : "memory"); rec[C.value] = t; });
        WAITV(16);
        VB();
        VB();
        asm volatile("s_waitcnt vmcnt(0)" : "+v"(rec[0]), "+v"(rec[1]), "+v"(rec[2]), "+v"(rec[3]), "+v"(rec[4]), "+v"(rec[5]), "+v"(rec[6]), "+v"(rec[7]),
                          "+v"(rec[8]), "+v"(rec[9]), "+v"(rec[10]), "+v"(rec[11]), "+v"(rec[12]), "+v"(rec[13]), "+v"(rec[14]), "+v"(rec[15]) :: "memory");
        float t1 = 0.f, t2 = 0.f; bool ok = true;
        #pragma unroll
        for (int c = 0; c < 16; c++) { t1 += asf(rec[c][0]); t2 += asf(rec[c][2]); ok = ok && rec[c][1] == T && rec[c][3] == T; }
        if (!__all(ok)) {
          const RSRC r_stat = MKRSRC(ws + WS_STAT, 2048*256);
          for (int spin = 0; spin < SPIN_MAX; ++spin) {
            t1 = 0.f; t2 = 0.f; ok = true;
            #pragma unroll
            for (int c = 0; c < 16; c++) {
              u32x4 v = __builtin_amdgcn_raw_buffer_load_b128(r_stat, vo + c*2048, 0, AUX_SC1);
              t1 += asf(v[0]); t2 += asf(v[2]); ok = ok && v[1] == T && v[3] == T;
            }
            if (__all(ok)) break;
            __builtin_amdgcn_s_sleep(1);
          }
        }
        const float mean = t1*(1.f/2304.f), var = fmaxf(t2*(1.f/2304.f) - mean*mean, 0.f);
        const float rs = __builtin_amdgcn_rsqf(var + 1e-5f);
        st[lane] = f32x2{rs, -mean*rs};
        WAITL(0);
        VB();
      }
      const RSRC r_z2n = MKRSRC(ws + WS_Z2N, 16*64*9*1024);
      #pragma unroll 1
      for (int k = 0; k < 2; k++) {
        f32x2 r2[4], h2[4];
        #pragma unroll
        for (int d = 0; d < 4; d++) { f32x2 u0 = st[32*k + 8*g + 2*d], u1 = st[32*k + 8*g + 2*d + 1]; r2[d] = f32x2{u0[0], u1[0]}; h2[d] = f32x2{u0[1], u1[1]}; }
        LAS char* p = lds + ZB_OFF + (gA + k)*IMG_W + (8*g + ((lane >> 2) & 3))*IMG_PITCH + (lane & 3)*8;
        int zo = lane*16 + (pj*64 + 4*pi + gA + k)*9216;
        #pragma unroll 1
        for (int j3 = 0; j3 < 3; j3++, p += 96, zo += 3072) {
          u32x4 v[3];
          #pragma unroll
          for (int u = 0; u < 3; u++) {
            s16x4 lo = __builtin_amdgcn_ds_read_tr16_b64_v4i16((LAS s16x4*)(p + u*32));
            s16x4 hi = __builtin_amdgcn_ds_read_tr16_b64_v4i16((LAS s16x4*)(p + u*32 + 4*IMG_PITCH));
            v[u] = __builtin_bit_cast(u32x4, __builtin_shufflevector(lo, hi, 0, 1, 2, 3, 4, 5, 6, 7));
          }
          #pragma unroll
          for (int u = 0; u < 3; u++) {
            u32x4 o;
            #pragma unroll
            for (int d = 0; d < 4; d++) o[d] = pack2(f32x2{opq(__builtin_fmaf(bf_lo(v[u][d]), r2[d][0], h2[d][0])), opq(__builtin_fmaf(bf_hi(v[u][d]), r2[d][1], h2[d][1]))});
            __builtin_amdgcn_raw_buffer_store_b128(o, r_z2n, zo + u*1024, 0, AUX_SC1);
          }
          VB();
        }
      }
      VB();
      VB();
    }
    STAMP(5);
    BAR();
    if (!isD) { WAITV(0); if (lane == 0) set_flag(ws, FL_Z, pj, 2*pi + (w - 2), T); }
    {
      #pragma unroll
      for (int m = 0; m < 2; m++) {
        LAS char* zx = lds + Z1B_OFF + w*IMG_W + (8*(fl >> 2) + 4*m + (fl & 3))*IMG_PITCH + g*8;
        #pragma unroll
        for (int n = 0; n < 9; n++) { u32x2 v = *(LAS u32x2*)(zx + n*32); z1p[n][2*m] = v[0]; z1p[n][2*m + 1] = v[1]; }
      }
    }
  }

  const u32x4 s_z2n = make_srd(ws + WS_Z2N + (size_t)pj*64*9*1024, 64*9*1024);
  const u32x4 s_wsp = make_srd(w_sp + (size_t)128*pi*2048, 128*4096);
  int vw[4];
  const int vlin = lane*16;
  auto p2_issue = [&](int s, auto ZR) {
    const int base = (s & 3)*P2_STAGE;
    if constexpr (ZR.value) {
      static_for<0, 9>([&](auto K) { SETM0(base + w*9216 + K.value*1024); DMAS(vlin, s_z2n, ((2*s + w)*9 + K.value)*1024, 0); });
    } else {
      static_for<0, 8>([&](auto K) { constexpr int k = K.value; const int wr = 2*(w - 2) + (k >> 2);
        SETM0(base + 18432 + wr*4096 + (k & 3)*1024); DMA(vw[k & 3], s_wsp, wr*32*4096 + s*128, 0); });
    }
  };
  if (wave >= 4) {
    { int l2 = lane; OPAQUE(l2);
      const int a = l2 >> 3, bb = l2 & 7;
      #pragma unroll
      for (int m = 0; m < 2; m++)
        #pragma unroll
        for (int hf = 0; hf < 2; hf++) { int rho = 8*hf + a; vw[2*m + hf] = (8*(rho >> 2) + 4*m + (rho & 3))*4096 + ((bb ^ ((rho >> 1) & 7))*16); } }
    if (w >= 2) {
      #pragma unroll 1
      for (int s = 0; s < 2; s++) p2_issue(s, IC<0>{});
    }
  }
  {
      int t2 = tid; OPAQUE(t2);
      const int u = t2 & 1, hh = (t2 >> 7) & 1, tl = ((t2 >> 1) & 63) + 64*(t2 >> 8);
      LAS char* rowp = lds + QB_OFF + (16 + tl)*QB_PITCH + 8*(pj & 1) + 36*hh;
      const int o8 = hh*4, o4 = hh ? 0 : 32;
      u32x2 qv[4]; unsigned q0;
      #pragma unroll
      for (int d = 0; d < 4; d++) qv[d] = *(LAS u32x2*)(rowp + o8 + d*8);
      q0 = *(LAS unsigned*)(rowp + o4);
      float lg[4];
      #pragma unroll
      for (int ww = 0; ww < 4; ww++) {
        LAS char* kr = rowp - (2*(4*u + ww))*QB_PITCH + 80;
        float sa = dot2(q0, *(LAS unsigned*)(kr + o4), 0.f), sb = 0.f;
        #pragma unroll
        for (int d = 0; d < 4; d++) { u32x2 kv = *(LAS u32x2*)(kr + o8 + d*8); sa = dot2(qv[d][0], kv[0], sa); sb = dot2(qv[d][1], kv[1], sb); }
        float s = (sa + sb)*0.23570226039551584f;
        if (128*pi + tl - 2*(4*u + ww) < 0) s = -1e9f;
        lg[ww] = s;
      }
      float l8[8];
      #pragma unroll
      for (int ww = 0; ww < 4; ww++) { float o = dpp_f<0xB1>(lg[ww]); l8[ww] = u ? o : lg[ww]; l8[4 + ww] = u ? lg[ww] : o; }
      float mx = l8[0];
      #pragma unroll
      for (int ww = 1; ww < 8; ww++) mx = fmaxf(mx, l8[ww]);
      float sum = 0.f;
      #pragma unroll
      for (int ww = 0; ww < 8; ww++) { l8[ww] = __builtin_amdgcn_exp2f((l8[ww] - mx)*1.4426950408889634f); sum += l8[ww]; }
      const float inv = __builtin_amdgcn_rcpf(sum);
      float olo[5], ohi[5];
      #pragma unroll
      for (int d = 0; d < 5; d++) { olo[d] = 0.f; ohi[d] = 0.f; }
      #pragma unroll
      for (int ww = 0; ww < 8; ww++) {
        const unsigned pb = pack_bf16(l8[ww]*inv, 0.f);
        const unsigned ph = pb << 16;
        LAS char* vr = rowp - (2*ww)*QB_PITCH + 160;
        u32x2 va = *(LAS u32x2*)(vr + o8 + 16*u), vb = *(LAS u32x2*)(vr + o8 + 16*u + 8); unsigned vc = *(LAS unsigned*)(vr + o4);
        olo[0] = dot2(va[0], pb, olo[0]); ohi[0] = dot2(va[0], ph, ohi[0]); olo[1] = dot2(va[1], pb, olo[1]); ohi[1] = dot2(va[1], ph, ohi[1]);
        olo[2] = dot2(vb[0], pb, olo[2]); ohi[2] = dot2(vb[0], ph, ohi[2]); olo[3] = dot2(vb[1], pb, olo[3]); ohi[3] = dot2(vb[1], ph, ohi[3]);
        olo[4] = dot2(vc, pb, olo[4]); ohi[4] = dot2(vc, ph, ohi[4]);
      }
      *(LAS u32x2*)(rowp + o8 + 16*u) = u32x2{pack_bf16(olo[0], ohi[0]), pack_bf16(olo[1], ohi[1])};
      *(LAS u32x2*)(rowp + o8 + 16*u + 8) = u32x2{pack_bf16(olo[2], ohi[2]), pack_bf16(olo[3], ohi[3])};
      if (u) *(LAS unsigned*)(rowp + o4) = pack_bf16(olo[4], ohi[4]);
      }
  WAITL(0);
  STAMP(21);
  BAR();
  STAMP(22);
  if (wave >= 4 && w < 2) { wait_flags(ws, FL_Z, pj, 32, T, lane); STAMP(13); p2_issue(0, IC<1>{}); p2_issue(1, IC<1>{}); }
  if (wave >= 4) {
    #pragma unroll
    for (int n = 0; n < 9; n++) {
      #pragma unroll
      for (int d = 0; d < 4; d++) z1p[n][d] = pack2(gelu2(f32x2{bf_lo(z1p[n][d]), bf_hi(z1p[n][d])}));
      asm volatile("" : "+v"(z1p[n][0]), "+v"(z1p[n][1]), "+v"(z1p[n][2]), "+v"(z1p[n][3]));
    }
  }
  if (wave < 4) {
    const RSRC r_o = MKRSRC(ws + WS_O, 128*18*1024);
    #pragma unroll 1
    for (int e = tid; e < 1152; e += 256) {
      const int tl = e / 9, p = e % 9;
      u32x2 v = *(LAS u32x2*)(lds + QB_OFF + (16 + tl)*QB_PITCH + 8*(pj & 1) + 8*p);
      const int col = 36*pj + 4*p, r32 = tl & 31;
      const int tt = 8*pi + 2*(tl >> 5) + ((r32 >> 2) & 1), rho = 4*(r32 >> 3) + (r32 & 3);
      const int ks = col >> 5, c32 = col & 31;
      __builtin_amdgcn_raw_buffer_store_b64(v, r_o, ((tt*18 + ks)*64 + rho + 16*((c32 & 15) >> 2))*16 + (c32 >> 4)*8, 0, AUX_SC1);
    }
  }
  WAITL(0);
  BAR();
  STAMP(6);

  const int p3cnt = (xcd < 2) ? 30 : 29;
  const int p3T = xcd*29 + (xcd < 2 ? xcd : 2) + rr;
  const bool p3act = rr < p3cnt;
  const int rg = p3T / 9, pc = p3T % 9;

  if (wave >= 4) {
    __builtin_amdgcn_s_setprio(0);
    auto p2_helper = [&](auto ZR) {
      constexpr int CNT = ZR.value ? 9 : 8;
      p2_issue(2, ZR); p2_issue(3, ZR);
      WAITV(3*CNT);
      VB();
      auto piece = [&](int s, auto K) {
        constexpr int k = K.value; const int base = (s & 3)*P2_STAGE;
        if constexpr (ZR.value) { SETM0(base + w*9216 + k*1024); DMAS(vlin, s_z2n, ((2*s + w)*9 + k)*1024, 0); }
        else if constexpr (k < 8) { const int wr = 2*(w - 2) + (k >> 2); SETM0(base + 18432 + wr*4096 + (k & 3)*1024); DMA(vw[k & 3], s_wsp, wr*32*4096 + s*128, 0); }
      };
      #pragma unroll 1
      for (int s = 0; s < 28; s++) {
        WAITV(2*CNT);
        VB();
        if (s == 4 && wave == 4 && lane == 0) set_flag(ws, FL_O, pi, pj, T);
        static_for<0, 9>([&](auto K) { piece(s + 4, K); });
      }
      WAITV(2*CNT); VB();
      WAITV(CNT); VB();
      #pragma unroll
      for (int n = 0; n < 9; n++) *(LAS u32x4*)(lds + w*9216 + n*1024 + lane*16) = u32x4{z1p[n][0], z1p[n][1], z1p[n][2], z1p[n][3]};
      WAITV(0); WAITL(0); VB();
      VB();
    };
    if (w < 2) p2_helper(IC<1>{}); else p2_helper(IC<0>{});
    STAMP(8);
    if (p3act) {
      const u32x4 s_o = make_srd(ws + WS_O, 128*18*1024), s_h = make_srd(ws + WS_H, 128*72*1024);
      const u32x4 s_wo = make_srd(wo, 576*1152), s_wout = make_srd(w_out, 2304*1152);
      int vW0, vW1, vA[5];
      int l2 = lane; OPAQUE(l2);
      { const int r = l2 >> 3;
        vW0 = r*1152 + (((l2 & 7) ^ (((r >> 1) & 3) << 1))*16);
        vW1 = (r + 8)*1152 + (((l2 & 7) ^ ((((r + 8) >> 1) & 3) << 1))*16);
        #pragma unroll
        for (int m5 = 0; m5 < 5; m5++) { int tt = 5*rg + m5; if (tt > 127) tt = 127; vA[m5] = l2*16 + tt*18*1024; } }
      int slot = 2;
      auto p3_ks = [&](u32x4 sw, u32x4 sa, int R0, int R1, int ks) {
        if (w < 3) {
          const int base = slot*P3_STAGE + w*9216, s0 = R0*1152 + pc*128, s1 = R1*1152 + pc*128;
          SETM0(base + 5120); DMA(vW0, sw, s0, 0); SETM0(base + 6144); DMA(vW1, sw, s0, 0);
          SETM0(base + 7168); DMA(vW0, sw, s1, 0); SETM0(base + 8192); DMA(vW1, sw, s1, 0);
          static_for<0, 5>([&](auto M) { SETM0(base + M.value*1024); DMAS(vA[M.value], sa, ks*1024, 0); });
        }
        slot = (slot == 4) ? 0 : slot + 1;
      };
      auto p3_wo = [&](int u) { const int ks = 3*u + w; p3_ks(s_wo, s_o, 32*ks, 32*ks + 16, ks); };
      auto p3_g3 = [&](int v) { const int ks = 3*v + w;
        const int R0 = (ks < 64) ? 144*(ks >> 2) + 32*(ks & 3) : 288*(ks - 64) + 128;
        p3_ks(s_wout, s_h, R0, R0 + ((ks < 64) ? 16 : 144), ks); };
      const int p0 = (5*rg) >> 3, p1 = ((5*rg + 4 > 127 ? 127 : 5*rg + 4)) >> 3;
      if (w < 3) { wait_flags(ws, FL_O, p0, 16, T, lane); wait_flags(ws, FL_O, p1, 16, T, lane); }
      STAMP(24);
      #pragma unroll 1
      for (int u = 0; u < 3; u++) p3_wo(u);
      BAR();
      p3_wo(3); p3_wo(4);
      WAITV(27);
      STAMP(26);
      BAR();
      WAITV(18); BAR();
      p3_wo(5); WAITV(18); BAR();
      WAITV(9); BAR();
      WAITV(0); BAR();
      BAR();
      #pragma unroll
      for (int m5 = 0; m5 < 5; m5++) { int tt = 5*rg + m5; if (tt > 127) tt = 127; vA[m5] = l2*16 + tt*72*1024; }
      if (w < 3) { wait_flags(ws, FL_H, p0, 16, T, lane); wait_flags(ws, FL_H, p1, 16, T, lane); }
      STAMP(27);
      RSTAMP(31);
      #pragma unroll 1
      for (int v = 0; v < 4; v++) p3_g3(v);
      WAITV(18); BAR();
      #pragma unroll 1
      for (int u = 6; u < 26; u++) {
        p3_g3(u - 2);
        WAITV(18); BAR();
      }
      WAITV(9); BAR();
      WAITV(0); BAR();
      BAR();
    }
  } else {
    int l2 = lane; OPAQUE(l2);
    const int g = l2 >> 4, fl = l2 & 15;
    const int zoff = l2*16;
    const int woff0 = 18432 + (4*w + (fl >> 3))*1024 + (fl & 7)*128 + ((g ^ ((fl >> 1) & 7))*16);
    float bs[2];
    #pragma unroll
    for (int m = 0; m < 2; m++) bs[m] = bf_lo(b_sp[128*pi + 32*w + 8*(fl >> 2) + 4*m + (fl & 3)]);
    #pragma unroll
    for (int n = 0; n < 9; n++) { acc[n][0] = f32x4{bs[0], bs[0], bs[0], bs[0]}; acc[n][1] = f32x4{bs[1], bs[1], bs[1], bs[1]}; }
    bf16x8 z0[9], w0[2], z1[9], w1[2];
    auto rd = [&](bf16x8 (&z)[9], bf16x8 (&wv)[2], int s, int kk) {
      const int base = (s & 3)*P2_STAGE;
      LAS char* pw = lds + base + (woff0 ^ (kk*64));
      LAS char* pz = lds + base + kk*9216 + zoff;
      wv[0] = *(LAS bf16x8*)pw; wv[1] = *(LAS bf16x8*)(pw + 2048);
      #pragma unroll
      for (int n = 0; n < 9; n++) z[n] = *(LAS bf16x8*)(pz + n*1024);
    };
    auto mm = [&](bf16x8 (&z)[9], bf16x8 (&wv)[2]) {
      #pragma unroll
      for (int n = 0; n < 9; n++) { acc[n][0] = MFMA(z[n], wv[0], acc[n][0]); acc[n][1] = MFMA(z[n], wv[1], acc[n][1]); }
      #pragma unroll
      for (int q = 0; q < 11; q++) { SG(0x8, 1); SG(0x100, 1); }
      SG(0x8, 7);
    };
    BAR();
    rd(z0, w0, 0, 0);
    CWAITL0(); SB();
    #pragma unroll 1
    for (int s = 0; s < 32; s++) {
      CWAITL0(); SB();
      rd(z1, w1, s, 1);
      mm(z0, w0);
      SB(); CWAITL0(); PIN_ACC(acc);
      if (s == 4) WAITV(0);
      BAR(); SB();
      rd(z0, w0, (s + 1 < 32 ? s + 1 : s), 0);
      mm(z1, w1);
      SB();
    }
    STAMP(8);
    {
      const RSRC r_h = MKRSRC(ws + WS_H, 128*72*1024);
      u32x4 z1g[9];
      #pragma unroll
      for (int n = 0; n < 9; n++) z1g[n] = *(LAS u32x4*)(lds + w*9216 + n*1024 + zoff);
      #pragma unroll
      for (int m = 0; m < 2; m++) {
        const int tt = 8*pi + 2*w + m;
        unsigned hp[9][2];
        #pragma unroll
        for (int n = 0; n < 9; n++) {
          hp[n][0] = pack2(f32x2{bf_lo(z1g[n][2*m]), bf_hi(z1g[n][2*m])}*f32x2{acc[n][m][0], acc[n][m][1]});
          hp[n][1] = pack2(f32x2{bf_lo(z1g[n][2*m + 1]), bf_hi(z1g[n][2*m + 1])}*f32x2{acc[n][m][2], acc[n][m][3]});
        }
        #pragma unroll
        for (int q = 0; q < 4; q++)
          __builtin_amdgcn_raw_buffer_store_b128(u32x4{hp[2*q][0], hp[2*q][1], hp[2*q + 1][0], hp[2*q + 1][1]}, r_h, zoff + (tt*72 + 4*pj + q)*1024, 0, AUX_SC1);
        __builtin_amdgcn_raw_buffer_store_b64(u32x2{hp[8][0], hp[8][1]}, r_h, zoff + (pj & 1)*8 + (tt*72 + 64 + (pj >> 1))*1024, 0, AUX_SC1);
      }
    }
    STAMP(19);
    RSTAMP(29);
    if (p3act) {
      WAITL(0);
      BAR();
      f32x4 aS[5], aC[5];
      u32x2 xv[5];
      const int q4 = (l2 >> 2) & 3, p4 = l2 & 3, r0 = 4*g + q4;
      const int woff = 5120 + r0*128 + (((2*w + (p4 >> 1)) ^ (((r0 >> 1) & 3) << 1))*16) + (p4 & 1)*8;
      bf16x8 hA[3][5], wA[3], hB[3][5], wB[3];
      int slot = 2;
      auto rd3 = [&](bf16x8 (&h)[3][5], bf16x8 (&wv)[3]) {
        LAS char* p = lds + slot*P3_STAGE;
        #pragma unroll
        for (int kk = 0; kk < 3; kk++) {
          s16x4 lo = __builtin_amdgcn_ds_read_tr16_b64_v4i16((LAS s16x4*)(p + kk*9216 + woff));
          s16x4 hi = __builtin_amdgcn_ds_read_tr16_b64_v4i16((LAS s16x4*)(p + kk*9216 + woff + 2048));
          wv[kk] = __builtin_shufflevector(lo, hi, 0, 1, 2, 3, 4, 5, 6, 7);
          #pragma unroll
          for (int m5 = 0; m5 < 5; m5++) h[kk][m5] = *(LAS bf16x8*)(p + kk*9216 + m5*1024 + zoff);
        }
        slot = (slot == 4) ? 0 : slot + 1;
      };
      auto mm3 = [&](bf16x8 (&h)[3][5], bf16x8 (&wv)[3]) {
        #pragma unroll
        for (int kk = 0; kk < 3; kk++)
          #pragma unroll
          for (int m5 = 0; m5 < 5; m5++) aC[m5] = MFMA(wv[kk], h[kk][m5], aC[m5]);
        #pragma unroll
        for (int q = 0; q < 15; q++) { SG(0x8, 1); SG(0x100, 1); }
        SG(0x100, 6);
      };
      #pragma unroll
      for (int m5 = 0; m5 < 5; m5++) {
        int tt = 5*rg + m5; if (tt > 127) tt = 127;
        const int row = 32*(tt >> 1) + 8*(fl >> 2) + 4*(tt & 1) + (fl & 3);
        xv[m5] = *(const u32x2*)(x + (size_t)row*D_ + 64*pc + 16*w + 4*g);
      }
      BAR();
      #pragma unroll 1
      for (int ph = 0; ph < 2; ph++) {
        #pragma unroll
        for (int m5 = 0; m5 < 5; m5++) aC[m5] = f32x4{0,0,0,0};
        rd3(hA, wA); SB();
        const int nit = ph ? 12 : 3;
        #pragma unroll 1
        for (int it = 0; it < nit; it++) {
          CWAITL0(); SB(); rd3(hB, wB); mm3(hA, wA); SB(); PIN5(aC); BAR(); SB();
          CWAITL0(); SB(); rd3(hA, wA); mm3(hB, wB); SB(); PIN5(aC);
          if (ph == 0 && it == 1) { WAITV(0); RSTAMP(30); }
          if (ph == 0 || it < 11) BAR();
          SB();
          if (ph == 0 && it == 1 && tid == 0) set_flag(ws, FL_H, pi, pj, T);
        }
        if (ph == 0) {
          #pragma unroll
          for (int m5 = 0; m5 < 5; m5++) aS[m5] = aC[m5];
          slot = 3;
          STAMP(10);
        }
      }
      STAMP(11);
      #pragma unroll
      for (int m5 = 0; m5 < 5; m5++) {
        const int tt = 5*rg + m5;
        if (tt < 128) {
          const int row = 32*(tt >> 1) + 8*(fl >> 2) + 4*(tt & 1) + (fl & 3);
          const size_t o = (size_t)row*D_ + 64*pc + 16*w + 4*g;
          float r[4];
          #pragma unroll
          for (int e = 0; e < 4; e++) {
            float bv = aC[m5][e], sv = aS[m5][e];
            float sl = bv*__builtin_amdgcn_rcpf(1.f + __builtin_amdgcn_exp2f(-1.4426950408889634f*bv));
            unsigned xw = xv[m5][e >> 1];
            float xx = (e & 1) ? bf_hi(xw) : bf_lo(xw);
            r[e] = __builtin_fmaf(sv, sl, xx);
          }
          *(u32x2*)(out + o) = u32x2{pack_bf16(r[0], r[1]), pack_bf16(r[2], r[3])};
        }
      }
    } else {
      WAITV(0);
      BAR();
      if (tid == 0) set_flag(ws, FL_H, pi, pj, T);
    }
    if (b == 0 && tid == 0) __hip_atomic_store((gu32*)ws, T, RLX_AGENT);
    STAMP(12);
  }
  TDUMP();
  asm volatile(".Lpf_end:");
}

extern "C" void kernel_launch(void* const* d_in, const int* in_sizes, int n_in, void* d_out, int out_size, void* d_ws, size_t ws_size, hipStream_t stream) {
  (void)hipFuncSetAttribute((const void*)fused_kernel, hipFuncAttributeMaxDynamicSharedMemorySize, LDS_BYTES);
  fused_kernel<<<256, 512, LDS_BYTES, stream>>>((const u16*)d_in[0], (const u16*)d_in[1], (const u16*)d_in[2], (const u16*)d_in[4],
      (const u16*)d_in[5], (const u16*)d_in[6], (const u16*)d_in[7], (u16*)d_out, (char*)d_ws);
}
```

```cpp
#include <hip/hip_runtime.h>
#include <stdint.h>

typedef unsigned short u16;
typedef __attribute__((ext_vector_type(8))) short bf16x8;
typedef __attribute__((ext_vector_type(4))) short s16x4;
typedef __attribute__((ext_vector_type(4))) float f32x4;
typedef __attribute__((ext_vector_type(16))) float f32x16;
typedef __attribute__((ext_vector_type(2))) float f32x2;
typedef __attribute__((ext_vector_type(4))) unsigned u32x4;
typedef __attribute__((ext_vector_type(2))) unsigned u32x2;
typedef __attribute__((ext_vector_type(2))) __bf16 bf16x2;
#define GAS __attribute__((address_space(1)))
#define LAS __attribute__((address_space(3)))
typedef GAS unsigned gu32;
#define RLX_AGENT __ATOMIC_RELAXED, __HIP_MEMORY_SCOPE_AGENT
#define BAR() asm volatile("s_barrier" ::: "memory")
#define SB() __builtin_amdgcn_sched_barrier(0)
#define SG(mask, n) __builtin_amdgcn_sched_group_barrier(mask, n, 0)
#define WAITV(n) asm volatile("s_waitcnt vmcnt(%0)" :: "i"(n) : "memory")
#define WAITL(n) asm volatile("s_waitcnt lgkmcnt(%0)" :: "i"(n) : "memory")
#define CWAITL0() __builtin_amdgcn_s_waitcnt(0xC07F)
#define RSRC __amdgpu_buffer_rsrc_t
#define MKRSRC(p, bytes) __builtin_amdgcn_make_buffer_rsrc((void*)(p), 0, (bytes), 0x00020000)
#define AUX_SC1 16
#define OPAQUE(x) asm volatile("" : "+v"(x))

constexpr int D_ = 576, FH = 2304;
constexpr size_t WS_FLAGS = 4096;
constexpr size_t WS_STAT  = 32768;
constexpr size_t WS_HALO  = WS_STAT + 2048*256;
constexpr size_t WS_Z2N   = WS_HALO + 16*16*4096;
constexpr size_t WS_H     = WS_Z2N + (size_t)16*64*9*1024;
constexpr size_t WS_O     = WS_H + (size_t)128*72*1024;
constexpr size_t WS_DBG   = WS_O + (size_t)128*18*1024;
constexpr int FL_STAT = 0, FL_Z = 1, FL_HALO = 2, FL_O = 3, FL_H = 4;
constexpr int LDS_BYTES = 163840;
constexpr int P1_STAGE = 18432, P1_NS = 5;
constexpr int XCH_OFF = 4*P1_STAGE;
constexpr int ZB_OFF = P1_NS*P1_STAGE;
constexpr int QB_PITCH = 248;
constexpr int IMG_PITCH = 296, IMG_W = 32*IMG_PITCH;
constexpr int QB_OFF = ZB_OFF + 4*IMG_W - 16*QB_PITCH;
constexpr int MISC_OFF = QB_OFF + 144*QB_PITCH;
constexpr int Z1B_OFF = 4*P1_STAGE;
constexpr int P2_STAGE = 34816;
constexpr int P3_STAGE = 27648;
constexpr int SPIN_MAX = 1 << 18;

template<int N> struct IC { static constexpr int value = N; };
template<int B, int E, class F> __device__ __forceinline__ void static_for(F&& f) {
  if constexpr (B < E) { f(IC<B>{}); static_for<B + 1, E>(f); }
}

__device__ __forceinline__ unsigned pack2(f32x2 v) { return __builtin_bit_cast(unsigned, __builtin_convertvector(v, bf16x2)); }
__device__ __forceinline__ float opq(float x) { asm("" : "+v"(x)); return x; }
__device__ __forceinline__ unsigned pack_bf16(float a, float b) { return pack2(f32x2{a, b}); }
__device__ __forceinline__ float asf(unsigned u) { return __builtin_bit_cast(float, u); }
__device__ __forceinline__ float bf_lo(unsigned u) { return __builtin_bit_cast(float, u << 16); }
__device__ __forceinline__ float bf_hi(unsigned u) { return __builtin_bit_cast(float, u & 0xffff0000u); }
__device__ __forceinline__ float dot2(unsigned a, unsigned b, float c) {
  return __builtin_amdgcn_fdot2_f32_bf16(__builtin_bit_cast(bf16x2, a), __builtin_bit_cast(bf16x2, b), c, false);
}
__device__ __forceinline__ float gelu_f(float x) {
  const float c1 = -2.302208198f, c2 = -0.1029432397f;
  float t = __builtin_fmaf(x * x, c2, c1);
  float e = __builtin_amdgcn_exp2f(x * t);
  return x * __builtin_amdgcn_rcpf(1.f + e);
}
__device__ __forceinline__ f32x2 gelu2(f32x2 x) {
  const f32x2 c1 = {-2.302208198f, -2.302208198f}, c2 = {-0.1029432397f, -0.1029432397f}, one = {1.f, 1.f};
  f32x2 t = __builtin_elementwise_fma(x * x, c2, c1);
  f32x2 a = x * t;
  f32x2 e = {__builtin_amdgcn_exp2f(a[0]), __builtin_amdgcn_exp2f(a[1])};
  f32x2 d = e + one;
  f32x2 r = {__builtin_amdgcn_rcpf(d[0]), __builtin_amdgcn_rcpf(d[1])};
  return x * r;
}
template<int CTRL> __device__ __forceinline__ float dpp_f(float v) {
  return __builtin_bit_cast(float, __builtin_amdgcn_update_dpp(0, __builtin_bit_cast(int, v), CTRL, 0xf, 0xf, true));
}
__device__ __forceinline__ float row16_allsum(float v) {
  v += dpp_f<0x128>(v); v += dpp_f<0x124>(v); v += dpp_f<0x122>(v); v += dpp_f<0x121>(v); return v;
}
__device__ __forceinline__ bool poll_flags(const char* ws, int group, int idx, int nflags, unsigned T, int lane) {
  gu32* p = (gu32*)(ws + WS_FLAGS + (size_t)(group*16 + idx)*256) + (lane & (nflags - 1));
  unsigned v = __hip_atomic_load(p, RLX_AGENT);
  return __all(v == T);
}
__device__ __forceinline__ void wait_flags(const char* ws, int group, int idx, int nflags, unsigned T, int lane) {
  for (int spin = 0; spin < SPIN_MAX; ++spin) {
    if (poll_flags(ws, group, idx, nflags, T, lane)) break;
    __builtin_amdgcn_s_sleep(1);
  }
}
__device__ __forceinline__ void set_flag(char* ws, int group, int idx, int k, unsigned T) {
  __hip_atomic_store((gu32*)(ws + WS_FLAGS + (size_t)(group*16 + idx)*256) + k, T, RLX_AGENT);
}
__device__ __forceinline__ unsigned load_epoch(const char* ws) {
  unsigned e;
  asm volatile("s_load_dword %0, %1, 0x0\n\ts_waitcnt lgkmcnt(0)" : "=s"(e) : "s"(ws) : "memory");
  return e + 1u;
}
__device__ __forceinline__ int kperm(int r) { return 8*(r >> 3) + 4*(r & 1) + ((r >> 1) & 3); }
__device__ __forceinline__ u32x4 make_srd(const void* p, unsigned bytes) {
  unsigned long long a = (unsigned long long)p;
  return u32x4{(unsigned)a, (unsigned)(a >> 32) & 0xffffu, bytes, 0x00020000u};
}
__device__ __forceinline__ void SETM0(int v) { asm volatile("s_mov_b32 m0, %0\n\ts_nop 0" :: "s"(v) : "memory"); }
template<int OFF> __device__ __forceinline__ void dma_o(int voff, u32x4 srd, int soff) {
  asm volatile("buffer_load_dwordx4 %0, %1, %2 offen offset:%3 lds" :: "v"(voff), "s"(srd), "s"(soff), "i"(OFF) : "memory");
}
template<int OFF> __device__ __forceinline__ void dma_s(int voff, u32x4 srd, int soff) {
  asm volatile("buffer_load_dwordx4 %0, %1, %2 offen offset:%3 sc1 lds" :: "v"(voff), "s"(srd), "s"(soff), "i"(OFF) : "memory");
}
#define DMA(voff, srd, soff, OFF) dma_o<OFF>((voff), (srd), (soff))
#define DMAS(voff, srd, soff, OFF) dma_s<OFF>((voff), (srd), (soff))

#define SETPRIO1() __builtin_amdgcn_s_setprio(0)
#define PIN4(a) asm volatile("" : "+v"(a))
#define PIN_ACC(acc) asm volatile("" : "+v"(acc[0][0]), "+v"(acc[0][1]), "+v"(acc[1][0]), "+v"(acc[1][1]), "+v"(acc[2][0]), "+v"(acc[2][1]), \
  "+v"(acc[3][0]), "+v"(acc[3][1]), "+v"(acc[4][0]), "+v"(acc[4][1]), "+v"(acc[5][0]), "+v"(acc[5][1]), "+v"(acc[6][0]), "+v"(acc[6][1]), \
  "+v"(acc[7][0]), "+v"(acc[7][1]), "+v"(acc[8][0]), "+v"(acc[8][1]))
#define PIN5(a) asm volatile("" : "+v"(a[0]), "+v"(a[1]), "+v"(a[2]), "+v"(a[3]), "+v"(a[4]))
#define MFMA32(a, b, c) __builtin_amdgcn_mfma_f32_32x32x16_bf16((a), (b), (c), 0, 0, 0)
#define MFMA(a, b, c) __builtin_amdgcn_mfma_f32_16x16x32_bf16((a), (b), (c), 0, 0, 0)

#define STAMP(k)
#define RSTAMP(k)

#define VB() BAR()
#define TK(v)
#define ACC(k, a, b)
#define TDUMP()
extern "C" __global__ __launch_bounds__(512, 2) void fused_kernel(const u16* __restrict__ x, const u16* __restrict__ wqkv, const u16* __restrict__ wo,
    const u16* __restrict__ w_in, const u16* __restrict__ w_sp, const u16* __restrict__ b_sp, const u16* __restrict__ w_out,
    u16* __restrict__ out, char* __restrict__ ws)
{
  extern __shared__ __attribute__((aligned(16))) char smem[];
  LAS char* lds = (LAS char*)smem;
  const int tid = threadIdx.x, lane = tid & 63;
  const int wave = __builtin_amdgcn_readfirstlane(tid >> 6);
  const int w = wave & 3;
  const int b = blockIdx.x, xcd = b & 7, rr = b >> 3;
  const int pi = 4*(xcd & 3) + (rr & 3), pj = 8*(xcd >> 2) + (rr >> 2);
  STAMP(0);
  RSTAMP(28);
  if (wave == 7) {
    unsigned long long pc; unsigned off, sz;
    asm volatile("s_getpc_b64 %0\n.Lpf_here:\n\ts_mov_b32 %1, .Lpf_here-fused_kernel\n\ts_mov_b32 %2, .Lpf_end-fused_kernel" : "=s"(pc), "=s"(off), "=s"(sz));
    pc -= off;
    const u32x4 s_code = u32x4{(unsigned)pc, (unsigned)(pc >> 32) & 0xffffu, sz, 0x00020000u};
    const int per = 2048;
    SETM0(149504); DMA(lane*16, s_code, rr*per, 0); DMA(lane*16, s_code, rr*per, 1024);
  }
  unsigned T;

  int cv[9];
  u32x4 csrd = make_srd(w_in, 576*9216);
  int p1dst = (w & 1)*9216, p1so = 32*(w & 1)*9216 + (FH + 144*pj)*2, p1step = 64*9216;
  int cq[9];
  auto p1_offsets = [&](auto QKV, int (&dst)[9]) {
    int l3 = lane; OPAQUE(l3);
    int r = l3 / 18, ch = l3 % 18;
    #pragma unroll
    for (int t = 0; t < 9; t++) {
      const int kp = kperm(r);
      dst[t] = (QKV.value ? kp*3456 + (ch < 15 ? (ch / 5)*1152 + (ch % 5)*16 : 0) : kp*9216 + ch*16) - (t & 3)*1024;
      r += 3; ch += 10; if (ch >= 18) { ch -= 18; r += 1; }
    }
  };
  auto p1_issue = [&]() {
    SETM0(p1dst); DMA(cv[0], csrd, p1so, 0); DMA(cv[1], csrd, p1so, 1024); DMA(cv[2], csrd, p1so, 2048); DMA(cv[3], csrd, p1so, 3072);
    SETM0(p1dst + 4096); DMA(cv[4], csrd, p1so, 0); DMA(cv[5], csrd, p1so, 1024); DMA(cv[6], csrd, p1so, 2048); DMA(cv[7], csrd, p1so, 3072);
    SETM0(p1dst + 8192); DMA(cv[8], csrd, p1so, 0);
    p1dst = (p1dst >= 4*P1_STAGE) ? p1dst - 4*P1_STAGE : p1dst + P1_STAGE; p1so += p1step;
  };
  const bool isD = (wave == 4 || wave == 5);

  if (wave >= 4) __builtin_amdgcn_s_setprio(3);
  const int mt = wave >> 2;
  bf16x8 xn[36];
  {
    const u32x4 s_x = make_srd(x + (size_t)(128*pi + 32*w)*D_, 32*1152);
    const int a = lane >> 2, bq = lane & 3;
    u32x4 raw[18];
    {
      const int vx = (16*mt + a)*1152 + bq*16;
      static_for<0, 18>([&](auto C) { u32x4 t; asm volatile("buffer_load_dwordx4 %0, %1, %2, 0 offen offset:%3" : "=v"(t) : "v"(vx), "s"(s_x), "i"(C.value*64) : "memory"); raw[C.value] = t; });
    }
    if (isD) { p1_offsets(IC<0>{}, cv); p1_issue(); }
    STAMP(23);
    if (isD) WAITV(9); else WAITV(0);
    asm volatile("" : "+v"(raw[0]), "+v"(raw[1]), "+v"(raw[2]), "+v"(raw[3]), "+v"(raw[4]), "+v"(raw[5]), "+v"(raw[6]), "+v"(raw[7]), "+v"(raw[8]),
                      "+v"(raw[9]), "+v"(raw[10]), "+v"(raw[11]), "+v"(raw[12]), "+v"(raw[13]), "+v"(raw[14]), "+v"(raw[15]), "+v"(raw[16]), "+v"(raw[17]));
    STAMP(20);
    float s1 = 0.f, s2 = 0.f, s3 = 0.f, s4 = 0.f;
    unsigned ones = 0x3f803f80u; OPAQUE(ones);
    #pragma unroll
    for (int c = 0; c < 18; c++) {
      s1 = dot2(raw[c][0], ones, s1); s2 = dot2(raw[c][0], raw[c][0], s2); s3 = dot2(raw[c][1], ones, s3); s4 = dot2(raw[c][1], raw[c][1], s4);
      s1 = dot2(raw[c][2], ones, s1); s2 = dot2(raw[c][2], raw[c][2], s2); s3 = dot2(raw[c][3], ones, s3); s4 = dot2(raw[c][3], raw[c][3], s4);
    }
    s1 += s3; s2 += s4;
    s1 += dpp_f<0xB1>(s1); s1 += dpp_f<0x4E>(s1);
    s2 += dpp_f<0xB1>(s2); s2 += dpp_f<0x4E>(s2);
    const float mean = s1*(1.f/576.f), var = fmaxf(s2*(1.f/576.f) - mean*mean, 0.f);
    const float rs = __builtin_amdgcn_rsqf(var + 1e-5f), sh = -mean*rs;
    const f32x2 rs2 = {rs, rs}, sh2 = {sh, sh};
    int xw = XCH_OFF + w*18432 + (bq >> 1)*1024 + (32*(bq & 1) + ((16*mt + a) ^ (2*bq)))*16;
    int xr0 = XCH_OFF + w*18432 + (32*(lane >> 5) + ((lane & 31) ^ (2*(lane >> 5))))*16;
    int xr1 = XCH_OFF + w*18432 + 1024 + (32*(lane >> 5) + ((lane & 31) ^ (4 + 2*(lane >> 5))))*16;
    OPAQUE(xw); OPAQUE(xr0); OPAQUE(xr1);
    u32x4 xo[18];
    #pragma unroll
    for (int c = 0; c < 18; c++)
      #pragma unroll
      for (int d = 0; d < 4; d++) xo[c][d] = pack2(__builtin_elementwise_fma(f32x2{bf_lo(raw[c][d]), bf_hi(raw[c][d])}, rs2, sh2));
    T = load_epoch(ws);
    STAMP(1);
    #pragma unroll
    for (int c = 0; c < 9; c++) *(LAS u32x4*)(lds + xw + c*2048) = xo[c];
    WAITL(0);
    STAMP(7);
    BAR();
    if (isD) p1_issue();
    if (wave < 4) {
      #pragma unroll
      for (int c = 0; c < 9; c++) { xn[2*c] = *(LAS bf16x8*)(lds + xr0 + c*2048); xn[2*c + 1] = *(LAS bf16x8*)(lds + xr1 + c*2048); }
      WAITL(0);
    }
    STAMP(9);
    BAR();
    #pragma unroll
    for (int c = 0; c < 9; c++) *(LAS u32x4*)(lds + xw + c*2048) = xo[9 + c];
    if (isD) WAITV(9);
    WAITL(0);
    BAR();
    if (isD) { p1_issue(); p1_issue(); p1_offsets(IC<1>{}, cq); }
    if (wave >= 6) {
      const u32x4 s_a = make_srd(w_in, 576*9216), s_b = make_srd(wqkv, 576*3456);
      SETM0(149504);
      const int vl = lane*16;
      #pragma unroll
      for (int k = 0; k < 11; k++) DMA(vl, s_a, b*20736 + ((w - 2)*11 + k)*1024, 0);
      #pragma unroll
      for (int k = 0; k < 4; k++) DMA(vl, s_b, b*7776 + ((w - 2)*4 + k)*1024, 0);
    }
    if (wave < 4) {
      #pragma unroll
      for (int c = 0; c < 9; c++) { xn[18 + 2*c] = *(LAS bf16x8*)(lds + xr0 + c*2048); xn[18 + 2*c + 1] = *(LAS bf16x8*)(lds + xr1 + c*2048); }
    }
    STAMP(2);
  }

  unsigned z1p[9][4];
  f32x4 acc[9][2];

  if (wave < 4) {
    constexpr int D = 3, NB = 4;
    int wl = (8*(lane >> 5) + 2*((lane >> 2) & 3))*288 + ((lane >> 4) & 1)*32 + (lane & 3)*8;
    int slot_off = 0;
    f32x16 a32[5];
    auto body = [&](auto NTC) {
      constexpr int NT = NTC.value, FS = 4*NT;
      bf16x8 wf[NB];
      int wc = wl + slot_off, wn;
      OPAQUE(wc);
      auto rdw = [&](auto G, int base) {
        constexpr int i = G.value % FS, k16 = i / NT, n = i % NT;
        LAS char* p = lds + base + (k16 >> 1)*9216 + (k16 & 1)*16*288 + n*64;
        s16x4 lo = __builtin_amdgcn_ds_read_tr16_b64_v4i16((LAS s16x4*)p);
        s16x4 hi = __builtin_amdgcn_ds_read_tr16_b64_v4i16((LAS s16x4*)(p + 288));
        wf[G.value % NB] = __builtin_shufflevector(lo, hi, 0, 1, 2, 3, 4, 5, 6, 7);
      };
      #pragma unroll
      for (int n = 0; n < NT; n++)
        #pragma unroll
        for (int e = 0; e < 16; e++) a32[n][e] = 0.f;
      static_for<0, D>([&](auto G) { rdw(G, wc); });
      SB();
            static_for<0, 9*FS>([&](auto G) {
        constexpr int st = G.value / FS, i = G.value % FS, k16 = i / NT, n = i % NT;
        if constexpr (i == 0) { slot_off = (slot_off >= 4*P1_STAGE) ? 0 : slot_off + P1_STAGE; wn = wl + slot_off; OPAQUE(wn); }
        if constexpr (i == FS - D) { SB(); PIN5(a32);
          BAR(); SB(); }
        a32[n] = MFMA32(wf[G.value % NB], xn[4*st + k16], a32[n]);
        if constexpr (G.value + D < 9*FS) { if constexpr (i + D < FS) rdw(IC<G.value + D>{}, wc); else rdw(IC<G.value + D>{}, wn); }
        SG(0x8, 1); SG(0x100, 2);
        SB();
        if constexpr (i == FS - 1) wc = wn;
      });
      PIN5(a32);
    };
    #pragma unroll 1
    for (int rep = 0; rep < 2; rep++) {
      asm volatile("" : "+s"(rep));
      body(IC<5>{});
      if (rep == 0) {
        {
      int l2 = lane; OPAQUE(l2);
      const int sr = l2 & 31, h4 = (l2 >> 5)*4;
      auto pk4 = [&](int n, int rq) { return u32x2{pack2(f32x2{a32[n][4*rq], a32[n][4*rq + 1]}), pack2(f32x2{a32[n][4*rq + 2], a32[n][4*rq + 3]})}; };
        f32x2 q1 = {0.f, 0.f}, q2 = {0.f, 0.f};
        LAS char* p = lds + ZB_OFF + w*IMG_W + sr*IMG_PITCH + h4*2;
        #pragma unroll
        for (int n = 0; n < 5; n++)
          #pragma unroll
          for (int rq = 0; rq < 4; rq++) if (n < 4 || rq < 2) {
            const f32x2 ya = gelu2(f32x2{a32[n][4*rq], a32[n][4*rq + 1]}), yb = gelu2(f32x2{a32[n][4*rq + 2], a32[n][4*rq + 3]});
            q1 += ya; q2 = __builtin_elementwise_fma(ya, ya, q2);
            q1 += yb; q2 = __builtin_elementwise_fma(yb, yb, q2);
            *(LAS u32x2*)(p + n*64 + rq*16) = u32x2{pack2(ya), pack2(yb)};
          }
        float t1 = q1[0] + q1[1], t2 = q2[0] + q2[1];
        t1 += __shfl_xor(t1, 32); t2 += __shfl_xor(t2, 32);
        const u32x4 rec = {__builtin_bit_cast(unsigned, t1), T, __builtin_bit_cast(unsigned, t2), T};
        if (l2 < 32) __builtin_amdgcn_raw_buffer_store_b128(rec, MKRSRC(ws + WS_STAT, 2048*256), ((pi*16 + pj)*128 + 32*w + sr)*16, 0, AUX_SC1);
        STAMP(3);
        WAITL(0);
        }
        body(IC<4>{});
        {
      int l2 = lane; OPAQUE(l2);
      const int sr = l2 & 31, h4 = (l2 >> 5)*4;
      auto pk4 = [&](int n, int rq) { return u32x2{pack2(f32x2{a32[n][4*rq], a32[n][4*rq + 1]}), pack2(f32x2{a32[n][4*rq + 2], a32[n][4*rq + 3]})}; };
        LAS char* p = lds + QB_OFF + (16 + 32*w + sr)*QB_PITCH + h4*2;
        #pragma unroll
        for (int n = 0; n < 4; n++)
          #pragma unroll
          for (int rq = 0; rq < 4; rq++) if (n < 3 || rq < 3 || h4 == 0) *(LAS u32x2*)(p + n*64 + rq*16) = pk4(n, rq);
        STAMP(4);
        WAITL(0);
        }
      }
    }
    {
      int l2 = lane; OPAQUE(l2);
      const int sr = l2 & 31, h4 = (l2 >> 5)*4;
      LAS char* p = lds + Z1B_OFF + w*IMG_W + sr*IMG_PITCH + h4*2;
      #pragma unroll
      for (int n = 0; n < 5; n++)
        #pragma unroll
        for (int rq = 0; rq < 4; rq++) if (n < 4 || rq < 2)
          *(LAS u32x2*)(p + n*64 + rq*16) = u32x2{pack2(f32x2{a32[n][4*rq], a32[n][4*rq + 1]}), pack2(f32x2{a32[n][4*rq + 2], a32[n][4*rq + 3]})};
      STAMP(5);
      WAITL(0);
    }
    BAR();
  } else {
    const int g = lane >> 4, fl = lane & 15;
    __builtin_amdgcn_s_setprio(3);
    if (isD) {
      WAITV(18); VB();
      #pragma unroll 1
      for (int q = 1; q < 6; q++) { p1_issue(); WAITV(18); VB(); }
      #pragma unroll
      for (int t = 0; t < 9; t++) { const int t_ = cv[t]; cv[t] = cq[t]; cq[t] = t_; }
      csrd = make_srd(wqkv, 576*3456); p1so = 32*(w & 1)*3456 + ((72*pj) & ~15); p1step = 64*3456;
      #pragma unroll 1
      for (int q = 6; q < 15; q++) { p1_issue(); WAITV(18); VB(); }
      #pragma unroll
      for (int t = 0; t < 9; t++) cv[t] = cq[t];
      csrd = make_srd(w_in, 576*9216); p1so = 32*(w & 1)*9216 + 144*pj*2; p1step = 64*9216;
      #pragma unroll 1
      for (int q = 15; q < 24; q++) {
        if (q == 19 && wave == 4 && pi < 15) {
          const RSRC r_halo = MKRSRC(ws + WS_HALO + (size_t)(pi*16 + pj)*4096, 4096);
          #pragma unroll
          for (int c = 0; c < 4; c++) { int ch = c*64 + lane; if (ch > 247) ch = 247;
            u32x4 v = *(LAS u32x4*)(lds + QB_OFF + 128*QB_PITCH + ch*16);
            __builtin_amdgcn_raw_buffer_store_b128(v, r_halo, ch*16, 0, AUX_SC1); }
        }
        p1_issue(); WAITV(18); VB();
        if (q == 20 && wave == 4 && lane == 0 && pi < 15) set_flag(ws, FL_HALO, pi, pj, T);
      }
      u32x4 hv[4]; unsigned hf = T;
      const bool hal = (wave == 5 && pi > 0);
      gu32* fp = (gu32*)(ws + WS_FLAGS + (size_t)(FL_HALO*16 + (pi > 0 ? pi - 1 : 0))*256) + pj;
      if (hal) asm volatile("global_load_dword %0, %1, %2 sc1" : "=v"(hf) : "v"(0), "s"(fp) : "memory");
      if (hal) WAITV(10); else WAITV(9);
      VB();
      if (hal) {
        asm volatile("s_waitcnt vmcnt(0)" : "+v"(hf) :: "memory");
        if (__builtin_amdgcn_readfirstlane(hf) != T)
          for (int spin = 0; spin < SPIN_MAX; ++spin) { if (__builtin_amdgcn_readfirstlane(__hip_atomic_load(fp, RLX_AGENT)) == T) break; __builtin_amdgcn_s_sleep(1); }
        const u32x4 s_hal = make_srd(ws + WS_HALO + (size_t)((pi - 1)*16 + pj)*4096, 4096);
        static_for<0, 4>([&](auto C) { int ch = C.value*64 + lane; if (ch > 247) ch = 247; u32x4 t;
          asm volatile("buffer_load_dwordx4 %0, %1, %2, 0 offen sc1" : "=v"(t) : "v"(ch*16), "s"(s_hal) : "memory"); hv[C.value] = t; });
      } else WAITV(0);
      VB();
      if (wave == 5) {
        if (pi > 0) asm volatile("s_waitcnt vmcnt(0)" : "+v"(hv[0]), "+v"(hv[1]), "+v"(hv[2]), "+v"(hv[3]) :: "memory");
        #pragma unroll
        for (int c = 0; c < 4; c++) { int ch = c*64 + lane; if (ch > 247) ch = 247; *(LAS u32x4*)(lds + QB_OFF + ch*16) = (pi > 0) ? hv[c] : u32x4{0, 0, 0, 0}; }
        WAITL(0);
      }
      VB();
    } else {
      const int gA = 2*(w - 2);
      {
        const u32x4 s_a = make_srd(w_sp, 2048*4096), s_b = make_srd(w_out, 2304*1152), s_c = make_srd(wo, 576*1152);
        SETM0(149504);
        const int vl = lane*16;
        #pragma unroll 1
        for (int q = 0; q < 4; q++) VB();
        #pragma unroll 1
        for (int q = 4; q < 16; q++) {
          const int k = q - 4;
          if (k < 8) { DMA(vl, s_a, b*32768 + ((w - 2)*16 + 2*k)*1024, 0); DMA(vl, s_a, b*32768 + ((w - 2)*16 + 2*k + 1)*1024, 0); }
          else if (k < 11) { DMA(vl, s_b, b*10368 + ((w - 2)*6 + 2*(k - 8))*1024, 0); DMA(vl, s_b, b*10368 + ((w - 2)*6 + 2*(k - 8) + 1)*1024, 0); }
          else { DMA(vl, s_c, b*2592 + ((w - 2)*2)*1024, 0); DMA(vl, s_c, b*2592 + ((w - 2)*2 + 1)*1024, 0); }
          VB();
        }
      }
      LAS f32x2* st = (LAS f32x2*)(lds + MISC_OFF + (w - 2)*512);
      {
        const u32x4 s_st = make_srd(ws + WS_STAT, 2048*256);
        const int vo = (pi*16*128 + 32*gA + lane)*16;
        u32x4 rec[16];
        static_for<0, 16>([&](auto C) { u32x4 t; asm volatile("buffer_load_dwordx4 %0, %1, %2, %3 offen sc1" : "=v"(t) : "v"(vo), "s"(s_st), "s"(C.value*2048) : "memory"); rec[C.value] = t; });
        WAITV(16);
        VB();
        VB();
        asm volatile("s_waitcnt vmcnt(0)" : "+v"(rec[0]), "+v"(rec[1]), "+v"(rec[2]), "+v"(rec[3]), "+v"(rec[4]), "+v"(rec[5]), "+v"(rec[6]), "+v"(rec[7]),
                          "+v"(rec[8]), "+v"(rec[9]), "+v"(rec[10]), "+v"(rec[11]), "+v"(rec[12]), "+v"(rec[13]), "+v"(rec[14]), "+v"(rec[15]) :: "memory");
        float t1 = 0.f, t2 = 0.f; bool ok = true;
        #pragma unroll
        for (int c = 0; c < 16; c++) { t1 += asf(rec[c][0]); t2 += asf(rec[c][2]); ok = ok && rec[c][1] == T && rec[c][3] == T; }
        if (!__all(ok)) {
          const RSRC r_stat = MKRSRC(ws + WS_STAT, 2048*256);
          for (int spin = 0; spin < SPIN_MAX; ++spin) {
            t1 = 0.f; t2 = 0.f; ok = true;
            #pragma unroll
            for (int c = 0; c < 16; c++) {
              u32x4 v = __builtin_amdgcn_raw_buffer_load_b128(r_stat, vo + c*2048, 0, AUX_SC1);
              t1 += asf(v[0]); t2 += asf(v[2]); ok = ok && v[1] == T && v[3] == T;
            }
            if (__all(ok)) break;
            __builtin_amdgcn_s_sleep(1);
          }
        }
        const float mean = t1*(1.f/2304.f), var = fmaxf(t2*(1.f/2304.f) - mean*mean, 0.f);
        const float rs = __builtin_amdgcn_rsqf(var + 1e-5f);
        st[lane] = f32x2{rs, -mean*rs};
        WAITL(0);
        VB();
      }
      const RSRC r_z2n = MKRSRC(ws + WS_Z2N, 16*64*9*1024);
      #pragma unroll 1
      for (int k = 0; k < 2; k++) {
        f32x2 r2[4], h2[4];
        #pragma unroll
        for (int d = 0; d < 4; d++) { f32x2 u0 = st[32*k + 8*g + 2*d], u1 = st[32*k + 8*g + 2*d + 1]; r2[d] = f32x2{u0[0], u1[0]}; h2[d] = f32x2{u0[1], u1[1]}; }
        LAS char* p = lds + ZB_OFF + (gA + k)*IMG_W + (8*g + ((lane >> 2) & 3))*IMG_PITCH + (lane & 3)*8;
        int zo = lane*16 + (pj*64 + 4*pi + gA + k)*9216;
        #pragma unroll 1
        for (int j3 = 0; j3 < 3; j3++, p += 96, zo += 3072) {
          u32x4 v[3];
          #pragma unroll
          for (int u = 0; u < 3; u++) {
            s16x4 lo = __builtin_amdgcn_ds_read_tr16_b64_v4i16((LAS s16x4*)(p + u*32));
            s16x4 hi = __builtin_amdgcn_ds_read_tr16_b64_v4i16((LAS s16x4*)(p + u*32 + 4*IMG_PITCH));
            v[u] = __builtin_bit_cast(u32x4, __builtin_shufflevector(lo, hi, 0, 1, 2, 3, 4, 5, 6, 7));
          }
          #pragma unroll
          for (int u = 0; u < 3; u++) {
            u32x4 o;
            #pragma unroll
            for (int d = 0; d < 4; d++) o[d] = pack2(f32x2{opq(__builtin_fmaf(bf_lo(v[u][d]), r2[d][0], h2[d][0])), opq(__builtin_fmaf(bf_hi(v[u][d]), r2[d][1], h2[d][1]))});
            __builtin_amdgcn_raw_buffer_store_b128(o, r_z2n, zo + u*1024, 0, AUX_SC1);
          }
          VB();
        }
      }
      VB();
      VB();
    }
    STAMP(5);
    BAR();
    if (!isD) { WAITV(0); if (lane == 0) set_flag(ws, FL_Z, pj, 2*pi + (w - 2), T); }
    {
      #pragma unroll
      for (int m = 0; m < 2; m++) {
        LAS char* zx = lds + Z1B_OFF + w*IMG_W + (8*(fl >> 2) + 4*m + (fl & 3))*IMG_PITCH + g*8;
        #pragma unroll
        for (int n = 0; n < 9; n++) { u32x2 v = *(LAS u32x2*)(zx + n*32); z1p[n][2*m] = v[0]; z1p[n][2*m + 1] = v[1]; }
      }
    }
  }

  const u32x4 s_z2n = make_srd(ws + WS_Z2N + (size_t)pj*64*9*1024, 64*9*1024);
  const u32x4 s_wsp = make_srd(w_sp + (size_t)128*pi*2048, 128*4096);
  int vw[4];
  const int vlin = lane*16;
  auto p2_issue = [&](int s, auto ZR) {
    const int base = (s & 3)*P2_STAGE;
    if constexpr (ZR.value) {
      static_for<0, 9>([&](auto K) { SETM0(base + w*9216 + K.value*1024); DMAS(vlin, s_z2n, ((2*s + w)*9 + K.value)*1024, 0); });
    } else {
      static_for<0, 8>([&](auto K) { constexpr int k = K.value; const int wr = 2*(w - 2) + (k >> 2);
        SETM0(base + 18432 + wr*4096 + (k & 3)*1024); DMA(vw[k & 3], s_wsp, wr*32*4096 + s*128, 0); });
    }
  };
  if (wave >= 4) {
    { int l2 = lane; OPAQUE(l2);
      const int a = l2 >> 3, bb = l2 & 7;
      #pragma unroll
      for (int m = 0; m < 2; m++)
        #pragma unroll
        for (int hf = 0; hf < 2; hf++) { int rho = 8*hf + a; vw[2*m + hf] = (8*(rho >> 2) + 4*m + (rho & 3))*4096 + ((bb ^ ((rho >> 1) & 7))*16); } }
    if (w >= 2) {
      #pragma unroll 1
      for (int s = 0; s < 2; s++) p2_issue(s, IC<0>{});
    }
  }
  {
      int t2 = tid; OPAQUE(t2);
      const int u = t2 & 1, hh = (t2 >> 7) & 1, tl = ((t2 >> 1) & 63) + 64*(t2 >> 8);
      LAS char* rowp = lds + QB_OFF + (16 + tl)*QB_PITCH + 8*(pj & 1) + 36*hh;
      const int o8 = hh*4, o4 = hh ? 0 : 32;
      u32x2 qv[4]; unsigned q0;
      #pragma unroll
      for (int d = 0; d < 4; d++) qv[d] = *(LAS u32x2*)(rowp + o8 + d*8);
      q0 = *(LAS unsigned*)(rowp + o4);
      float lg[4];
      #pragma unroll
      for (int ww = 0; ww < 4; ww++) {
        LAS char* kr = rowp - (2*(4*u + ww))*QB_PITCH + 80;
        float sa = dot2(q0, *(LAS unsigned*)(kr + o4), 0.f), sb = 0.f;
        #pragma unroll
        for (int d = 0; d < 4; d++) { u32x2 kv = *(LAS u32x2*)(kr + o8 + d*8); sa = dot2(qv[d][0], kv[0], sa); sb = dot2(qv[d][1], kv[1], sb); }
        float s = (sa + sb)*0.23570226039551584f;
        if (128*pi + tl - 2*(4*u + ww) < 0) s = -1e9f;
        lg[ww] = s;
      }
      float l8[8];
      #pragma unroll
      for (int ww = 0; ww < 4; ww++) { float o = dpp_f<0xB1>(lg[ww]); l8[ww] = u ? o : lg[ww]; l8[4 + ww] = u ? lg[ww] : o; }
      float mx = l8[0];
      #pragma unroll
      for (int ww = 1; ww < 8; ww++) mx = fmaxf(mx, l8[ww]);
      float sum = 0.f;
      #pragma unroll
      for (int ww = 0; ww < 8; ww++) { l8[ww] = __builtin_amdgcn_exp2f((l8[ww] - mx)*1.4426950408889634f); sum += l8[ww]; }
      const float inv = __builtin_amdgcn_rcpf(sum);
      float olo[5], ohi[5];
      #pragma unroll
      for (int d = 0; d < 5; d++) { olo[d] = 0.f; ohi[d] = 0.f; }
      #pragma unroll
      for (int ww = 0; ww < 8; ww++) {
        const unsigned pb = pack_bf16(l8[ww]*inv, 0.f);
        const unsigned ph = pb << 16;
        LAS char* vr = rowp - (2*ww)*QB_PITCH + 160;
        u32x2 va = *(LAS u32x2*)(vr + o8 + 16*u), vb = *(LAS u32x2*)(vr + o8 + 16*u + 8); unsigned vc = *(LAS unsigned*)(vr + o4);
        olo[0] = dot2(va[0], pb, olo[0]); ohi[0] = dot2(va[0], ph, ohi[0]); olo[1] = dot2(va[1], pb, olo[1]); ohi[1] = dot2(va[1], ph, ohi[1]);
        olo[2] = dot2(vb[0], pb, olo[2]); ohi[2] = dot2(vb[0], ph, ohi[2]); olo[3] = dot2(vb[1], pb, olo[3]); ohi[3] = dot2(vb[1], ph, ohi[3]);
        olo[4] = dot2(vc, pb, olo[4]); ohi[4] = dot2(vc, ph, ohi[4]);
      }
      *(LAS u32x2*)(rowp + o8 + 16*u) = u32x2{pack_bf16(olo[0], ohi[0]), pack_bf16(olo[1], ohi[1])};
      *(LAS u32x2*)(rowp + o8 + 16*u + 8) = u32x2{pack_bf16(olo[2], ohi[2]), pack_bf16(olo[3], ohi[3])};
      if (u) *(LAS unsigned*)(rowp + o4) = pack_bf16(olo[4], ohi[4]);
      }
  WAITL(0);
  STAMP(21);
  BAR();
  STAMP(22);
  if (wave >= 4 && w < 2) { wait_flags(ws, FL_Z, pj, 32, T, lane); STAMP(13); p2_issue(0, IC<1>{}); p2_issue(1, IC<1>{}); }
  if (wave >= 4) {
    #pragma unroll
    for (int n = 0; n < 3; n++) {
      #pragma unroll
      for (int d = 0; d < 4; d++) z1p[n][d] = pack2(gelu2(f32x2{bf_lo(z1p[n][d]), bf_hi(z1p[n][d])}));
      asm volatile("" : "+v"(z1p[n][0]), "+v"(z1p[n][1]), "+v"(z1p[n][2]), "+v"(z1p[n][3]));
    }
  }
  if (wave < 4) {
    const RSRC r_o = MKRSRC(ws + WS_O, 128*18*1024);
    #pragma unroll 1
    for (int e = tid; e < 1152; e += 256) {
      const int tl = e / 9, p = e % 9;
      u32x2 v = *(LAS u32x2*)(lds + QB_OFF + (16 + tl)*QB_PITCH + 8*(pj & 1) + 8*p);
      const int col = 36*pj + 4*p, r32 = tl & 31;
      const int tt = 8*pi + 2*(tl >> 5) + ((r32 >> 2) & 1), rho = 4*(r32 >> 3) + (r32 & 3);
      const int ks = col >> 5, c32 = col & 31;
      __builtin_amdgcn_raw_buffer_store_b64(v, r_o, ((tt*18 + ks)*64 + rho + 16*((c32 & 15) >> 2))*16 + (c32 >> 4)*8, 0, AUX_SC1);
    }
  }
  WAITL(0);
  BAR();
  STAMP(6);

  const int p3cnt = (xcd < 2) ? 30 : 29;
  const int p3T = xcd*29 + (xcd < 2 ? xcd : 2) + rr;
  const bool p3act = rr < p3cnt;
  const int rg = p3T / 9, pc = p3T % 9;

  if (wave >= 4) {
    __builtin_amdgcn_s_setprio(0);
    auto p2_helper = [&](auto ZR) {
      constexpr int CNT = ZR.value ? 9 : 8;
      p2_issue(2, ZR); p2_issue(3, ZR);
      WAITV(3*CNT);
      VB();
      auto piece = [&](int s, auto K) {
        constexpr int k = K.value; const int base = (s & 3)*P2_STAGE;
        if constexpr (ZR.value) { SETM0(base + w*9216 + k*1024); DMAS(vlin, s_z2n, ((2*s + w)*9 + k)*1024, 0); }
        else if constexpr (k < 8) { const int wr = 2*(w - 2) + (k >> 2); SETM0(base + 18432 + wr*4096 + (k & 3)*1024); DMA(vw[k & 3], s_wsp, wr*32*4096 + s*128, 0); }
      };
      auto step = [&](int s, auto PR) {
        WAITV(2*CNT);
        VB();
        static_for<0, 9>([&](auto K) {
          piece(s + 4, K);
          if constexpr (PR.value >= 0 && K.value == 2) {
            constexpr int pr = PR.value;
            unsigned rr2 = pack2(f32x2{opq(gelu_f(opq(bf_lo(z1p[pr / 4][pr % 4])))), opq(gelu_f(opq(bf_hi(z1p[pr / 4][pr % 4]))))});
            asm volatile("" : "+v"(rr2));
            z1p[pr / 4][pr % 4] = rr2;
          }
        });
      };
      static_for<0, 24>([&](auto S) {
        step(S.value, IC<12 + S.value>{});
        if constexpr (S.value == 4) { if (wave == 4 && lane == 0) set_flag(ws, FL_O, pi, pj, T); }
      });
      #pragma unroll 1
      for (int s = 24; s < 28; s++) step(s, IC<-1>{});
      WAITV(2*CNT); VB();
      WAITV(CNT); VB();
      #pragma unroll
      for (int n = 0; n < 9; n++) *(LAS u32x4*)(lds + w*9216 + n*1024 + lane*16) = u32x4{z1p[n][0], z1p[n][1], z1p[n][2], z1p[n][3]};
      WAITV(0); WAITL(0); VB();
      VB();
    };
    if (w < 2) p2_helper(IC<1>{}); else p2_helper(IC<0>{});
    STAMP(8);
    if (p3act) {
      const u32x4 s_o = make_srd(ws + WS_O, 128*18*1024), s_h = make_srd(ws + WS_H, 128*72*1024);
      const u32x4 s_wo = make_srd(wo, 576*1152), s_wout = make_srd(w_out, 2304*1152);
      int vW0, vW1, vA[5];
      int l2 = lane; OPAQUE(l2);
      { const int r = l2 >> 3;
        vW0 = r*1152 + (((l2 & 7) ^ (((r >> 1) & 3) << 1))*16);
        vW1 = (r + 8)*1152 + (((l2 & 7) ^ ((((r + 8) >> 1) & 3) << 1))*16);
        #pragma unroll
        for (int m5 = 0; m5 < 5; m5++) { int tt = 5*rg + m5; if (tt > 127) tt = 127; vA[m5] = l2*16 + tt*18*1024; } }
      int slot = 2;
      auto p3_ks = [&](u32x4 sw, u32x4 sa, int R0, int R1, int ks) {
        if (w < 3) {
          const int base = slot*P3_STAGE + w*9216, s0 = R0*1152 + pc*128, s1 = R1*1152 + pc*128;
          SETM0(base + 5120); DMA(vW0, sw, s0, 0); SETM0(base + 6144); DMA(vW1, sw, s0, 0);
          SETM0(base + 7168); DMA(vW0, sw, s1, 0); SETM0(base + 8192); DMA(vW1, sw, s1, 0);
          static_for<0, 5>([&](auto M) { SETM0(base + M.value*1024); DMAS(vA[M.value], sa, ks*1024, 0); });
        }
        slot = (slot == 4) ? 0 : slot + 1;
      };
      auto p3_wo = [&](int u) { const int ks = 3*u + w; p3_ks(s_wo, s_o, 32*ks, 32*ks + 16, ks); };
      auto p3_g3 = [&](int v) { const int ks = 3*v + w;
        const int R0 = (ks < 64) ? 144*(ks >> 2) + 32*(ks & 3) : 288*(ks - 64) + 128;
        p3_ks(s_wout, s_h, R0, R0 + ((ks < 64) ? 16 : 144), ks); };
      const int p0 = (5*rg) >> 3, p1 = ((5*rg + 4 > 127 ? 127 : 5*rg + 4)) >> 3;
      if (w < 3) { wait_flags(ws, FL_O, p0, 16, T, lane); wait_flags(ws, FL_O, p1, 16, T, lane); }
      STAMP(24);
      #pragma unroll 1
      for (int u = 0; u < 3; u++) p3_wo(u);
      BAR();
      p3_wo(3); p3_wo(4);
      WAITV(27);
      STAMP(26);
      BAR();
      WAITV(18); BAR();
      p3_wo(5); WAITV(18); BAR();
      WAITV(9); BAR();
      WAITV(0); BAR();
      BAR();
      #pragma unroll
      for (int m5 = 0; m5 < 5; m5++) { int tt = 5*rg + m5; if (tt > 127) tt = 127; vA[m5] = l2*16 + tt*72*1024; }
      if (w < 3) { wait_flags(ws, FL_H, p0, 16, T, lane); wait_flags(ws, FL_H, p1, 16, T, lane); }
      STAMP(27);
      RSTAMP(31);
      #pragma unroll 1
      for (int v = 0; v < 4; v++) p3_g3(v);
      WAITV(18); BAR();
      #pragma unroll 1
      for (int u = 6; u < 26; u++) {
        p3_g3(u - 2);
        WAITV(18); BAR();
      }
      WAITV(9); BAR();
      WAITV(0); BAR();
      BAR();
    }
  } else {
    int l2 = lane; OPAQUE(l2);
    const int g = l2 >> 4, fl = l2 & 15;
    const int zoff = l2*16;
    const int woff0 = 18432 + (4*w + (fl >> 3))*1024 + (fl & 7)*128 + ((g ^ ((fl >> 1) & 7))*16);
    float bs[2];
    #pragma unroll
    for (int m = 0; m < 2; m++) bs[m] = bf_lo(b_sp[128*pi + 32*w + 8*(fl >> 2) + 4*m + (fl & 3)]);
    #pragma unroll
    for (int n = 0; n < 9; n++) { acc[n][0] = f32x4{bs[0], bs[0], bs[0], bs[0]}; acc[n][1] = f32x4{bs[1], bs[1], bs[1], bs[1]}; }
    bf16x8 z0[9], w0[2], z1[9], w1[2];
    auto rd = [&](bf16x8 (&z)[9], bf16x8 (&wv)[2], int s, int kk) {
      const int base = (s & 3)*P2_STAGE;
      LAS char* pw = lds + base + (woff0 ^ (kk*64));
      LAS char* pz = lds + base + kk*9216 + zoff;
      wv[0] = *(LAS bf16x8*)pw; wv[1] = *(LAS bf16x8*)(pw + 2048);
      #pragma unroll
      for (int n = 0; n < 9; n++) z[n] = *(LAS bf16x8*)(pz + n*1024);
    };
    auto mm = [&](bf16x8 (&z)[9], bf16x8 (&wv)[2]) {
      #pragma unroll
      for (int n = 0; n < 9; n++) { acc[n][0] = MFMA(z[n], wv[0], acc[n][0]); acc[n][1] = MFMA(z[n], wv[1], acc[n][1]); }
      #pragma unroll
      for (int q = 0; q < 11; q++) { SG(0x8, 1); SG(0x100, 1); }
      SG(0x8, 7);
    };
    BAR();
    rd(z0, w0, 0, 0);
    CWAITL0(); SB();
    #pragma unroll 1
    for (int s = 0; s < 32; s++) {
      CWAITL0(); SB();
      rd(z1, w1, s, 1);
      mm(z0, w0);
      SB(); CWAITL0(); PIN_ACC(acc);
      if (s == 4) WAITV(0);
      BAR(); SB();
      rd(z0, w0, (s + 1 < 32 ? s + 1 : s), 0);
      mm(z1, w1);
      SB();
    }
    STAMP(8);
    {
      const RSRC r_h = MKRSRC(ws + WS_H, 128*72*1024);
      u32x4 z1g[9];
      #pragma unroll
      for (int n = 0; n < 9; n++) z1g[n] = *(LAS u32x4*)(lds + w*9216 + n*1024 + zoff);
      #pragma unroll
      for (int m = 0; m < 2; m++) {
        const int tt = 8*pi + 2*w + m;
        unsigned hp[9][2];
        #pragma unroll
        for (int n = 0; n < 9; n++) {
          hp[n][0] = pack2(f32x2{bf_lo(z1g[n][2*m]), bf_hi(z1g[n][2*m])}*f32x2{acc[n][m][0], acc[n][m][1]});
          hp[n][1] = pack2(f32x2{bf_lo(z1g[n][2*m + 1]), bf_hi(z1g[n][2*m + 1])}*f32x2{acc[n][m][2], acc[n][m][3]});
        }
        #pragma unroll
        for (int q = 0; q < 4; q++)
          __builtin_amdgcn_raw_buffer_store_b128(u32x4{hp[2*q][0], hp[2*q][1], hp[2*q + 1][0], hp[2*q + 1][1]}, r_h, zoff + (tt*72 + 4*pj + q)*1024, 0, AUX_SC1);
        __builtin_amdgcn_raw_buffer_store_b64(u32x2{hp[8][0], hp[8][1]}, r_h, zoff + (pj & 1)*8 + (tt*72 + 64 + (pj >> 1))*1024, 0, AUX_SC1);
      }
    }
    STAMP(19);
    RSTAMP(29);
    if (p3act) {
      WAITL(0);
      BAR();
      f32x4 aS[5], aC[5];
      u32x2 xv[5];
      const int q4 = (l2 >> 2) & 3, p4 = l2 & 3, r0 = 4*g + q4;
      const int woff = 5120 + r0*128 + (((2*w + (p4 >> 1)) ^ (((r0 >> 1) & 3) << 1))*16) + (p4 & 1)*8;
      bf16x8 hA[3][5], wA[3], hB[3][5], wB[3];
      int slot = 2;
      auto rd3 = [&](bf16x8 (&h)[3][5], bf16x8 (&wv)[3]) {
        LAS char* p = lds + slot*P3_STAGE;
        #pragma unroll
        for (int kk = 0; kk < 3; kk++) {
          s16x4 lo = __builtin_amdgcn_ds_read_tr16_b64_v4i16((LAS s16x4*)(p + kk*9216 + woff));
          s16x4 hi = __builtin_amdgcn_ds_read_tr16_b64_v4i16((LAS s16x4*)(p + kk*9216 + woff + 2048));
          wv[kk] = __builtin_shufflevector(lo, hi, 0, 1, 2, 3, 4, 5, 6, 7);
          #pragma unroll
          for (int m5 = 0; m5 < 5; m5++) h[kk][m5] = *(LAS bf16x8*)(p + kk*9216 + m5*1024 + zoff);
        }
        slot = (slot == 4) ? 0 : slot + 1;
      };
      auto mm3 = [&](bf16x8 (&h)[3][5], bf16x8 (&wv)[3]) {
        #pragma unroll
        for (int kk = 0; kk < 3; kk++)
          #pragma unroll
          for (int m5 = 0; m5 < 5; m5++) aC[m5] = MFMA(wv[kk], h[kk][m5], aC[m5]);
        #pragma unroll
        for (int q = 0; q < 15; q++) { SG(0x8, 1); SG(0x100, 1); }
        SG(0x100, 6);
      };
      #pragma unroll
      for (int m5 = 0; m5 < 5; m5++) {
        int tt = 5*rg + m5; if (tt > 127) tt = 127;
        const int row = 32*(tt >> 1) + 8*(fl >> 2) + 4*(tt & 1) + (fl & 3);
        xv[m5] = *(const u32x2*)(x + (size_t)row*D_ + 64*pc + 16*w + 4*g);
      }
      BAR();
      #pragma unroll 1
      for (int ph = 0; ph < 2; ph++) {
        #pragma unroll
        for (int m5 = 0; m5 < 5; m5++) aC[m5] = f32x4{0,0,0,0};
        rd3(hA, wA); SB();
        const int nit = ph ? 12 : 3;
        #pragma unroll 1
        for (int it = 0; it < nit; it++) {
          CWAITL0(); SB(); rd3(hB, wB); mm3(hA, wA); SB(); PIN5(aC); BAR(); SB();
          CWAITL0(); SB(); rd3(hA, wA); mm3(hB, wB); SB(); PIN5(aC);
          if (ph == 0 && it == 1) { WAITV(0); RSTAMP(30); }
          if (ph == 0 || it < 11) BAR();
          SB();
          if (ph == 0 && it == 1 && tid == 0) set_flag(ws, FL_H, pi, pj, T);
        }
        if (ph == 0) {
          #pragma unroll
          for (int m5 = 0; m5 < 5; m5++) aS[m5] = aC[m5];
          slot = 3;
          STAMP(10);
        }
      }
      STAMP(11);
      #pragma unroll
      for (int m5 = 0; m5 < 5; m5++) {
        const int tt = 5*rg + m5;
        if (tt < 128) {
          const int row = 32*(tt >> 1) + 8*(fl >> 2) + 4*(tt & 1) + (fl & 3);
          const size_t o = (size_t)row*D_ + 64*pc + 16*w + 4*g;
          float r[4];
          #pragma unroll
          for (int e = 0; e < 4; e++) {
            float bv = aC[m5][e], sv = aS[m5][e];
            float sl = bv*__builtin_amdgcn_rcpf(1.f + __builtin_amdgcn_exp2f(-1.4426950408889634f*bv));
            unsigned xw = xv[m5][e >> 1];
            float xx = (e & 1) ? bf_hi(xw) : bf_lo(xw);
            r[e] = __builtin_fmaf(sv, sl, xx);
          }
          *(u32x2*)(out + o) = u32x2{pack_bf16(r[0], r[1]), pack_bf16(r[2], r[3])};
        }
      }
    } else {
      WAITV(0);
      BAR();
      if (tid == 0) set_flag(ws, FL_H, pi, pj, T);
    }
    if (b == 0 && tid == 0) __hip_atomic_store((gu32*)ws, T, RLX_AGENT);
    STAMP(12);
  }
  TDUMP();
  asm volatile(".Lpf_end:");
}

extern "C" void kernel_launch(void* const* d_in, const int* in_sizes, int n_in, void* d_out, int out_size, void* d_ws, size_t ws_size, hipStream_t stream) {
  (void)hipFuncSetAttribute((const void*)fused_kernel, hipFuncAttributeMaxDynamicSharedMemorySize, LDS_BYTES);
  fused_kernel<<<256, 512, LDS_BYTES, stream>>>((const u16*)d_in[0], (const u16*)d_in[1], (const u16*)d_in[2], (const u16*)d_in[4],
      (const u16*)d_in[5], (const u16*)d_in[6], (const u16*)d_in[7], (u16*)d_out, (char*)d_ws);
}
```

```cpp
#include <hip/hip_runtime.h>
#include <stdint.h>

typedef unsigned short u16;
typedef __attribute__((ext_vector_type(8))) short bf16x8;
typedef __attribute__((ext_vector_type(4))) short s16x4;
typedef __attribute__((ext_vector_type(4))) float f32x4;
typedef __attribute__((ext_vector_type(16))) float f32x16;
typedef __attribute__((ext_vector_type(2))) float f32x2;
typedef __attribute__((ext_vector_type(4))) unsigned u32x4;
typedef __attribute__((ext_vector_type(2))) unsigned u32x2;
typedef __attribute__((ext_vector_type(2))) __bf16 bf16x2;
#define GAS __attribute__((address_space(1)))
#define LAS __attribute__((address_space(3)))
typedef GAS unsigned gu32;
#define RLX_AGENT __ATOMIC_RELAXED, __HIP_MEMORY_SCOPE_AGENT
#define BAR() asm volatile("s_barrier" ::: "memory")
#define SB() __builtin_amdgcn_sched_barrier(0)
#define SG(mask, n) __builtin_amdgcn_sched_group_barrier(mask, n, 0)
#define WAITV(n) asm volatile("s_waitcnt vmcnt(%0)" :: "i"(n) : "memory")
#define WAITL(n) asm volatile("s_waitcnt lgkmcnt(%0)" :: "i"(n) : "memory")
#define CWAITL0() __builtin_amdgcn_s_waitcnt(0xC07F)
#define RSRC __amdgpu_buffer_rsrc_t
#define MKRSRC(p, bytes) __builtin_amdgcn_make_buffer_rsrc((void*)(p), 0, (bytes), 0x00020000)
#define AUX_SC1 16
#define OPAQUE(x) asm volatile("" : "+v"(x))

constexpr int D_ = 576, FH = 2304;
constexpr size_t WS_FLAGS = 4096;
constexpr size_t WS_STAT  = 32768;
constexpr size_t WS_HALO  = WS_STAT + 2048*256;
constexpr int ZJS = 64*9*1024 + 256, HTS = 72*1024 + 256, OTS = 18*1024 + 256;
constexpr size_t WS_Z2N   = WS_HALO + 16*16*4096;
constexpr size_t WS_H     = WS_Z2N + (size_t)16*ZJS;
constexpr size_t WS_O     = WS_H + (size_t)128*HTS;
constexpr size_t WS_DBG   = WS_O + (size_t)128*OTS;
constexpr int FL_STAT = 0, FL_Z = 1, FL_HALO = 2, FL_O = 3, FL_H = 4;
constexpr int LDS_BYTES = 163840;
constexpr int P1_STAGE = 18432, P1_NS = 5;
constexpr int XCH_OFF = 4*P1_STAGE;
constexpr int ZB_OFF = P1_NS*P1_STAGE;
constexpr int QB_PITCH = 248;
constexpr int IMG_PITCH = 296, IMG_W = 32*IMG_PITCH;
constexpr int QB_OFF = ZB_OFF + 4*IMG_W - 16*QB_PITCH;
constexpr int MISC_OFF = QB_OFF + 144*QB_PITCH;
constexpr int Z1B_OFF = 4*P1_STAGE;
constexpr int P2_STAGE = 34816;
constexpr int P3_STAGE = 27648;
constexpr int SPIN_MAX = 1 << 18;

template<int N> struct IC { static constexpr int value = N; };
template<int B, int E, class F> __device__ __forceinline__ void static_for(F&& f) {
  if constexpr (B < E) { f(IC<B>{}); static_for<B + 1, E>(f); }
}

__device__ __forceinline__ unsigned pack2(f32x2 v) { return __builtin_bit_cast(unsigned, __builtin_convertvector(v, bf16x2)); }
__device__ __forceinline__ float opq(float x) { asm("" : "+v"(x)); return x; }
__device__ __forceinline__ unsigned pack_bf16(float a, float b) { return pack2(f32x2{a, b}); }
__device__ __forceinline__ float asf(unsigned u) { return __builtin_bit_cast(float, u); }
__device__ __forceinline__ float bf_lo(unsigned u) { return __builtin_bit_cast(float, u << 16); }
__device__ __forceinline__ float bf_hi(unsigned u) { return __builtin_bit_cast(float, u & 0xffff0000u); }
__device__ __forceinline__ float dot2(unsigned a, unsigned b, float c) {
  return __builtin_amdgcn_fdot2_f32_bf16(__builtin_bit_cast(bf16x2, a), __builtin_bit_cast(bf16x2, b), c, false);
}
__device__ __forceinline__ float gelu_f(float x) {
  const float c1 = -2.302208198f, c2 = -0.1029432397f;
  float t = __builtin_fmaf(x * x, c2, c1);
  float e = __builtin_amdgcn_exp2f(x * t);
  return x * __builtin_amdgcn_rcpf(1.f + e);
}
__device__ __forceinline__ f32x2 gelu2(f32x2 x) {
  const f32x2 c1 = {-2.302208198f, -2.302208198f}, c2 = {-0.1029432397f, -0.1029432397f}, one = {1.f, 1.f};
  f32x2 t = __builtin_elementwise_fma(x * x, c2, c1);
  f32x2 a = x * t;
  f32x2 e = {__builtin_amdgcn_exp2f(a[0]), __builtin_amdgcn_exp2f(a[1])};
  f32x2 d = e + one;
  f32x2 r = {__builtin_amdgcn_rcpf(d[0]), __builtin_amdgcn_rcpf(d[1])};
  return x * r;
}
template<int CTRL> __device__ __forceinline__ float dpp_f(float v) {
  return __builtin_bit_cast(float, __builtin_amdgcn_update_dpp(0, __builtin_bit_cast(int, v), CTRL, 0xf, 0xf, true));
}
__device__ __forceinline__ float row16_allsum(float v) {
  v += dpp_f<0x128>(v); v += dpp_f<0x124>(v); v += dpp_f<0x122>(v); v += dpp_f<0x121>(v); return v;
}
__device__ __forceinline__ bool poll_flags(const char* ws, int group, int idx, int nflags, unsigned T, int lane) {
  gu32* p = (gu32*)(ws + WS_FLAGS + (size_t)(group*16 + idx)*256) + (lane & (nflags - 1));
  unsigned v = __hip_atomic_load(p, RLX_AGENT);
  return __all(v == T);
}
__device__ __forceinline__ void wait_flags(const char* ws, int group, int idx, int nflags, unsigned T, int lane) {
  for (int spin = 0; spin < SPIN_MAX; ++spin) {
    if (poll_flags(ws, group, idx, nflags, T, lane)) break;
    __builtin_amdgcn_s_sleep(1);
  }
}
__device__ __forceinline__ void set_flag(char* ws, int group, int idx, int k, unsigned T) {
  __hip_atomic_store((gu32*)(ws + WS_FLAGS + (size_t)(group*16 + idx)*256) + k, T, RLX_AGENT);
}
__device__ __forceinline__ unsigned load_epoch(const char* ws) {
  unsigned e;
  asm volatile("s_load_dword %0, %1, 0x0\n\ts_waitcnt lgkmcnt(0)" : "=s"(e) : "s"(ws) : "memory");
  return e + 1u;
}
__device__ __forceinline__ int kperm(int r) { return 8*(r >> 3) + 4*(r & 1) + ((r >> 1) & 3); }
__device__ __forceinline__ u32x4 make_srd(const void* p, unsigned bytes) {
  unsigned long long a = (unsigned long long)p;
  return u32x4{(unsigned)a, (unsigned)(a >> 32) & 0xffffu, bytes, 0x00020000u};
}
__device__ __forceinline__ void SETM0(int v) { asm volatile("s_mov_b32 m0, %0\n\ts_nop 0" :: "s"(v) : "memory"); }
template<int OFF> __device__ __forceinline__ void dma_o(int voff, u32x4 srd, int soff) {
  asm volatile("buffer_load_dwordx4 %0, %1, %2 offen offset:%3 lds" :: "v"(voff), "s"(srd), "s"(soff), "i"(OFF) : "memory");
}
template<int OFF> __device__ __forceinline__ void dma_s(int voff, u32x4 srd, int soff) {
  asm volatile("buffer_load_dwordx4 %0, %1, %2 offen offset:%3 sc1 lds" :: "v"(voff), "s"(srd), "s"(soff), "i"(OFF) : "memory");
}
#define DMA(voff, srd, soff, OFF) dma_o<OFF>((voff), (srd), (soff))
#define DMAS(voff, srd, soff, OFF) dma_s<OFF>((voff), (srd), (soff))

#define SETPRIO1() __builtin_amdgcn_s_setprio(0)
#define PIN4(a) asm volatile("" : "+v"(a))
#define PIN_ACC(acc) asm volatile("" : "+v"(acc[0][0]), "+v"(acc[0][1]), "+v"(acc[1][0]), "+v"(acc[1][1]), "+v"(acc[2][0]), "+v"(acc[2][1]), \
  "+v"(acc[3][0]), "+v"(acc[3][1]), "+v"(acc[4][0]), "+v"(acc[4][1]), "+v"(acc[5][0]), "+v"(acc[5][1]), "+v"(acc[6][0]), "+v"(acc[6][1]), \
  "+v"(acc[7][0]), "+v"(acc[7][1]), "+v"(acc[8][0]), "+v"(acc[8][1]))
#define PIN5(a) asm volatile("" : "+v"(a[0]), "+v"(a[1]), "+v"(a[2]), "+v"(a[3]), "+v"(a[4]))
#define MFMA32(a, b, c) __builtin_amdgcn_mfma_f32_32x32x16_bf16((a), (b), (c), 0, 0, 0)
#define MFMA(a, b, c) __builtin_amdgcn_mfma_f32_16x16x32_bf16((a), (b), (c), 0, 0, 0)

#define STAMP(k)
#define RSTAMP(k)

#define VB() BAR()
#define TK(v)
#define ACC(k, a, b)
#define TDUMP()
extern "C" __global__ __launch_bounds__(512, 2) void fused_kernel(const u16* __restrict__ x, const u16* __restrict__ wqkv, const u16* __restrict__ wo,
    const u16* __restrict__ w_in, const u16* __restrict__ w_sp, const u16* __restrict__ b_sp, const u16* __restrict__ w_out,
    u16* __restrict__ out, char* __restrict__ ws)
{
  extern __shared__ __attribute__((aligned(16))) char smem[];
  LAS char* lds = (LAS char*)smem;
  const int tid = threadIdx.x, lane = tid & 63;
  const int wave = __builtin_amdgcn_readfirstlane(tid >> 6);
  const int w = wave & 3;
  const int b = blockIdx.x, xcd = b & 7, rr = b >> 3;
  const int pi = 4*(xcd & 3) + (rr & 3), pj = 8*(xcd >> 2) + (rr >> 2);
  STAMP(0);
  RSTAMP(28);
  if (wave == 7) {
    unsigned long long pc; unsigned off, sz;
    asm volatile("s_getpc_b64 %0\n.Lpf_here:\n\ts_mov_b32 %1, .Lpf_here-fused_kernel\n\ts_mov_b32 %2, .Lpf_end-fused_kernel" : "=s"(pc), "=s"(off), "=s"(sz));
    pc -= off;
    const u32x4 s_code = u32x4{(unsigned)pc, (unsigned)(pc >> 32) & 0xffffu, sz, 0x00020000u};
    const int per = 2048;
    SETM0(149504); DMA(lane*16, s_code, rr*per, 0); DMA(lane*16, s_code, rr*per, 1024);
  }
  unsigned T;

  int cv[9];
  u32x4 csrd = make_srd(w_in, 576*9216);
  int p1dst = (w & 1)*9216, p1so = 32*(w & 1)*9216 + (FH + 144*pj)*2, p1step = 64*9216;
  int cq[9];
  auto p1_offsets = [&](auto QKV, int (&dst)[9]) {
    int l3 = lane; OPAQUE(l3);
    int r = l3 / 18, ch = l3 % 18;
    #pragma unroll
    for (int t = 0; t < 9; t++) {
      const int kp = kperm(r);
      dst[t] = (QKV.value ? kp*3456 + (ch < 15 ? (ch / 5)*1152 + (ch % 5)*16 : 0) : kp*9216 + ch*16) - (t & 3)*1024;
      r += 3; ch += 10; if (ch >= 18) { ch -= 18; r += 1; }
    }
  };
  auto p1_issue = [&]() {
    SETM0(p1dst); DMA(cv[0], csrd, p1so, 0); DMA(cv[1], csrd, p1so, 1024); DMA(cv[2], csrd, p1so, 2048); DMA(cv[3], csrd, p1so, 3072);
    SETM0(p1dst + 4096); DMA(cv[4], csrd, p1so, 0); DMA(cv[5], csrd, p1so, 1024); DMA(cv[6], csrd, p1so, 2048); DMA(cv[7], csrd, p1so, 3072);
    SETM0(p1dst + 8192); DMA(cv[8], csrd, p1so, 0);
    p1dst = (p1dst >= 4*P1_STAGE) ? p1dst - 4*P1_STAGE : p1dst + P1_STAGE; p1so += p1step;
  };
  const bool isD = (wave == 4 || wave == 5);

  if (wave >= 4) __builtin_amdgcn_s_setprio(3);
  const int mt = wave >> 2;
  bf16x8 xn[36];
  {
    const u32x4 s_x = make_srd(x + (size_t)(128*pi + 32*w)*D_, 32*1152);
    const int a = lane >> 2, bq = lane & 3;
    u32x4 raw[18];
    {
      const int vx = (16*mt + a)*1152 + bq*16;
      static_for<0, 18>([&](auto C) { u32x4 t; asm volatile("buffer_load_dwordx4 %0, %1, %2, 0 offen offset:%3" : "=v"(t) : "v"(vx), "s"(s_x), "i"(C.value*64) : "memory"); raw[C.value] = t; });
    }
    if (isD) { p1_offsets(IC<0>{}, cv); p1_issue(); }
    STAMP(23);
    if (isD) WAITV(9); else WAITV(0);
    asm volatile("" : "+v"(raw[0]), "+v"(raw[1]), "+v"(raw[2]), "+v"(raw[3]), "+v"(raw[4]), "+v"(raw[5]), "+v"(raw[6]), "+v"(raw[7]), "+v"(raw[8]),
                      "+v"(raw[9]), "+v"(raw[10]), "+v"(raw[11]), "+v"(raw[12]), "+v"(raw[13]), "+v"(raw[14]), "+v"(raw[15]), "+v"(raw[16]), "+v"(raw[17]));
    STAMP(20);
    float s1 = 0.f, s2 = 0.f, s3 = 0.f, s4 = 0.f;
    unsigned ones = 0x3f803f80u; OPAQUE(ones);
    #pragma unroll
    for (int c = 0; c < 18; c++) {
      s1 = dot2(raw[c][0], ones, s1); s2 = dot2(raw[c][0], raw[c][0], s2); s3 = dot2(raw[c][1], ones, s3); s4 = dot2(raw[c][1], raw[c][1], s4);
      s1 = dot2(raw[c][2], ones, s1); s2 = dot2(raw[c][2], raw[c][2], s2); s3 = dot2(raw[c][3], ones, s3); s4 = dot2(raw[c][3], raw[c][3], s4);
    }
    s1 += s3; s2 += s4;
    s1 += dpp_f<0xB1>(s1); s1 += dpp_f<0x4E>(s1);
    s2 += dpp_f<0xB1>(s2); s2 += dpp_f<0x4E>(s2);
    const float mean = s1*(1.f/576.f), var = fmaxf(s2*(1.f/576.f) - mean*mean, 0.f);
    const float rs = __builtin_amdgcn_rsqf(var + 1e-5f), sh = -mean*rs;
    const f32x2 rs2 = {rs, rs}, sh2 = {sh, sh};
    int xw = XCH_OFF + w*18432 + (bq >> 1)*1024 + (32*(bq & 1) + ((16*mt + a) ^ (2*bq)))*16;
    int xr0 = XCH_OFF + w*18432 + (32*(lane >> 5) + ((lane & 31) ^ (2*(lane >> 5))))*16;
    int xr1 = XCH_OFF + w*18432 + 1024 + (32*(lane >> 5) + ((lane & 31) ^ (4 + 2*(lane >> 5))))*16;
    OPAQUE(xw); OPAQUE(xr0); OPAQUE(xr1);
    u32x4 xo[18];
    #pragma unroll
    for (int c = 0; c < 18; c++)
      #pragma unroll
      for (int d = 0; d < 4; d++) xo[c][d] = pack2(__builtin_elementwise_fma(f32x2{bf_lo(raw[c][d]), bf_hi(raw[c][d])}, rs2, sh2));
    T = load_epoch(ws);
    STAMP(1);
    #pragma unroll
    for (int c = 0; c < 9; c++) *(LAS u32x4*)(lds + xw + c*2048) = xo[c];
    WAITL(0);
    STAMP(7);
    BAR();
    if (isD) p1_issue();
    if (wave < 4) {
      #pragma unroll
      for (int c = 0; c < 9; c++) { xn[2*c] = *(LAS bf16x8*)(lds + xr0 + c*2048); xn[2*c + 1] = *(LAS bf16x8*)(lds + xr1 + c*2048); }
      WAITL(0);
    }
    STAMP(9);
    BAR();
    #pragma unroll
    for (int c = 0; c < 9; c++) *(LAS u32x4*)(lds + xw + c*2048) = xo[9 + c];
    if (isD) WAITV(9);
    WAITL(0);
    BAR();
    if (isD) { p1_issue(); p1_issue(); p1_offsets(IC<1>{}, cq); }
    if (wave >= 6) {
      const u32x4 s_a = make_srd(w_in, 576*9216), s_b = make_srd(wqkv, 576*3456);
      SETM0(149504);
      const int vl = lane*16;
      #pragma unroll
      for (int k = 0; k < 11; k++) DMA(vl, s_a, b*20736 + ((w - 2)*11 + k)*1024, 0);
      #pragma unroll
      for (int k = 0; k < 4; k++) DMA(vl, s_b, b*7776 + ((w - 2)*4 + k)*1024, 0);
    }
    if (wave < 4) {
      #pragma unroll
      for (int c = 0; c < 9; c++) { xn[18 + 2*c] = *(LAS bf16x8*)(lds + xr0 + c*2048); xn[18 + 2*c + 1] = *(LAS bf16x8*)(lds + xr1 + c*2048); }
    }
    STAMP(2);
  }

  unsigned z1p[9][4];
  f32x4 acc[9][2];

  if (wave < 4) {
    constexpr int D = 3, NB = 4;
    int wl = (8*(lane >> 5) + 2*((lane >> 2) & 3))*288 + ((lane >> 4) & 1)*32 + (lane & 3)*8;
    int slot_off = 0;
    f32x16 a32[5];
    auto body = [&](auto NTC) {
      constexpr int NT = NTC.value, FS = 4*NT;
      bf16x8 wf[NB];
      int wc = wl + slot_off, wn;
      OPAQUE(wc);
      auto rdw = [&](auto G, int base) {
        constexpr int i = G.value % FS, k16 = i / NT, n = i % NT;
        LAS char* p = lds + base + (k16 >> 1)*9216 + (k16 & 1)*16*288 + n*64;
        s16x4 lo = __builtin_amdgcn_ds_read_tr16_b64_v4i16((LAS s16x4*)p);
        s16x4 hi = __builtin_amdgcn_ds_read_tr16_b64_v4i16((LAS s16x4*)(p + 288));
        wf[G.value % NB] = __builtin_shufflevector(lo, hi, 0, 1, 2, 3, 4, 5, 6, 7);
      };
      #pragma unroll
      for (int n = 0; n < NT; n++)
        #pragma unroll
        for (int e = 0; e < 16; e++) a32[n][e] = 0.f;
      static_for<0, D>([&](auto G) { rdw(G, wc); });
      SB();
            static_for<0, 9*FS>([&](auto G) {
        constexpr int st = G.value / FS, i = G.value % FS, k16 = i / NT, n = i % NT;
        if constexpr (i == 0) { slot_off = (slot_off >= 4*P1_STAGE) ? 0 : slot_off + P1_STAGE; wn = wl + slot_off; OPAQUE(wn); }
        if constexpr (i == FS - D) { SB(); if constexpr (NT == 5) PIN5(a32); else asm volatile("" : "+v"(a32[0]), "+v"(a32[1]), "+v"(a32[2]), "+v"(a32[3]));
          BAR(); SB(); }
        a32[n] = MFMA32(wf[G.value % NB], xn[4*st + k16], a32[n]);
        if constexpr (G.value + D < 9*FS) { if constexpr (i + D < FS) rdw(IC<G.value + D>{}, wc); else rdw(IC<G.value + D>{}, wn); }
        SG(0x8, 1); SG(0x100, 2);
        SB();
        if constexpr (i == FS - 1) wc = wn;
      });
      if constexpr (NT == 5) PIN5(a32); else asm volatile("" : "+v"(a32[0]), "+v"(a32[1]), "+v"(a32[2]), "+v"(a32[3]));
    };
    #pragma unroll 1
    for (int rep = 0; rep < 2; rep++) {
      asm volatile("" : "+s"(rep));
      body(IC<5>{});
      if (rep == 0) {
        {
      int l2 = lane; OPAQUE(l2);
      const int sr = l2 & 31, h4 = (l2 >> 5)*4;
      auto pk4 = [&](int n, int rq) { return u32x2{pack2(f32x2{a32[n][4*rq], a32[n][4*rq + 1]}), pack2(f32x2{a32[n][4*rq + 2], a32[n][4*rq + 3]})}; };
        f32x2 q1 = {0.f, 0.f}, q2 = {0.f, 0.f};
        LAS char* p = lds + ZB_OFF + w*IMG_W + sr*IMG_PITCH + h4*2;
        #pragma unroll
        for (int n = 0; n < 5; n++)
          #pragma unroll
          for (int rq = 0; rq < 4; rq++) if (n < 4 || rq < 2) {
            const f32x2 ya = gelu2(f32x2{a32[n][4*rq], a32[n][4*rq + 1]}), yb = gelu2(f32x2{a32[n][4*rq + 2], a32[n][4*rq + 3]});
            q1 += ya; q2 = __builtin_elementwise_fma(ya, ya, q2);
            q1 += yb; q2 = __builtin_elementwise_fma(yb, yb, q2);
            *(LAS u32x2*)(p + n*64 + rq*16) = u32x2{pack2(ya), pack2(yb)};
          }
        float t1 = q1[0] + q1[1], t2 = q2[0] + q2[1];
        t1 += __shfl_xor(t1, 32); t2 += __shfl_xor(t2, 32);
        const u32x4 rec = {__builtin_bit_cast(unsigned, t1), T, __builtin_bit_cast(unsigned, t2), T};
        if (l2 < 32) __builtin_amdgcn_raw_buffer_store_b128(rec, MKRSRC(ws + WS_STAT, 2048*256), ((pi*16 + pj)*128 + 32*w + sr)*16, 0, AUX_SC1);
        STAMP(3);
        WAITL(0);
        }
        body(IC<4>{});
        {
      int l2 = lane; OPAQUE(l2);
      const int sr = l2 & 31, h4 = (l2 >> 5)*4;
      auto pk4 = [&](int n, int rq) { return u32x2{pack2(f32x2{a32[n][4*rq], a32[n][4*rq + 1]}), pack2(f32x2{a32[n][4*rq + 2], a32[n][4*rq + 3]})}; };
        LAS char* p = lds + QB_OFF + (16 + 32*w + sr)*QB_PITCH + h4*2;
        #pragma unroll
        for (int n = 0; n < 4; n++)
          #pragma unroll
          for (int rq = 0; rq < 4; rq++) if (n < 3 || rq < 3 || h4 == 0) *(LAS u32x2*)(p + n*64 + rq*16) = pk4(n, rq);
        STAMP(4);
        WAITL(0);
        }
      }
    }
    {
      int l2 = lane; OPAQUE(l2);
      const int sr = l2 & 31, h4 = (l2 >> 5)*4;
      LAS char* p = lds + Z1B_OFF + w*IMG_W + sr*IMG_PITCH + h4*2;
      #pragma unroll
      for (int n = 0; n < 5; n++)
        #pragma unroll
        for (int rq = 0; rq < 4; rq++) if (n < 4 || rq < 2)
          *(LAS u32x2*)(p + n*64 + rq*16) = u32x2{pack2(f32x2{a32[n][4*rq], a32[n][4*rq + 1]}), pack2(f32x2{a32[n][4*rq + 2], a32[n][4*rq + 3]})};
      STAMP(5);
      WAITL(0);
    }
    BAR();
  } else {
    const int g = lane >> 4, fl = lane & 15;
    __builtin_amdgcn_s_setprio(3);
    if (isD) {
      WAITV(18); VB();
      #pragma unroll 1
      for (int q = 1; q < 6; q++) { p1_issue(); WAITV(18); VB(); }
      #pragma unroll
      for (int t = 0; t < 9; t++) { const int t_ = cv[t]; cv[t] = cq[t]; cq[t] = t_; }
      csrd = make_srd(wqkv, 576*3456); p1so = 32*(w & 1)*3456 + ((72*pj) & ~15); p1step = 64*3456;
      #pragma unroll 1
      for (int q = 6; q < 15; q++) { p1_issue(); WAITV(18); VB(); }
      #pragma unroll
      for (int t = 0; t < 9; t++) cv[t] = cq[t];
      csrd = make_srd(w_in, 576*9216); p1so = 32*(w & 1)*9216 + 144*pj*2; p1step = 64*9216;
      #pragma unroll 1
      for (int q = 15; q < 24; q++) {
        if (q == 19 && wave == 4 && pi < 15) {
          const RSRC r_halo = MKRSRC(ws + WS_HALO + (size_t)(pi*16 + pj)*4096, 4096);
          #pragma unroll
          for (int c = 0; c < 4; c++) { int ch = c*64 + lane; if (ch > 247) ch = 247;
            u32x4 v = *(LAS u32x4*)(lds + QB_OFF + 128*QB_PITCH + ch*16);
            __builtin_amdgcn_raw_buffer_store_b128(v, r_halo, ch*16, 0, AUX_SC1); }
        }
        p1_issue(); WAITV(18); VB();
        if (q == 20 && wave == 4 && lane == 0 && pi < 15) set_flag(ws, FL_HALO, pi, pj, T);
      }
      u32x4 hv[4]; unsigned hf = T;
      const bool hal = (wave == 5 && pi > 0);
      gu32* fp = (gu32*)(ws + WS_FLAGS + (size_t)(FL_HALO*16 + (pi > 0 ? pi - 1 : 0))*256) + pj;
      if (hal) asm volatile("global_load_dword %0, %1, %2 sc1" : "=v"(hf) : "v"(0), "s"(fp) : "memory");
      if (hal) WAITV(10); else WAITV(9);
      VB();
      if (hal) {
        asm volatile("s_waitcnt vmcnt(0)" : "+v"(hf) :: "memory");
        if (__builtin_amdgcn_readfirstlane(hf) != T)
          for (int spin = 0; spin < SPIN_MAX; ++spin) { if (__builtin_amdgcn_readfirstlane(__hip_atomic_load(fp, RLX_AGENT)) == T) break; __builtin_amdgcn_s_sleep(1); }
        const u32x4 s_hal = make_srd(ws + WS_HALO + (size_t)((pi - 1)*16 + pj)*4096, 4096);
        static_for<0, 4>([&](auto C) { int ch = C.value*64 + lane; if (ch > 247) ch = 247; u32x4 t;
          asm volatile("buffer_load_dwordx4 %0, %1, %2, 0 offen sc1" : "=v"(t) : "v"(ch*16), "s"(s_hal) : "memory"); hv[C.value] = t; });
      } else WAITV(0);
      VB();
      if (wave == 5) {
        if (pi > 0) asm volatile("s_waitcnt vmcnt(0)" : "+v"(hv[0]), "+v"(hv[1]), "+v"(hv[2]), "+v"(hv[3]) :: "memory");
        #pragma unroll
        for (int c = 0; c < 4; c++) { int ch = c*64 + lane; if (ch > 247) ch = 247; *(LAS u32x4*)(lds + QB_OFF + ch*16) = (pi > 0) ? hv[c] : u32x4{0, 0, 0, 0}; }
        WAITL(0);
      }
      VB();
    } else {
      const int gA = 2*(w - 2);
      {
        const u32x4 s_a = make_srd(w_sp, 2048*4096), s_b = make_srd(w_out, 2304*1152), s_c = make_srd(wo, 576*1152);
        SETM0(149504);
        const int vl = lane*16;
        #pragma unroll 1
        for (int q = 0; q < 4; q++) VB();
        #pragma unroll 1
        for (int q = 4; q < 16; q++) {
          const int k = q - 4;
          if (k < 8) { DMA(vl, s_a, b*32768 + ((w - 2)*16 + 2*k)*1024, 0); DMA(vl, s_a, b*32768 + ((w - 2)*16 + 2*k + 1)*1024, 0); }
          else if (k < 11) { DMA(vl, s_b, b*10368 + ((w - 2)*6 + 2*(k - 8))*1024, 0); DMA(vl, s_b, b*10368 + ((w - 2)*6 + 2*(k - 8) + 1)*1024, 0); }
          else { DMA(vl, s_c, b*2592 + ((w - 2)*2)*1024, 0); DMA(vl, s_c, b*2592 + ((w - 2)*2 + 1)*1024, 0); }
          VB();
        }
      }
      LAS f32x2* st = (LAS f32x2*)(lds + MISC_OFF + (w - 2)*512);
      {
        const u32x4 s_st = make_srd(ws + WS_STAT, 2048*256);
        const int vo = (pi*16*128 + 32*gA + lane)*16;
        u32x4 rec[16];
        static_for<0, 16>([&](auto C) { u32x4 t; asm volatile("buffer_load_dwordx4 %0, %1, %2, %3 offen sc1" : "=v"(t) : "v"(vo), "s"(s_st), "s"(C.value*2048) : "memory"); rec[C.value] = t; });
        WAITV(16);
        VB();
        VB();
        asm volatile("s_waitcnt vmcnt(0)" : "+v"(rec[0]), "+v"(rec[1]), "+v"(rec[2]), "+v"(rec[3]), "+v"(rec[4]), "+v"(rec[5]), "+v"(rec[6]), "+v"(rec[7]),
                          "+v"(rec[8]), "+v"(rec[9]), "+v"(rec[10]), "+v"(rec[11]), "+v"(rec[12]), "+v"(rec[13]), "+v"(rec[14]), "+v"(rec[15]) :: "memory");
        float t1 = 0.f, t2 = 0.f; bool ok = true;
        #pragma unroll
        for (int c = 0; c < 16; c++) { t1 += asf(rec[c][0]); t2 += asf(rec[c][2]); ok = ok && rec[c][1] == T && rec[c][3] == T; }
        if (!__all(ok)) {
          const RSRC r_stat = MKRSRC(ws + WS_STAT, 2048*256);
          for (int spin = 0; spin < SPIN_MAX; ++spin) {
            t1 = 0.f; t2 = 0.f; ok = true;
            #pragma unroll
            for (int c = 0; c < 16; c++) {
              u32x4 v = __builtin_amdgcn_raw_buffer_load_b128(r_stat, vo + c*2048, 0, AUX_SC1);
              t1 += asf(v[0]); t2 += asf(v[2]); ok = ok && v[1] == T && v[3] == T;
            }
            if (__all(ok)) break;
            __builtin_amdgcn_s_sleep(1);
          }
        }
        const float mean = t1*(1.f/2304.f), var = fmaxf(t2*(1.f/2304.f) - mean*mean, 0.f);
        const float rs = __builtin_amdgcn_rsqf(var + 1e-5f);
        st[lane] = f32x2{rs, -mean*rs};
        WAITL(0);
        VB();
      }
      const RSRC r_z2n = MKRSRC(ws + WS_Z2N, 16*ZJS);
      #pragma unroll 1
      for (int k = 0; k < 2; k++) {
        f32x2 r2[4], h2[4];
        #pragma unroll
        for (int d = 0; d < 4; d++) { f32x2 u0 = st[32*k + 8*g + 2*d], u1 = st[32*k + 8*g + 2*d + 1]; r2[d] = f32x2{u0[0], u1[0]}; h2[d] = f32x2{u0[1], u1[1]}; }
        LAS char* p = lds + ZB_OFF + (gA + k)*IMG_W + (8*g + ((lane >> 2) & 3))*IMG_PITCH + (lane & 3)*8;
        int zo = lane*16 + pj*ZJS + (4*pi + gA + k)*9216;
        #pragma unroll 1
        for (int j3 = 0; j3 < 3; j3++, p += 96, zo += 3072) {
          u32x4 v[3];
          #pragma unroll
          for (int u = 0; u < 3; u++) {
            s16x4 lo = __builtin_amdgcn_ds_read_tr16_b64_v4i16((LAS s16x4*)(p + u*32));
            s16x4 hi = __builtin_amdgcn_ds_read_tr16_b64_v4i16((LAS s16x4*)(p + u*32 + 4*IMG_PITCH));
            v[u] = __builtin_bit_cast(u32x4, __builtin_shufflevector(lo, hi, 0, 1, 2, 3, 4, 5, 6, 7));
          }
          #pragma unroll
          for (int u = 0; u < 3; u++) {
            u32x4 o;
            #pragma unroll
            for (int d = 0; d < 4; d++) o[d] = pack2(f32x2{opq(__builtin_fmaf(bf_lo(v[u][d]), r2[d][0], h2[d][0])), opq(__builtin_fmaf(bf_hi(v[u][d]), r2[d][1], h2[d][1]))});
            __builtin_amdgcn_raw_buffer_store_b128(o, r_z2n, zo + u*1024, 0, AUX_SC1);
          }
          VB();
        }
      }
      VB();
      VB();
    }
    STAMP(5);
    BAR();
    if (!isD) { WAITV(0); if (lane == 0) set_flag(ws, FL_Z, pj, 2*pi + (w - 2), T); }
    {
      #pragma unroll
      for (int m = 0; m < 2; m++) {
        LAS char* zx = lds + Z1B_OFF + w*IMG_W + (8*(fl >> 2) + 4*m + (fl & 3))*IMG_PITCH + g*8;
        #pragma unroll
        for (int n = 0; n < 9; n++) { u32x2 v = *(LAS u32x2*)(zx + n*32); z1p[n][2*m] = v[0]; z1p[n][2*m + 1] = v[1]; }
      }
    }
  }

  const u32x4 s_z2n = make_srd(ws + WS_Z2N + (size_t)pj*ZJS, 64*9*1024);
  const u32x4 s_wsp = make_srd(w_sp + (size_t)128*pi*2048, 128*4096);
  int vw[4];
  const int vlin = lane*16;
  auto p2_issue = [&](int s, auto ZR) {
    const int base = (s & 3)*P2_STAGE;
    if constexpr (ZR.value) {
      static_for<0, 9>([&](auto K) { SETM0(base + w*9216 + K.value*1024); DMAS(vlin, s_z2n, ((2*s + w)*9 + K.value)*1024, 0); });
    } else {
      static_for<0, 8>([&](auto K) { constexpr int k = K.value; const int wr = 2*(w - 2) + (k >> 2);
        SETM0(base + 18432 + wr*4096 + (k & 3)*1024); DMA(vw[k & 3], s_wsp, wr*32*4096 + s*128, 0); });
    }
  };
  if (wave >= 4) {
    { int l2 = lane; OPAQUE(l2);
      const int a = l2 >> 3, bb = l2 & 7;
      #pragma unroll
      for (int m = 0; m < 2; m++)
        #pragma unroll
        for (int hf = 0; hf < 2; hf++) { int rho = 8*hf + a; vw[2*m + hf] = (8*(rho >> 2) + 4*m + (rho & 3))*4096 + ((bb ^ ((rho >> 1) & 7))*16); } }
    if (w >= 2) {
      #pragma unroll 1
      for (int s = 0; s < 2; s++) p2_issue(s, IC<0>{});
    }
  }
  {
      int t2 = tid; OPAQUE(t2);
      const int u = t2 & 1, hh = (t2 >> 7) & 1, tl = ((t2 >> 1) & 63) + 64*(t2 >> 8);
      LAS char* rowp = lds + QB_OFF + (16 + tl)*QB_PITCH + 8*(pj & 1) + 36*hh;
      const int o8 = hh*4, o4 = hh ? 0 : 32;
      u32x2 qv[4]; unsigned q0;
      #pragma unroll
      for (int d = 0; d < 4; d++) qv[d] = *(LAS u32x2*)(rowp + o8 + d*8);
      q0 = *(LAS unsigned*)(rowp + o4);
      float lg[4];
      #pragma unroll
      for (int ww = 0; ww < 4; ww++) {
        LAS char* kr = rowp - (2*(4*u + ww))*QB_PITCH + 80;
        float sa = dot2(q0, *(LAS unsigned*)(kr + o4), 0.f), sb = 0.f;
        #pragma unroll
        for (int d = 0; d < 4; d++) { u32x2 kv = *(LAS u32x2*)(kr + o8 + d*8); sa = dot2(qv[d][0], kv[0], sa); sb = dot2(qv[d][1], kv[1], sb); }
        float s = (sa + sb)*0.23570226039551584f;
        if (128*pi + tl - 2*(4*u + ww) < 0) s = -1e9f;
        lg[ww] = s;
      }
      float l8[8];
      #pragma unroll
      for (int ww = 0; ww < 4; ww++) { float o = dpp_f<0xB1>(lg[ww]); l8[ww] = u ? o : lg[ww]; l8[4 + ww] = u ? lg[ww] : o; }
      float mx = l8[0];
      #pragma unroll
      for (int ww = 1; ww < 8; ww++) mx = fmaxf(mx, l8[ww]);
      float sum = 0.f;
      #pragma unroll
      for (int ww = 0; ww < 8; ww++) { l8[ww] = __builtin_amdgcn_exp2f((l8[ww] - mx)*1.4426950408889634f); sum += l8[ww]; }
      const float inv = __builtin_amdgcn_rcpf(sum);
      float olo[5], ohi[5];
      #pragma unroll
      for (int d = 0; d < 5; d++) { olo[d] = 0.f; ohi[d] = 0.f; }
      #pragma unroll
      for (int ww = 0; ww < 8; ww++) {
        const unsigned pb = pack_bf16(l8[ww]*inv, 0.f);
        const unsigned ph = pb << 16;
        LAS char* vr = rowp - (2*ww)*QB_PITCH + 160;
        u32x2 va = *(LAS u32x2*)(vr + o8 + 16*u), vb = *(LAS u32x2*)(vr + o8 + 16*u + 8); unsigned vc = *(LAS unsigned*)(vr + o4);
        olo[0] = dot2(va[0], pb, olo[0]); ohi[0] = dot2(va[0], ph, ohi[0]); olo[1] = dot2(va[1], pb, olo[1]); ohi[1] = dot2(va[1], ph, ohi[1]);
        olo[2] = dot2(vb[0], pb, olo[2]); ohi[2] = dot2(vb[0], ph, ohi[2]); olo[3] = dot2(vb[1], pb, olo[3]); ohi[3] = dot2(vb[1], ph, ohi[3]);
        olo[4] = dot2(vc, pb, olo[4]); ohi[4] = dot2(vc, ph, ohi[4]);
      }
      *(LAS u32x2*)(rowp + o8 + 16*u) = u32x2{pack_bf16(olo[0], ohi[0]), pack_bf16(olo[1], ohi[1])};
      *(LAS u32x2*)(rowp + o8 + 16*u + 8) = u32x2{pack_bf16(olo[2], ohi[2]), pack_bf16(olo[3], ohi[3])};
      if (u) *(LAS unsigned*)(rowp + o4) = pack_bf16(olo[4], ohi[4]);
      }
  WAITL(0);
  STAMP(21);
  BAR();
  STAMP(22);
  if (wave >= 4 && w < 2) { wait_flags(ws, FL_Z, pj, 32, T, lane); STAMP(13); p2_issue(0, IC<1>{}); p2_issue(1, IC<1>{}); }
  if (wave >= 4) {
    #pragma unroll
    for (int n = 0; n < 3; n++) {
      #pragma unroll
      for (int d = 0; d < 4; d++) z1p[n][d] = pack2(gelu2(f32x2{bf_lo(z1p[n][d]), bf_hi(z1p[n][d])}));
      asm volatile("" : "+v"(z1p[n][0]), "+v"(z1p[n][1]), "+v"(z1p[n][2]), "+v"(z1p[n][3]));
    }
  }
  if (wave < 4) {
    const RSRC r_o = MKRSRC(ws + WS_O, 128*OTS);
    #pragma unroll 1
    for (int e = tid; e < 1152; e += 256) {
      const int tl = e / 9, p = e % 9;
      u32x2 v = *(LAS u32x2*)(lds + QB_OFF + (16 + tl)*QB_PITCH + 8*(pj & 1) + 8*p);
      const int col = 36*pj + 4*p, r32 = tl & 31;
      const int tt = 8*pi + 2*(tl >> 5) + ((r32 >> 2) & 1), rho = 4*(r32 >> 3) + (r32 & 3);
      const int ks = col >> 5, c32 = col & 31;
      __builtin_amdgcn_raw_buffer_store_b64(v, r_o, tt*OTS + (ks*64 + rho + 16*((c32 & 15) >> 2))*16 + (c32 >> 4)*8, 0, AUX_SC1);
    }
  }
  WAITL(0);
  BAR();
  STAMP(6);

  const int p3cnt = (xcd < 2) ? 30 : 29;
  const int p3T = xcd*29 + (xcd < 2 ? xcd : 2) + rr;
  const bool p3act = rr < p3cnt;
  const int rg = p3T / 9, pc = p3T % 9;

  if (wave >= 4) {
    __builtin_amdgcn_s_setprio(0);
    auto p2_helper = [&](auto ZR) {
      constexpr int CNT = ZR.value ? 9 : 8;
      p2_issue(2, ZR); p2_issue(3, ZR);
      WAITV(3*CNT);
      VB();
      auto piece = [&](int s, auto K) {
        constexpr int k = K.value; const int base = (s & 3)*P2_STAGE;
        if constexpr (ZR.value) { SETM0(base + w*9216 + k*1024); DMAS(vlin, s_z2n, ((2*s + w)*9 + k)*1024, 0); }
        else if constexpr (k < 8) { const int wr = 2*(w - 2) + (k >> 2); SETM0(base + 18432 + wr*4096 + (k & 3)*1024); DMA(vw[k & 3], s_wsp, wr*32*4096 + s*128, 0); }
      };
      auto step = [&](int s, auto PR) {
        WAITV(2*CNT);
        VB();
        static_for<0, 9>([&](auto K) {
          piece(s + 4, K);
          if constexpr (PR.value >= 0 && K.value == 2) {
            constexpr int pr = PR.value;
            unsigned rr2 = pack2(f32x2{opq(gelu_f(opq(bf_lo(z1p[pr / 4][pr % 4])))), opq(gelu_f(opq(bf_hi(z1p[pr / 4][pr % 4]))))});
            asm volatile("" : "+v"(rr2));
            z1p[pr / 4][pr % 4] = rr2;
          }
        });
      };
      static_for<0, 24>([&](auto S) {
        step(S.value, IC<12 + S.value>{});
        if constexpr (S.value == 4) { if (wave == 4 && lane == 0) set_flag(ws, FL_O, pi, pj, T); }
      });
      #pragma unroll 1
      for (int s = 24; s < 28; s++) step(s, IC<-1>{});
      WAITV(2*CNT); VB();
      WAITV(CNT); VB();
      #pragma unroll
      for (int n = 0; n < 9; n++) *(LAS u32x4*)(lds + w*9216 + n*1024 + lane*16) = u32x4{z1p[n][0], z1p[n][1], z1p[n][2], z1p[n][3]};
      WAITV(0); WAITL(0); VB();
      VB();
    };
    if (w < 2) p2_helper(IC<1>{}); else p2_helper(IC<0>{});
    STAMP(8);
    if (p3act) {
      const u32x4 s_o = make_srd(ws + WS_O, 128*OTS), s_h = make_srd(ws + WS_H, 128*HTS);
      const u32x4 s_wo = make_srd(wo, 576*1152), s_wout = make_srd(w_out, 2304*1152);
      int vW0, vW1, vA[5];
      int l2 = lane; OPAQUE(l2);
      { const int r = l2 >> 3;
        vW0 = r*1152 + (((l2 & 7) ^ (((r >> 1) & 3) << 1))*16);
        vW1 = (r + 8)*1152 + (((l2 & 7) ^ ((((r + 8) >> 1) & 3) << 1))*16);
        #pragma unroll
        for (int m5 = 0; m5 < 5; m5++) { int tt = 5*rg + m5; if (tt > 127) tt = 127; vA[m5] = l2*16 + tt*OTS; } }
      int slot = 2;
      auto p3_ks = [&](u32x4 sw, u32x4 sa, int R0, int R1, int ks) {
        if (w < 3) {
          const int base = slot*P3_STAGE + w*9216, s0 = R0*1152 + pc*128, s1 = R1*1152 + pc*128;
          SETM0(base + 5120); DMA(vW0, sw, s0, 0); SETM0(base + 6144); DMA(vW1, sw, s0, 0);
          SETM0(base + 7168); DMA(vW0, sw, s1, 0); SETM0(base + 8192); DMA(vW1, sw, s1, 0);
          static_for<0, 5>([&](auto M) { SETM0(base + M.value*1024); DMAS(vA[M.value], sa, ks*1024, 0); });
        }
        slot = (slot == 4) ? 0 : slot + 1;
      };
      auto p3_wo = [&](int u) { const int ks = 3*u + w; p3_ks(s_wo, s_o, 32*ks, 32*ks + 16, ks); };
      auto p3_g3 = [&](int v) { const int ks = 3*v + w;
        const int R0 = (ks < 64) ? 144*(ks >> 2) + 32*(ks & 3) : 288*(ks - 64) + 128;
        p3_ks(s_wout, s_h, R0, R0 + ((ks < 64) ? 16 : 144), ks); };
      const int p0 = (5*rg) >> 3, p1 = ((5*rg + 4 > 127 ? 127 : 5*rg + 4)) >> 3;
      if (w < 3) { wait_flags(ws, FL_O, p0, 16, T, lane); wait_flags(ws, FL_O, p1, 16, T, lane); }
      STAMP(24);
      #pragma unroll 1
      for (int u = 0; u < 3; u++) p3_wo(u);
      BAR();
      p3_wo(3); p3_wo(4);
      WAITV(27);
      STAMP(26);
      BAR();
      WAITV(18); BAR();
      p3_wo(5); WAITV(18); BAR();
      WAITV(9); BAR();
      WAITV(0); BAR();
      BAR();
      #pragma unroll
      for (int m5 = 0; m5 < 5; m5++) { int tt = 5*rg + m5; if (tt > 127) tt = 127; vA[m5] = l2*16 + tt*HTS; }
      if (w < 3) { wait_flags(ws, FL_H, p0, 16, T, lane); wait_flags(ws, FL_H, p1, 16, T, lane); }
      STAMP(27);
      RSTAMP(31);
      #pragma unroll 1
      for (int v = 0; v < 4; v++) p3_g3(v);
      WAITV(18); BAR();
      #pragma unroll 1
      for (int u = 6; u < 26; u++) {
        p3_g3(u - 2);
        WAITV(18); BAR();
      }
      WAITV(9); BAR();
      WAITV(0); BAR();
      BAR();
    }
  } else {
    int l2 = lane; OPAQUE(l2);
    const int g = l2 >> 4, fl = l2 & 15;
    const int zoff = l2*16;
    const int woff0 = 18432 + (4*w + (fl >> 3))*1024 + (fl & 7)*128 + ((g ^ ((fl >> 1) & 7))*16);
    float bs[2];
    #pragma unroll
    for (int m = 0; m < 2; m++) bs[m] = bf_lo(b_sp[128*pi + 32*w + 8*(fl >> 2) + 4*m + (fl & 3)]);
    #pragma unroll
    for (int n = 0; n < 9; n++) { acc[n][0] = f32x4{bs[0], bs[0], bs[0], bs[0]}; acc[n][1] = f32x4{bs[1], bs[1], bs[1], bs[1]}; }
    bf16x8 z0[9], w0[2], z1[9], w1[2];
    auto rd = [&](bf16x8 (&z)[9], bf16x8 (&wv)[2], int s, int kk) {
      const int base = (s & 3)*P2_STAGE;
      LAS char* pw = lds + base + (woff0 ^ (kk*64));
      LAS char* pz = lds + base + kk*9216 + zoff;
      wv[0] = *(LAS bf16x8*)pw; wv[1] = *(LAS bf16x8*)(pw + 2048);
      #pragma unroll
      for (int n = 0; n < 9; n++) z[n] = *(LAS bf16x8*)(pz + n*1024);
    };
    auto mm = [&](bf16x8 (&z)[9], bf16x8 (&wv)[2]) {
      #pragma unroll
      for (int n = 0; n < 9; n++) { acc[n][0] = MFMA(z[n], wv[0], acc[n][0]); acc[n][1] = MFMA(z[n], wv[1], acc[n][1]); }
      #pragma unroll
      for (int q = 0; q < 11; q++) { SG(0x8, 1); SG(0x100, 1); }
      SG(0x8, 7);
    };
    BAR();
    rd(z0, w0, 0, 0);
    CWAITL0(); SB();
    #pragma unroll 1
    for (int s = 0; s < 32; s++) {
      CWAITL0(); SB();
      rd(z1, w1, s, 1);
      mm(z0, w0);
      SB(); CWAITL0(); PIN_ACC(acc);
      if (s == 4) WAITV(0);
      BAR(); SB();
      rd(z0, w0, (s + 1 < 32 ? s + 1 : s), 0);
      mm(z1, w1);
      SB();
    }
    STAMP(8);
    {
      const RSRC r_h = MKRSRC(ws + WS_H, 128*HTS);
      u32x4 z1g[9];
      #pragma unroll
      for (int n = 0; n < 9; n++) z1g[n] = *(LAS u32x4*)(lds + w*9216 + n*1024 + zoff);
      #pragma unroll
      for (int m = 0; m < 2; m++) {
        const int tt = 8*pi + 2*w + m;
        unsigned hp[9][2];
        #pragma unroll
        for (int n = 0; n < 9; n++) {
          hp[n][0] = pack2(f32x2{bf_lo(z1g[n][2*m]), bf_hi(z1g[n][2*m])}*f32x2{acc[n][m][0], acc[n][m][1]});
          hp[n][1] = pack2(f32x2{bf_lo(z1g[n][2*m + 1]), bf_hi(z1g[n][2*m + 1])}*f32x2{acc[n][m][2], acc[n][m][3]});
        }
        #pragma unroll
        for (int q = 0; q < 4; q++)
          __builtin_amdgcn_raw_buffer_store_b128(u32x4{hp[2*q][0], hp[2*q][1], hp[2*q + 1][0], hp[2*q + 1][1]}, r_h, zoff + tt*HTS + (4*pj + q)*1024, 0, AUX_SC1);
        __builtin_amdgcn_raw_buffer_store_b64(u32x2{hp[8][0], hp[8][1]}, r_h, zoff + (pj & 1)*8 + tt*HTS + (64 + (pj >> 1))*1024, 0, AUX_SC1);
      }
    }
    STAMP(19);
    RSTAMP(29);
    if (p3act) {
      WAITL(0);
      BAR();
      f32x4 aS[5], aC[5];
      u32x2 xv[5];
      const int q4 = (l2 >> 2) & 3, p4 = l2 & 3, r0 = 4*g + q4;
      const int woff = 5120 + r0*128 + (((2*w + (p4 >> 1)) ^ (((r0 >> 1) & 3) << 1))*16) + (p4 & 1)*8;
      bf16x8 hA[3][5], wA[3], hB[3][5], wB[3];
      int slot = 2;
      auto rd3 = [&](bf16x8 (&h)[3][5], bf16x8 (&wv)[3]) {
        LAS char* p = lds + slot*P3_STAGE;
        #pragma unroll
        for (int kk = 0; kk < 3; kk++) {
          s16x4 lo = __builtin_amdgcn_ds_read_tr16_b64_v4i16((LAS s16x4*)(p + kk*9216 + woff));
          s16x4 hi = __builtin_amdgcn_ds_read_tr16_b64_v4i16((LAS s16x4*)(p + kk*9216 + woff + 2048));
          wv[kk] = __builtin_shufflevector(lo, hi, 0, 1, 2, 3, 4, 5, 6, 7);
          #pragma unroll
          for (int m5 = 0; m5 < 5; m5++) h[kk][m5] = *(LAS bf16x8*)(p + kk*9216 + m5*1024 + zoff);
        }
        slot = (slot == 4) ? 0 : slot + 1;
      };
      auto mm3 = [&](bf16x8 (&h)[3][5], bf16x8 (&wv)[3]) {
        #pragma unroll
        for (int kk = 0; kk < 3; kk++)
          #pragma unroll
          for (int m5 = 0; m5 < 5; m5++) aC[m5] = MFMA(wv[kk], h[kk][m5], aC[m5]);
        #pragma unroll
        for (int q = 0; q < 15; q++) { SG(0x8, 1); SG(0x100, 1); }
        SG(0x100, 6);
      };
      #pragma unroll
      for (int m5 = 0; m5 < 5; m5++) {
        int tt = 5*rg + m5; if (tt > 127) tt = 127;
        const int row = 32*(tt >> 1) + 8*(fl >> 2) + 4*(tt & 1) + (fl & 3);
        xv[m5] = *(const u32x2*)(x + (size_t)row*D_ + 64*pc + 16*w + 4*g);
      }
      BAR();
      #pragma unroll 1
      for (int ph = 0; ph < 2; ph++) {
        #pragma unroll
        for (int m5 = 0; m5 < 5; m5++) aC[m5] = f32x4{0,0,0,0};
        rd3(hA, wA); SB();
        const int nit = ph ? 12 : 3;
        #pragma unroll 1
        for (int it = 0; it < nit; it++) {
          CWAITL0(); SB(); rd3(hB, wB); mm3(hA, wA); SB(); PIN5(aC); BAR(); SB();
          CWAITL0(); SB(); rd3(hA, wA); mm3(hB, wB); SB(); PIN5(aC);
          if (ph == 0 && it == 1) { WAITV(0); RSTAMP(30); }
          if (ph == 0 || it < 11) BAR();
          SB();
          if (ph == 0 && it == 1 && tid == 0) set_flag(ws, FL_H, pi, pj, T);
        }
        if (ph == 0) {
          #pragma unroll
          for (int m5 = 0; m5 < 5; m5++) aS[m5] = aC[m5];
          slot = 3;
          STAMP(10);
        }
      }
      STAMP(11);
      #pragma unroll
      for (int m5 = 0; m5 < 5; m5++) {
        const int tt = 5*rg + m5;
        if (tt < 128) {
          const int row = 32*(tt >> 1) + 8*(fl >> 2) + 4*(tt & 1) + (fl & 3);
          const size_t o = (size_t)row*D_ + 64*pc + 16*w + 4*g;
          float r[4];
          #pragma unroll
          for (int e = 0; e < 4; e++) {
            float bv = aC[m5][e], sv = aS[m5][e];
            float sl = bv*__builtin_amdgcn_rcpf(1.f + __builtin_amdgcn_exp2f(-1.4426950408889634f*bv));
            unsigned xw = xv[m5][e >> 1];
            float xx = (e & 1) ? bf_hi(xw) : bf_lo(xw);
            r[e] = __builtin_fmaf(sv, sl, xx);
          }
          *(u32x2*)(out + o) = u32x2{pack_bf16(r[0], r[1]), pack_bf16(r[2], r[3])};
        }
      }
    } else {
      WAITV(0);
      BAR();
      if (tid == 0) set_flag(ws, FL_H, pi, pj, T);
    }
    if (b == 0 && tid == 0) __hip_atomic_store((gu32*)ws, T, RLX_AGENT);
    STAMP(12);
  }
  TDUMP();
  asm volatile(".Lpf_end:");
}

extern "C" void kernel_launch(void* const* d_in, const int* in_sizes, int n_in, void* d_out, int out_size, void* d_ws, size_t ws_size, hipStream_t stream) {
  (void)hipFuncSetAttribute((const void*)fused_kernel, hipFuncAttributeMaxDynamicSharedMemorySize, LDS_BYTES);
  fused_kernel<<<256, 512, LDS_BYTES, stream>>>((const u16*)d_in[0], (const u16*)d_in[1], (const u16*)d_in[2], (const u16*)d_in[4],
      (const u16*)d_in[5], (const u16*)d_in[6], (const u16*)d_in[7], (u16*)d_out, (char*)d_ws);
}
```

```cpp
#include <hip/hip_runtime.h>
#include <stdint.h>

typedef unsigned short u16;
typedef __attribute__((ext_vector_type(8))) short bf16x8;
typedef __attribute__((ext_vector_type(4))) short s16x4;
typedef __attribute__((ext_vector_type(4))) float f32x4;
typedef __attribute__((ext_vector_type(16))) float f32x16;
typedef __attribute__((ext_vector_type(2))) float f32x2;
typedef __attribute__((ext_vector_type(4))) unsigned u32x4;
typedef __attribute__((ext_vector_type(2))) unsigned u32x2;
typedef __attribute__((ext_vector_type(2))) __bf16 bf16x2;
#define GAS __attribute__((address_space(1)))
#define LAS __attribute__((address_space(3)))
typedef GAS unsigned gu32;
#define RLX_AGENT __ATOMIC_RELAXED, __HIP_MEMORY_SCOPE_AGENT
#define BAR() asm volatile("s_barrier" ::: "memory")
#define SB() __builtin_amdgcn_sched_barrier(0)
#define SG(mask, n) __builtin_amdgcn_sched_group_barrier(mask, n, 0)
#define WAITV(n) asm volatile("s_waitcnt vmcnt(%0)" :: "i"(n) : "memory")
#define WAITL(n) asm volatile("s_waitcnt lgkmcnt(%0)" :: "i"(n) : "memory")
#define CWAITL0() __builtin_amdgcn_s_waitcnt(0xC07F)
#define RSRC __amdgpu_buffer_rsrc_t
#define MKRSRC(p, bytes) __builtin_amdgcn_make_buffer_rsrc((void*)(p), 0, (bytes), 0x00020000)
#define AUX_SC1 16
#define OPAQUE(x) asm volatile("" : "+v"(x))

constexpr int D_ = 576, FH = 2304;
constexpr size_t WS_FLAGS = 4096;
constexpr size_t WS_STAT  = 32768;
constexpr size_t WS_HALO  = WS_STAT + 2048*256;
constexpr int ZJS = 64*9*1024 + 256, HTS = 72*1024 + 256, OTS = 18*1024 + 256;
constexpr size_t WS_Z2N   = WS_HALO + 16*16*4096;
constexpr size_t WS_H     = WS_Z2N + (size_t)16*ZJS;
constexpr size_t WS_O     = WS_H + (size_t)128*HTS;
constexpr size_t WS_DBG   = WS_O + (size_t)128*OTS;
constexpr int FL_STAT = 0, FL_Z = 1, FL_HALO = 2, FL_O = 3, FL_H = 4;
constexpr int LDS_BYTES = 163840;
constexpr int P1_STAGE = 18432, P1_NS = 5;
constexpr int XCH_OFF = 4*P1_STAGE;
constexpr int ZB_OFF = P1_NS*P1_STAGE;
constexpr int QB_PITCH = 248;
constexpr int IMG_PITCH = 296, IMG_W = 32*IMG_PITCH;
constexpr int QB_OFF = ZB_OFF + 4*IMG_W - 16*QB_PITCH;
constexpr int MISC_OFF = QB_OFF + 144*QB_PITCH;
constexpr int Z1B_OFF = 4*P1_STAGE;
constexpr int P2_STAGE = 34816;
constexpr int P3_STAGE = 27648;
constexpr int SPIN_MAX = 1 << 18;

template<int N> struct IC { static constexpr int value = N; };
template<int B, int E, class F> __device__ __forceinline__ void static_for(F&& f) {
  if constexpr (B < E) { f(IC<B>{}); static_for<B + 1, E>(f); }
}

__device__ __forceinline__ unsigned pack2(f32x2 v) { return __builtin_bit_cast(unsigned, __builtin_convertvector(v, bf16x2)); }
__device__ __forceinline__ float opq(float x) { asm("" : "+v"(x)); return x; }
__device__ __forceinline__ unsigned pack_bf16(float a, float b) { return pack2(f32x2{a, b}); }
__device__ __forceinline__ float asf(unsigned u) { return __builtin_bit_cast(float, u); }
__device__ __forceinline__ float bf_lo(unsigned u) { return __builtin_bit_cast(float, u << 16); }
__device__ __forceinline__ float bf_hi(unsigned u) { return __builtin_bit_cast(float, u & 0xffff0000u); }
__device__ __forceinline__ float dot2(unsigned a, unsigned b, float c) {
  return __builtin_amdgcn_fdot2_f32_bf16(__builtin_bit_cast(bf16x2, a), __builtin_bit_cast(bf16x2, b), c, false);
}
__device__ __forceinline__ float gelu_f(float x) {
  const float c1 = -2.302208198f, c2 = -0.1029432397f;
  float t = __builtin_fmaf(x * x, c2, c1);
  float e = __builtin_amdgcn_exp2f(x * t);
  return x * __builtin_amdgcn_rcpf(1.f + e);
}
__device__ __forceinline__ f32x2 gelu2(f32x2 x) {
  const f32x2 c1 = {-2.302208198f, -2.302208198f}, c2 = {-0.1029432397f, -0.1029432397f}, one = {1.f, 1.f};
  f32x2 t = __builtin_elementwise_fma(x * x, c2, c1);
  f32x2 a = x * t;
  f32x2 e = {__builtin_amdgcn_exp2f(a[0]), __builtin_amdgcn_exp2f(a[1])};
  f32x2 d = e + one;
  f32x2 r = {__builtin_amdgcn_rcpf(d[0]), __builtin_amdgcn_rcpf(d[1])};
  return x * r;
}
template<int CTRL> __device__ __forceinline__ float dpp_f(float v) {
  return __builtin_bit_cast(float, __builtin_amdgcn_update_dpp(0, __builtin_bit_cast(int, v), CTRL, 0xf, 0xf, true));
}
__device__ __forceinline__ float row16_allsum(float v) {
  v += dpp_f<0x128>(v); v += dpp_f<0x124>(v); v += dpp_f<0x122>(v); v += dpp_f<0x121>(v); return v;
}
__device__ __forceinline__ bool poll_flags(const char* ws, int group, int idx, int nflags, unsigned T, int lane) {
  gu32* p = (gu32*)(ws + WS_FLAGS + (size_t)(group*16 + idx)*256) + (lane & (nflags - 1));
  unsigned v = __hip_atomic_load(p, RLX_AGENT);
  return __all(v == T);
}
__device__ __forceinline__ void wait_flags(const char* ws, int group, int idx, int nflags, unsigned T, int lane) {
  for (int spin = 0; spin < SPIN_MAX; ++spin) {
    if (poll_flags(ws, group, idx, nflags, T, lane)) break;
    __builtin_amdgcn_s_sleep(1);
  }
}
__device__ __forceinline__ void set_flag(char* ws, int group, int idx, int k, unsigned T) {
  __hip_atomic_store((gu32*)(ws + WS_FLAGS + (size_t)(group*16 + idx)*256) + k, T, RLX_AGENT);
}
__device__ __forceinline__ unsigned load_epoch(const char* ws) {
  unsigned e;
  asm volatile("s_load_dword %0, %1, 0x0\n\ts_waitcnt lgkmcnt(0)" : "=s"(e) : "s"(ws) : "memory");
  return e + 1u;
}
__device__ __forceinline__ int kperm(int r) { return 8*(r >> 3) + 4*(r & 1) + ((r >> 1) & 3); }
__device__ __forceinline__ u32x4 make_srd(const void* p, unsigned bytes) {
  unsigned long long a = (unsigned long long)p;
  return u32x4{(unsigned)a, (unsigned)(a >> 32) & 0xffffu, bytes, 0x00020000u};
}
__device__ __forceinline__ void SETM0(int v) { asm volatile("s_mov_b32 m0, %0\n\ts_nop 0" :: "s"(v) : "memory"); }
template<int OFF> __device__ __forceinline__ void dma_o(int voff, u32x4 srd, int soff) {
  asm volatile("buffer_load_dwordx4 %0, %1, %2 offen offset:%3 lds" :: "v"(voff), "s"(srd), "s"(soff), "i"(OFF) : "memory");
}
template<int OFF> __device__ __forceinline__ void dma_s(int voff, u32x4 srd, int soff) {
  asm volatile("buffer_load_dwordx4 %0, %1, %2 offen offset:%3 sc1 lds" :: "v"(voff), "s"(srd), "s"(soff), "i"(OFF) : "memory");
}
#define DMA(voff, srd, soff, OFF) dma_o<OFF>((voff), (srd), (soff))
#define DMAS(voff, srd, soff, OFF) dma_s<OFF>((voff), (srd), (soff))

#define SETPRIO1() __builtin_amdgcn_s_setprio(0)
#define PIN4(a) asm volatile("" : "+v"(a))
#define PIN_ACC(acc) asm volatile("" : "+v"(acc[0][0]), "+v"(acc[0][1]), "+v"(acc[1][0]), "+v"(acc[1][1]), "+v"(acc[2][0]), "+v"(acc[2][1]), \
  "+v"(acc[3][0]), "+v"(acc[3][1]), "+v"(acc[4][0]), "+v"(acc[4][1]), "+v"(acc[5][0]), "+v"(acc[5][1]), "+v"(acc[6][0]), "+v"(acc[6][1]), \
  "+v"(acc[7][0]), "+v"(acc[7][1]), "+v"(acc[8][0]), "+v"(acc[8][1]))
#define PIN5(a) asm volatile("" : "+v"(a[0]), "+v"(a[1]), "+v"(a[2]), "+v"(a[3]), "+v"(a[4]))
#define MFMA32(a, b, c) __builtin_amdgcn_mfma_f32_32x32x16_bf16((a), (b), (c), 0, 0, 0)
#define MFMA(a, b, c) __builtin_amdgcn_mfma_f32_16x16x32_bf16((a), (b), (c), 0, 0, 0)

#define STAMP(k)
#define RSTAMP(k)

#define VB() BAR()
#define TK(v)
#define ACC(k, a, b)
#define TDUMP()
extern "C" __global__ __launch_bounds__(512, 2) void fused_kernel(const u16* __restrict__ x, const u16* __restrict__ wqkv, const u16* __restrict__ wo,
    const u16* __restrict__ w_in, const u16* __restrict__ w_sp, const u16* __restrict__ b_sp, const u16* __restrict__ w_out,
    u16* __restrict__ out, char* __restrict__ ws)
{
  extern __shared__ __attribute__((aligned(16))) char smem[];
  LAS char* lds = (LAS char*)smem;
  const int tid = threadIdx.x, lane = tid & 63;
  const int wave = __builtin_amdgcn_readfirstlane(tid >> 6);
  const int w = wave & 3;
  const int b = blockIdx.x, xcd = b & 7, rr = b >> 3;
  const int pi = 4*(xcd & 3) + (rr & 3), pj = 8*(xcd >> 2) + (rr >> 2);
  STAMP(0);
  RSTAMP(28);
  if (wave == 7) {
    unsigned long long pc; unsigned off, sz;
    asm volatile("s_getpc_b64 %0\n.Lpf_here:\n\ts_mov_b32 %1, .Lpf_here-fused_kernel\n\ts_mov_b32 %2, .Lpf_end-fused_kernel" : "=s"(pc), "=s"(off), "=s"(sz));
    pc -= off;
    const u32x4 s_code = u32x4{(unsigned)pc, (unsigned)(pc >> 32) & 0xffffu, sz, 0x00020000u};
    const int per = 2048;
    SETM0(149504); DMA(lane*16, s_code, rr*per, 0); DMA(lane*16, s_code, rr*per, 1024);
  }
  unsigned T;

  int cv[9];
  u32x4 csrd = make_srd(w_in, 576*9216);
  int p1dst = (w & 1)*9216, p1so = 32*(w & 1)*9216 + (FH + 144*pj)*2, p1step = 64*9216;
  int cq[9];
  auto p1_offsets = [&](auto QKV, int (&dst)[9]) {
    int l3 = lane; OPAQUE(l3);
    int r = l3 / 18, ch = l3 % 18;
    #pragma unroll
    for (int t = 0; t < 9; t++) {
      const int kp = kperm(r);
      dst[t] = (QKV.value ? kp*3456 + (ch < 15 ? (ch / 5)*1152 + (ch % 5)*16 : 0) : kp*9216 + ch*16) - (t & 3)*1024;
      r += 3; ch += 10; if (ch >= 18) { ch -= 18; r += 1; }
    }
  };
  auto p1_issue = [&]() {
    SETM0(p1dst); DMA(cv[0], csrd, p1so, 0); DMA(cv[1], csrd, p1so, 1024); DMA(cv[2], csrd, p1so, 2048); DMA(cv[3], csrd, p1so, 3072);
    SETM0(p1dst + 4096); DMA(cv[4], csrd, p1so, 0); DMA(cv[5], csrd, p1so, 1024); DMA(cv[6], csrd, p1so, 2048); DMA(cv[7], csrd, p1so, 3072);
    SETM0(p1dst + 8192); DMA(cv[8], csrd, p1so, 0);
    p1dst = (p1dst >= 4*P1_STAGE) ? p1dst - 4*P1_STAGE : p1dst + P1_STAGE; p1so += p1step;
  };
  const bool isD = (wave == 4 || wave == 5);

  if (wave >= 4) __builtin_amdgcn_s_setprio(3);
  const int mt = wave >> 2;
  bf16x8 xn[36];
  {
    const u32x4 s_x = make_srd(x + (size_t)(128*pi + 32*w)*D_, 32*1152);
    const int a = lane >> 2, bq = lane & 3;
    u32x4 raw[18];
    {
      const int vx = (16*mt + a)*1152 + bq*16;
      static_for<0, 18>([&](auto C) { u32x4 t; asm volatile("buffer_load_dwordx4 %0, %1, %2, 0 offen offset:%3" : "=v"(t) : "v"(vx), "s"(s_x), "i"(C.value*64) : "memory"); raw[C.value] = t; });
    }
    if (isD) { p1_offsets(IC<0>{}, cv); p1_issue(); }
    STAMP(23);
    if (isD) WAITV(9); else WAITV(0);
    asm volatile("" : "+v"(raw[0]), "+v"(raw[1]), "+v"(raw[2]), "+v"(raw[3]), "+v"(raw[4]), "+v"(raw[5]), "+v"(raw[6]), "+v"(raw[7]), "+v"(raw[8]),
                      "+v"(raw[9]), "+v"(raw[10]), "+v"(raw[11]), "+v"(raw[12]), "+v"(raw[13]), "+v"(raw[14]), "+v"(raw[15]), "+v"(raw[16]), "+v"(raw[17]));
    STAMP(20);
    float s1 = 0.f, s2 = 0.f, s3 = 0.f, s4 = 0.f;
    unsigned ones = 0x3f803f80u; OPAQUE(ones);
    #pragma unroll
    for (int c = 0; c < 18; c++) {
      s1 = dot2(raw[c][0], ones, s1); s2 = dot2(raw[c][0], raw[c][0], s2); s3 = dot2(raw[c][1], ones, s3); s4 = dot2(raw[c][1], raw[c][1], s4);
      s1 = dot2(raw[c][2], ones, s1); s2 = dot2(raw[c][2], raw[c][2], s2); s3 = dot2(raw[c][3], ones, s3); s4 = dot2(raw[c][3], raw[c][3], s4);
    }
    s1 += s3; s2 += s4;
    s1 += dpp_f<0xB1>(s1); s1 += dpp_f<0x4E>(s1);
    s2 += dpp_f<0xB1>(s2); s2 += dpp_f<0x4E>(s2);
    const float mean = s1*(1.f/576.f), var = fmaxf(s2*(1.f/576.f) - mean*mean, 0.f);
    const float rs = __builtin_amdgcn_rsqf(var + 1e-5f), sh = -mean*rs;
    const f32x2 rs2 = {rs, rs}, sh2 = {sh, sh};
    int xw = XCH_OFF + w*18432 + (bq >> 1)*1024 + (32*(bq & 1) + ((16*mt + a) ^ (2*bq)))*16;
    int xr0 = XCH_OFF + w*18432 + (32*(lane >> 5) + ((lane & 31) ^ (2*(lane >> 5))))*16;
    int xr1 = XCH_OFF + w*18432 + 1024 + (32*(lane >> 5) + ((lane & 31) ^ (4 + 2*(lane >> 5))))*16;
    OPAQUE(xw); OPAQUE(xr0); OPAQUE(xr1);
    u32x4 xo[18];
    #pragma unroll
    for (int c = 0; c < 18; c++)
      #pragma unroll
      for (int d = 0; d < 4; d++) xo[c][d] = pack2(__builtin_elementwise_fma(f32x2{bf_lo(raw[c][d]), bf_hi(raw[c][d])}, rs2, sh2));
    T = load_epoch(ws);
    STAMP(1);
    #pragma unroll
    for (int c = 0; c < 9; c++) *(LAS u32x4*)(lds + xw + c*2048) = xo[c];
    WAITL(0);
    STAMP(7);
    BAR();
    if (isD) p1_issue();
    if (wave < 4) {
      #pragma unroll
      for (int c = 0; c < 9; c++) { xn[2*c] = *(LAS bf16x8*)(lds + xr0 + c*2048); xn[2*c + 1] = *(LAS bf16x8*)(lds + xr1 + c*2048); }
      WAITL(0);
    }
    STAMP(9);
    BAR();
    #pragma unroll
    for (int c = 0; c < 9; c++) *(LAS u32x4*)(lds + xw + c*2048) = xo[9 + c];
    if (isD) WAITV(9);
    WAITL(0);
    BAR();
    if (isD) { p1_issue(); p1_issue(); p1_offsets(IC<1>{}, cq); }
    if (wave >= 6) {
      const u32x4 s_a = make_srd(w_in, 576*9216), s_b = make_srd(wqkv, 576*3456);
      SETM0(149504);
      const int vl = lane*16;
      #pragma unroll
      for (int k = 0; k < 11; k++) DMA(vl, s_a, b*20736 + ((w - 2)*11 + k)*1024, 0);
      #pragma unroll
      for (int k = 0; k < 4; k++) DMA(vl, s_b, b*7776 + ((w - 2)*4 + k)*1024, 0);
    }
    if (wave < 4) {
      #pragma unroll
      for (int c = 0; c < 9; c++) { xn[18 + 2*c] = *(LAS bf16x8*)(lds + xr0 + c*2048); xn[18 + 2*c + 1] = *(LAS bf16x8*)(lds + xr1 + c*2048); }
    }
    STAMP(2);
  }

  unsigned z1p[9][4];
  f32x4 acc[9][2];

  if (wave < 4) {
    constexpr int D = 3, NB = 4;
    int wl = (8*(lane >> 5) + 2*((lane >> 2) & 3))*288 + ((lane >> 4) & 1)*32 + (lane & 3)*8;
    int slot_off = 0;
    f32x16 a32[5];
    auto body = [&](auto NTC) {
      constexpr int NT = NTC.value, FS = 4*NT;
      bf16x8 wf[NB];
      int wc = wl + slot_off, wn;
      OPAQUE(wc);
      auto rdw = [&](auto G, int base) {
        constexpr int i = G.value % FS, k16 = i / NT, n = i % NT;
        LAS char* p = lds + base + (k16 >> 1)*9216 + (k16 & 1)*16*288 + n*64;
        s16x4 lo = __builtin_amdgcn_ds_read_tr16_b64_v4i16((LAS s16x4*)p);
        s16x4 hi = __builtin_amdgcn_ds_read_tr16_b64_v4i16((LAS s16x4*)(p + 288));
        wf[G.value % NB] = __builtin_shufflevector(lo, hi, 0, 1, 2, 3, 4, 5, 6, 7);
      };
      #pragma unroll
      for (int n = 0; n < NT; n++)
        #pragma unroll
        for (int e = 0; e < 16; e++) a32[n][e] = 0.f;
      static_for<0, D>([&](auto G) { rdw(G, wc); });
      SB();
            static_for<0, 9*FS>([&](auto G) {
        constexpr int st = G.value / FS, i = G.value % FS, k16 = i / NT, n = i % NT;
        if constexpr (i == 0) { slot_off = (slot_off >= 4*P1_STAGE) ? 0 : slot_off + P1_STAGE; wn = wl + slot_off; OPAQUE(wn); }
        if constexpr (i == FS - D) { SB(); if constexpr (NT == 5) PIN5(a32); else asm volatile("" : "+v"(a32[0]), "+v"(a32[1]), "+v"(a32[2]), "+v"(a32[3]));
          BAR(); SB(); }
        a32[n] = MFMA32(wf[G.value % NB], xn[4*st + k16], a32[n]);
        if constexpr (G.value + D < 9*FS) { if constexpr (i + D < FS) rdw(IC<G.value + D>{}, wc); else rdw(IC<G.value + D>{}, wn); }
        SG(0x8, 1); SG(0x100, 2);
        SB();
        if constexpr (i == FS - 1) wc = wn;
      });
      if constexpr (NT == 5) PIN5(a32); else asm volatile("" : "+v"(a32[0]), "+v"(a32[1]), "+v"(a32[2]), "+v"(a32[3]));
    };
    #pragma unroll 1
    for (int rep = 0; rep < 2; rep++) {
      asm volatile("" : "+s"(rep));
      body(IC<5>{});
      if (rep == 0) {
        {
      int l2 = lane; OPAQUE(l2);
      const int sr = l2 & 31, h4 = (l2 >> 5)*4;
      auto pk4 = [&](int n, int rq) { return u32x2{pack2(f32x2{a32[n][4*rq], a32[n][4*rq + 1]}), pack2(f32x2{a32[n][4*rq + 2], a32[n][4*rq + 3]})}; };
        f32x2 q1 = {0.f, 0.f}, q2 = {0.f, 0.f};
        LAS char* p = lds + ZB_OFF + w*IMG_W + sr*IMG_PITCH + h4*2;
        #pragma unroll
        for (int n = 0; n < 5; n++)
          #pragma unroll
          for (int rq = 0; rq < 4; rq++) if (n < 4 || rq < 2) {
            const f32x2 ya = gelu2(f32x2{a32[n][4*rq], a32[n][4*rq + 1]}), yb = gelu2(f32x2{a32[n][4*rq + 2], a32[n][4*rq + 3]});
            q1 += ya; q2 = __builtin_elementwise_fma(ya, ya, q2);
            q1 += yb; q2 = __builtin_elementwise_fma(yb, yb, q2);
            *(LAS u32x2*)(p + n*64 + rq*16) = u32x2{pack2(ya), pack2(yb)};
          }
        float t1 = q1[0] + q1[1], t2 = q2[0] + q2[1];
        t1 += __shfl_xor(t1, 32); t2 += __shfl_xor(t2, 32);
        const u32x4 rec = {__builtin_bit_cast(unsigned, t1), T, __builtin_bit_cast(unsigned, t2), T};
        if (l2 < 32) __builtin_amdgcn_raw_buffer_store_b128(rec, MKRSRC(ws + WS_STAT, 2048*256), ((pi*16 + pj)*128 + 32*w + sr)*16, 0, AUX_SC1);
        STAMP(3);
        WAITL(0);
        }
        body(IC<4>{});
        {
      int l2 = lane; OPAQUE(l2);
      const int sr = l2 & 31, h4 = (l2 >> 5)*4;
      auto pk4 = [&](int n, int rq) { return u32x2{pack2(f32x2{a32[n][4*rq], a32[n][4*rq + 1]}), pack2(f32x2{a32[n][4*rq + 2], a32[n][4*rq + 3]})}; };
        LAS char* p = lds + QB_OFF + (16 + 32*w + sr)*QB_PITCH + h4*2;
        #pragma unroll
        for (int n = 0; n < 4; n++)
          #pragma unroll
          for (int rq = 0; rq < 4; rq++) if (n < 3 || rq < 3 || h4 == 0) *(LAS u32x2*)(p + n*64 + rq*16) = pk4(n, rq);
        STAMP(4);
        WAITL(0);
        }
      }
    }
    {
      int l2 = lane; OPAQUE(l2);
      const int sr = l2 & 31, h4 = (l2 >> 5)*4;
      LAS char* p = lds + Z1B_OFF + w*IMG_W + sr*IMG_PITCH + h4*2;
      #pragma unroll
      for (int n = 0; n < 5; n++)
        #pragma unroll
        for (int rq = 0; rq < 4; rq++) if (n < 4 || rq < 2)
          *(LAS u32x2*)(p + n*64 + rq*16) = u32x2{pack2(f32x2{a32[n][4*rq], a32[n][4*rq + 1]}), pack2(f32x2{a32[n][4*rq + 2], a32[n][4*rq + 3]})};
      STAMP(5);
      WAITL(0);
    }
    BAR();
  } else {
    const int g = lane >> 4, fl = lane & 15;
    __builtin_amdgcn_s_setprio(3);
    if (isD) {
      WAITV(18); VB();
      #pragma unroll 1
      for (int q = 1; q < 6; q++) { p1_issue(); WAITV(18); VB(); }
      #pragma unroll
      for (int t = 0; t < 9; t++) { const int t_ = cv[t]; cv[t] = cq[t]; cq[t] = t_; }
      csrd = make_srd(wqkv, 576*3456); p1so = 32*(w & 1)*3456 + ((72*pj) & ~15); p1step = 64*3456;
      #pragma unroll 1
      for (int q = 6; q < 15; q++) { p1_issue(); WAITV(18); VB(); }
      #pragma unroll
      for (int t = 0; t < 9; t++) cv[t] = cq[t];
      csrd = make_srd(w_in, 576*9216); p1so = 32*(w & 1)*9216 + 144*pj*2; p1step = 64*9216;
      #pragma unroll 1
      for (int q = 15; q < 24; q++) {
        if (q == 19 && wave == 4 && pi < 15) {
          const RSRC r_halo = MKRSRC(ws + WS_HALO + (size_t)(pi*16 + pj)*4096, 4096);
          #pragma unroll
          for (int c = 0; c < 4; c++) { int ch = c*64 + lane; if (ch > 247) ch = 247;
            u32x4 v = *(LAS u32x4*)(lds + QB_OFF + 128*QB_PITCH + ch*16);
            __builtin_amdgcn_raw_buffer_store_b128(v, r_halo, ch*16, 0, AUX_SC1); }
        }
        p1_issue(); WAITV(18); VB();
        if (q == 20 && wave == 4 && lane == 0 && pi < 15) set_flag(ws, FL_HALO, pi, pj, T);
      }
      u32x4 hv[4]; unsigned hf = T;
      const bool hal = (wave == 5 && pi > 0);
      gu32* fp = (gu32*)(ws + WS_FLAGS + (size_t)(FL_HALO*16 + (pi > 0 ? pi - 1 : 0))*256) + pj;
      if (hal) asm volatile("global_load_dword %0, %1, %2 sc1" : "=v"(hf) : "v"(0), "s"(fp) : "memory");
      if (hal) WAITV(10); else WAITV(9);
      VB();
      if (hal) {
        asm volatile("s_waitcnt vmcnt(0)" : "+v"(hf) :: "memory");
        if (__builtin_amdgcn_readfirstlane(hf) != T)
          for (int spin = 0; spin < SPIN_MAX; ++spin) { if (__builtin_amdgcn_readfirstlane(__hip_atomic_load(fp, RLX_AGENT)) == T) break; __builtin_amdgcn_s_sleep(1); }
        const u32x4 s_hal = make_srd(ws + WS_HALO + (size_t)((pi - 1)*16 + pj)*4096, 4096);
        static_for<0, 4>([&](auto C) { int ch = C.value*64 + lane; if (ch > 247) ch = 247; u32x4 t;
          asm volatile("buffer_load_dwordx4 %0, %1, %2, 0 offen sc1" : "=v"(t) : "v"(ch*16), "s"(s_hal) : "memory"); hv[C.value] = t; });
      } else WAITV(0);
      VB();
      if (wave == 5) {
        if (pi > 0) asm volatile("s_waitcnt vmcnt(0)" : "+v"(hv[0]), "+v"(hv[1]), "+v"(hv[2]), "+v"(hv[3]) :: "memory");
        #pragma unroll
        for (int c = 0; c < 4; c++) { int ch = c*64 + lane; if (ch > 247) ch = 247; *(LAS u32x4*)(lds + QB_OFF + ch*16) = (pi > 0) ? hv[c] : u32x4{0, 0, 0, 0}; }
        WAITL(0);
      }
      VB();
    } else {
      const int gA = 2*(w - 2);
      {
        const u32x4 s_a = make_srd(w_sp, 2048*4096), s_b = make_srd(w_out, 2304*1152), s_c = make_srd(wo, 576*1152);
        SETM0(149504);
        const int vl = lane*16;
        #pragma unroll 1
        for (int q = 0; q < 4; q++) VB();
        #pragma unroll 1
        for (int q = 4; q < 16; q++) {
          const int k = q - 4;
          if (k < 8) { DMA(vl, s_a, b*32768 + ((w - 2)*16 + 2*k)*1024, 0); DMA(vl, s_a, b*32768 + ((w - 2)*16 + 2*k + 1)*1024, 0); }
          else if (k < 11) { DMA(vl, s_b, b*10368 + ((w - 2)*6 + 2*(k - 8))*1024, 0); DMA(vl, s_b, b*10368 + ((w - 2)*6 + 2*(k - 8) + 1)*1024, 0); }
          else { DMA(vl, s_c, b*2592 + ((w - 2)*2)*1024, 0); DMA(vl, s_c, b*2592 + ((w - 2)*2 + 1)*1024, 0); }
          VB();
        }
      }
      LAS f32x2* st = (LAS f32x2*)(lds + MISC_OFF + (w - 2)*512);
      {
        const u32x4 s_st = make_srd(ws + WS_STAT, 2048*256);
        const int vo = (pi*16*128 + 32*gA + lane)*16;
        u32x4 rec[16];
        static_for<0, 16>([&](auto C) { u32x4 t; asm volatile("buffer_load_dwordx4 %0, %1, %2, %3 offen sc1" : "=v"(t) : "v"(vo), "s"(s_st), "s"(C.value*2048) : "memory"); rec[C.value] = t; });
        WAITV(16);
        VB();
        VB();
        asm volatile("s_waitcnt vmcnt(0)" : "+v"(rec[0]), "+v"(rec[1]), "+v"(rec[2]), "+v"(rec[3]), "+v"(rec[4]), "+v"(rec[5]), "+v"(rec[6]), "+v"(rec[7]),
                          "+v"(rec[8]), "+v"(rec[9]), "+v"(rec[10]), "+v"(rec[11]), "+v"(rec[12]), "+v"(rec[13]), "+v"(rec[14]), "+v"(rec[15]) :: "memory");
        float t1 = 0.f, t2 = 0.f; bool ok = true;
        #pragma unroll
        for (int c = 0; c < 16; c++) { t1 += asf(rec[c][0]); t2 += asf(rec[c][2]); ok = ok && rec[c][1] == T && rec[c][3] == T; }
        if (!__all(ok)) {
          const RSRC r_stat = MKRSRC(ws + WS_STAT, 2048*256);
          for (int spin = 0; spin < SPIN_MAX; ++spin) {
            t1 = 0.f; t2 = 0.f; ok = true;
            #pragma unroll
            for (int c = 0; c < 16; c++) {
              u32x4 v = __builtin_amdgcn_raw_buffer_load_b128(r_stat, vo + c*2048, 0, AUX_SC1);
              t1 += asf(v[0]); t2 += asf(v[2]); ok = ok && v[1] == T && v[3] == T;
            }
            if (__all(ok)) break;
            __builtin_amdgcn_s_sleep(1);
          }
        }
        const float mean = t1*(1.f/2304.f), var = fmaxf(t2*(1.f/2304.f) - mean*mean, 0.f);
        const float rs = __builtin_amdgcn_rsqf(var + 1e-5f);
        st[lane] = f32x2{rs, -mean*rs};
        WAITL(0);
        VB();
      }
      const RSRC r_z2n = MKRSRC(ws + WS_Z2N, 16*ZJS);
      #pragma unroll 1
      for (int k = 0; k < 2; k++) {
        f32x2 r2[4], h2[4];
        #pragma unroll
        for (int d = 0; d < 4; d++) { f32x2 u0 = st[32*k + 8*g + 2*d], u1 = st[32*k + 8*g + 2*d + 1]; r2[d] = f32x2{u0[0], u1[0]}; h2[d] = f32x2{u0[1], u1[1]}; }
        LAS char* p = lds + ZB_OFF + (gA + k)*IMG_W + (8*g + ((lane >> 2) & 3))*IMG_PITCH + (lane & 3)*8;
        int zo = lane*16 + pj*ZJS + (4*pi + gA + k)*9216;
        #pragma unroll 1
        for (int j3 = 0; j3 < 3; j3++, p += 96, zo += 3072) {
          u32x4 v[3];
          #pragma unroll
          for (int u = 0; u < 3; u++) {
            s16x4 lo = __builtin_amdgcn_ds_read_tr16_b64_v4i16((LAS s16x4*)(p + u*32));
            s16x4 hi = __builtin_amdgcn_ds_read_tr16_b64_v4i16((LAS s16x4*)(p + u*32 + 4*IMG_PITCH));
            v[u] = __builtin_bit_cast(u32x4, __builtin_shufflevector(lo, hi, 0, 1, 2, 3, 4, 5, 6, 7));
          }
          #pragma unroll
          for (int u = 0; u < 3; u++) {
            u32x4 o;
            #pragma unroll
            for (int d = 0; d < 4; d++) o[d] = pack2(f32x2{opq(__builtin_fmaf(bf_lo(v[u][d]), r2[d][0], h2[d][0])), opq(__builtin_fmaf(bf_hi(v[u][d]), r2[d][1], h2[d][1]))});
            __builtin_amdgcn_raw_buffer_store_b128(o, r_z2n, zo + u*1024, 0, AUX_SC1);
          }
          VB();
        }
      }
      VB();
      VB();
    }
    STAMP(5);
    BAR();
    if (!isD) { WAITV(0); if (lane == 0) set_flag(ws, FL_Z, pj, 2*pi + (w - 2), T); }
    {
      #pragma unroll
      for (int m = 0; m < 2; m++) {
        LAS char* zx = lds + Z1B_OFF + w*IMG_W + (8*(fl >> 2) + 4*m + (fl & 3))*IMG_PITCH + g*8;
        #pragma unroll
        for (int n = 0; n < 9; n++) { u32x2 v = *(LAS u32x2*)(zx + n*32); z1p[n][2*m] = v[0]; z1p[n][2*m + 1] = v[1]; }
      }
    }
  }

  const u32x4 s_z2n = make_srd(ws + WS_Z2N + (size_t)pj*ZJS, 64*9*1024);
  const u32x4 s_wsp = make_srd(w_sp + (size_t)128*pi*2048, 128*4096);
  int vw[4];
  const int vlin = lane*16;
  auto p2_issue = [&](int s, auto ZR) {
    const int base = (s & 3)*P2_STAGE;
    if constexpr (ZR.value) {
      static_for<0, 9>([&](auto K) { SETM0(base + w*9216 + K.value*1024); DMAS(vlin, s_z2n, ((2*s + w)*9 + K.value)*1024, 0); });
    } else {
      static_for<0, 8>([&](auto K) { constexpr int k = K.value; const int wr = 2*(w - 2) + (k >> 2);
        SETM0(base + 18432 + wr*4096 + (k & 3)*1024); DMA(vw[k & 3], s_wsp, wr*32*4096 + s*128, 0); });
    }
  };
  if (wave >= 4) {
    { int l2 = lane; OPAQUE(l2);
      const int a = l2 >> 3, bb = l2 & 7;
      #pragma unroll
      for (int m = 0; m < 2; m++)
        #pragma unroll
        for (int hf = 0; hf < 2; hf++) { int rho = 8*hf + a; vw[2*m + hf] = (8*(rho >> 2) + 4*m + (rho & 3))*4096 + ((bb ^ ((rho >> 1) & 7))*16); } }
    if (w >= 2) {
      #pragma unroll 1
      for (int s = 0; s < 2; s++) p2_issue(s, IC<0>{});
    }
  }
  {
      int t2 = tid; OPAQUE(t2);
      const int u = t2 & 1, hh = (t2 >> 7) & 1, tl = ((t2 >> 1) & 63) + 64*(t2 >> 8);
      LAS char* rowp = lds + QB_OFF + (16 + tl)*QB_PITCH + 8*(pj & 1) + 36*hh;
      const int o8 = hh*4, o4 = hh ? 0 : 32;
      u32x2 qv[4]; unsigned q0;
      #pragma unroll
      for (int d = 0; d < 4; d++) qv[d] = *(LAS u32x2*)(rowp + o8 + d*8);
      q0 = *(LAS unsigned*)(rowp + o4);
      float lg[4];
      #pragma unroll
      for (int ww = 0; ww < 4; ww++) {
        LAS char* kr = rowp - (2*(4*u + ww))*QB_PITCH + 80;
        float sa = dot2(q0, *(LAS unsigned*)(kr + o4), 0.f), sb = 0.f;
        #pragma unroll
        for (int d = 0; d < 4; d++) { u32x2 kv = *(LAS u32x2*)(kr + o8 + d*8); sa = dot2(qv[d][0], kv[0], sa); sb = dot2(qv[d][1], kv[1], sb); }
        float s = (sa + sb)*0.23570226039551584f;
        if (128*pi + tl - 2*(4*u + ww) < 0) s = -1e9f;
        lg[ww] = s;
      }
      float l8[8];
      #pragma unroll
      for (int ww = 0; ww < 4; ww++) { float o = dpp_f<0xB1>(lg[ww]); l8[ww] = u ? o : lg[ww]; l8[4 + ww] = u ? lg[ww] : o; }
      float mx = l8[0];
      #pragma unroll
      for (int ww = 1; ww < 8; ww++) mx = fmaxf(mx, l8[ww]);
      float sum = 0.f;
      #pragma unroll
      for (int ww = 0; ww < 8; ww++) { l8[ww] = __builtin_amdgcn_exp2f((l8[ww] - mx)*1.4426950408889634f); sum += l8[ww]; }
      const float inv = __builtin_amdgcn_rcpf(sum);
      float olo[5], ohi[5];
      #pragma unroll
      for (int d = 0; d < 5; d++) { olo[d] = 0.f; ohi[d] = 0.f; }
      #pragma unroll
      for (int ww = 0; ww < 8; ww++) {
        const unsigned pb = pack_bf16(l8[ww]*inv, 0.f);
        const unsigned ph = pb << 16;
        LAS char* vr = rowp - (2*ww)*QB_PITCH + 160;
        u32x2 va = *(LAS u32x2*)(vr + o8 + 16*u), vb = *(LAS u32x2*)(vr + o8 + 16*u + 8); unsigned vc = *(LAS unsigned*)(vr + o4);
        olo[0] = dot2(va[0], pb, olo[0]); ohi[0] = dot2(va[0], ph, ohi[0]); olo[1] = dot2(va[1], pb, olo[1]); ohi[1] = dot2(va[1], ph, ohi[1]);
        olo[2] = dot2(vb[0], pb, olo[2]); ohi[2] = dot2(vb[0], ph, ohi[2]); olo[3] = dot2(vb[1], pb, olo[3]); ohi[3] = dot2(vb[1], ph, ohi[3]);
        olo[4] = dot2(vc, pb, olo[4]); ohi[4] = dot2(vc, ph, ohi[4]);
      }
      *(LAS u32x2*)(rowp + o8 + 16*u) = u32x2{pack_bf16(olo[0], ohi[0]), pack_bf16(olo[1], ohi[1])};
      *(LAS u32x2*)(rowp + o8 + 16*u + 8) = u32x2{pack_bf16(olo[2], ohi[2]), pack_bf16(olo[3], ohi[3])};
      if (u) *(LAS unsigned*)(rowp + o4) = pack_bf16(olo[4], ohi[4]);
      }
  WAITL(0);
  STAMP(21);
  BAR();
  STAMP(22);
  if (wave >= 4 && w < 2) { wait_flags(ws, FL_Z, pj, 32, T, lane); STAMP(13); p2_issue(0, IC<1>{}); p2_issue(1, IC<1>{}); }
  if (wave >= 4) {
    #pragma unroll
    for (int n = 0; n < 3; n++) {
      #pragma unroll
      for (int d = 0; d < 4; d++) z1p[n][d] = pack2(gelu2(f32x2{bf_lo(z1p[n][d]), bf_hi(z1p[n][d])}));
      asm volatile("" : "+v"(z1p[n][0]), "+v"(z1p[n][1]), "+v"(z1p[n][2]), "+v"(z1p[n][3]));
    }
  }
  if (wave < 4) {
    const RSRC r_o = MKRSRC(ws + WS_O, 128*OTS);
    if (tid < 252) {
      const int r0 = tid / 9, p = tid % 9;
      const int col = 36*pj + 4*p, c32 = col & 31;
      const int cpart = ((col >> 5)*64 + 16*((c32 & 15) >> 2))*16 + (c32 >> 4)*8;
      LAS char* src = lds + QB_OFF + (16 + r0)*QB_PITCH + 8*(pj & 1) + 8*p;
      #pragma unroll
      for (int it = 0; it < 5; it++) {
        const int tl = r0 + 28*it;
        if (tl < 128) {
          u32x2 v = *(LAS u32x2*)(src + it*28*QB_PITCH);
          const int r32 = tl & 31, tt = 8*pi + 2*(tl >> 5) + ((r32 >> 2) & 1), rho = 4*(r32 >> 3) + (r32 & 3);
          __builtin_amdgcn_raw_buffer_store_b64(v, r_o, tt*OTS + rho*16 + cpart, 0, AUX_SC1);
        }
      }
    }
  }
  WAITL(0);
  BAR();
  STAMP(6);

  const int p3cnt = (xcd < 2) ? 30 : 29;
  const int p3T = xcd*29 + (xcd < 2 ? xcd : 2) + rr;
  const bool p3act = rr < p3cnt;
  const int rg = p3T / 9, pc = p3T % 9;

  if (wave >= 4) {
    __builtin_amdgcn_s_setprio(0);
    auto p2_helper = [&](auto ZR) {
      constexpr int CNT = ZR.value ? 9 : 8;
      p2_issue(2, ZR); p2_issue(3, ZR);
      WAITV(3*CNT);
      VB();
      auto piece = [&](int s, auto K) {
        constexpr int k = K.value; const int base = (s & 3)*P2_STAGE;
        if constexpr (ZR.value) { SETM0(base + w*9216 + k*1024); DMAS(vlin, s_z2n, ((2*s + w)*9 + k)*1024, 0); }
        else if constexpr (k < 8) { const int wr = 2*(w - 2) + (k >> 2); SETM0(base + 18432 + wr*4096 + (k & 3)*1024); DMA(vw[k & 3], s_wsp, wr*32*4096 + s*128, 0); }
      };
      auto step = [&](int s, auto PR) {
        WAITV(2*CNT);
        VB();
        static_for<0, 9>([&](auto K) {
          piece(s + 4, K);
          if constexpr (PR.value >= 0 && K.value == 2) {
            constexpr int pr = PR.value;
            unsigned rr2 = pack2(f32x2{opq(gelu_f(opq(bf_lo(z1p[pr / 4][pr % 4])))), opq(gelu_f(opq(bf_hi(z1p[pr / 4][pr % 4]))))});
            asm volatile("" : "+v"(rr2));
            z1p[pr / 4][pr % 4] = rr2;
          }
        });
      };
      static_for<0, 24>([&](auto S) {
        step(S.value, IC<12 + S.value>{});
        if constexpr (S.value == 4) { if (wave == 4 && lane == 0) set_flag(ws, FL_O, pi, pj, T); }
      });
      #pragma unroll 1
      for (int s = 24; s < 28; s++) step(s, IC<-1>{});
      WAITV(2*CNT); VB();
      WAITV(CNT); VB();
      #pragma unroll
      for (int n = 0; n < 9; n++) *(LAS u32x4*)(lds + w*9216 + n*1024 + lane*16) = u32x4{z1p[n][0], z1p[n][1], z1p[n][2], z1p[n][3]};
      WAITV(0); WAITL(0); VB();
      VB();
    };
    if (w < 2) p2_helper(IC<1>{}); else p2_helper(IC<0>{});
    STAMP(8);
    if (p3act) {
      const u32x4 s_o = make_srd(ws + WS_O, 128*OTS), s_h = make_srd(ws + WS_H, 128*HTS);
      const u32x4 s_wo = make_srd(wo, 576*1152), s_wout = make_srd(w_out, 2304*1152);
      int vW0, vW1, vA[5];
      int l2 = lane; OPAQUE(l2);
      { const int r = l2 >> 3;
        vW0 = r*1152 + (((l2 & 7) ^ (((r >> 1) & 3) << 1))*16);
        vW1 = (r + 8)*1152 + (((l2 & 7) ^ ((((r + 8) >> 1) & 3) << 1))*16);
        #pragma unroll
        for (int m5 = 0; m5 < 5; m5++) { int tt = 5*rg + m5; if (tt > 127) tt = 127; vA[m5] = l2*16 + tt*OTS; } }
      int slot = 2;
      auto p3_w = [&](int sl, u32x4 sw, int R0, int R1) {
        const int base = sl*P3_STAGE + w*9216, s0 = R0*1152 + pc*128, s1 = R1*1152 + pc*128;
        SETM0(base + 5120); DMA(vW0, sw, s0, 0); SETM0(base + 6144); DMA(vW1, sw, s0, 0);
        SETM0(base + 7168); DMA(vW0, sw, s1, 0); SETM0(base + 8192); DMA(vW1, sw, s1, 0);
      };
      auto p3_a = [&](int sl, u32x4 sa, int ks) {
        const int base = sl*P3_STAGE + w*9216;
        static_for<0, 5>([&](auto M) { SETM0(base + M.value*1024); DMAS(vA[M.value], sa, ks*1024, 0); });
      };
      auto adv = [&]() { slot = (slot == 4) ? 0 : slot + 1; };
      auto p3_wo = [&](int u) { const int ks = 3*u + w; if (w < 3) { p3_w(slot, s_wo, 32*ks, 32*ks + 16); p3_a(slot, s_o, ks); } adv(); };
      auto g3_w = [&](int sl, int v) { const int ks = 3*v + w;
        const int R0 = (ks < 64) ? 144*(ks >> 2) + 32*(ks & 3) : 288*(ks - 64) + 128;
        p3_w(sl, s_wout, R0, R0 + ((ks < 64) ? 16 : 144)); };
      auto p3_g3 = [&](int v) { if (w < 3) { g3_w(slot, v); p3_a(slot, s_h, 3*v + w); } adv(); };
      const int p0 = (5*rg) >> 3, p1 = ((5*rg + 4 > 127 ? 127 : 5*rg + 4)) >> 3;
      if (w < 3) { wait_flags(ws, FL_O, p0, 16, T, lane); wait_flags(ws, FL_O, p1, 16, T, lane); }
      STAMP(24);
      #pragma unroll 1
      for (int u = 0; u < 3; u++) p3_wo(u);
      BAR();
      p3_wo(3); p3_wo(4);
      WAITV(27);
      STAMP(26);
      BAR();
      WAITV(18); BAR();
      p3_wo(5); WAITV(18); BAR();
      WAITV(9); BAR();
      WAITV(0); BAR();
      BAR();
      if (w < 3) { g3_w(3, 0); g3_w(4, 1); g3_w(0, 2); g3_w(1, 3); }
      #pragma unroll
      for (int m5 = 0; m5 < 5; m5++) { int tt = 5*rg + m5; if (tt > 127) tt = 127; vA[m5] = l2*16 + tt*HTS; }
      if (w < 3) { wait_flags(ws, FL_H, p0, 16, T, lane); wait_flags(ws, FL_H, p1, 16, T, lane); }
      STAMP(27);
      RSTAMP(31);
      if (w < 3) { p3_a(3, s_h, w); p3_a(4, s_h, 3 + w); p3_a(0, s_h, 6 + w); p3_a(1, s_h, 9 + w); }
      slot = 2;
      WAITV(10); BAR();
      p3_g3(4); WAITV(14); BAR();
      #pragma unroll 1
      for (int u = 7; u < 26; u++) {
        p3_g3(u - 2);
        WAITV(18); BAR();
      }
      WAITV(9); BAR();
      WAITV(0); BAR();
      BAR();
    }
  } else {
    int l2 = lane; OPAQUE(l2);
    const int g = l2 >> 4, fl = l2 & 15;
    const int zoff = l2*16;
    const int woff0 = 18432 + (4*w + (fl >> 3))*1024 + (fl & 7)*128 + ((g ^ ((fl >> 1) & 7))*16);
    float bs[2];
    #pragma unroll
    for (int m = 0; m < 2; m++) bs[m] = bf_lo(b_sp[128*pi + 32*w + 8*(fl >> 2) + 4*m + (fl & 3)]);
    #pragma unroll
    for (int n = 0; n < 9; n++) { acc[n][0] = f32x4{bs[0], bs[0], bs[0], bs[0]}; acc[n][1] = f32x4{bs[1], bs[1], bs[1], bs[1]}; }
    bf16x8 z0[9], w0[2], z1[9], w1[2];
    auto rd = [&](bf16x8 (&z)[9], bf16x8 (&wv)[2], int s, int kk) {
      const int base = (s & 3)*P2_STAGE;
      LAS char* pw = lds + base + (woff0 ^ (kk*64));
      LAS char* pz = lds + base + kk*9216 + zoff;
      wv[0] = *(LAS bf16x8*)pw; wv[1] = *(LAS bf16x8*)(pw + 2048);
      #pragma unroll
      for (int n = 0; n < 9; n++) z[n] = *(LAS bf16x8*)(pz + n*1024);
    };
    auto mm = [&](bf16x8 (&z)[9], bf16x8 (&wv)[2]) {
      #pragma unroll
      for (int n = 0; n < 9; n++) { acc[n][0] = MFMA(z[n], wv[0], acc[n][0]); acc[n][1] = MFMA(z[n], wv[1], acc[n][1]); }
      #pragma unroll
      for (int q = 0; q < 11; q++) { SG(0x8, 1); SG(0x100, 1); }
      SG(0x8, 7);
    };
    BAR();
    rd(z0, w0, 0, 0);
    CWAITL0(); SB();
    #pragma unroll 1
    for (int s = 0; s < 32; s++) {
      CWAITL0(); SB();
      rd(z1, w1, s, 1);
      mm(z0, w0);
      SB(); CWAITL0(); PIN_ACC(acc);
      if (s == 4) WAITV(0);
      BAR(); SB();
      rd(z0, w0, (s + 1 < 32 ? s + 1 : s), 0);
      mm(z1, w1);
      SB();
    }
    STAMP(8);
    {
      const RSRC r_h = MKRSRC(ws + WS_H, 128*HTS);
      u32x4 z1g[9];
      #pragma unroll
      for (int n = 0; n < 9; n++) z1g[n] = *(LAS u32x4*)(lds + w*9216 + n*1024 + zoff);
      #pragma unroll
      for (int m = 0; m < 2; m++) {
        const int tt = 8*pi + 2*w + m;
        unsigned hp[9][2];
        #pragma unroll
        for (int n = 0; n < 9; n++) {
          hp[n][0] = pack2(f32x2{bf_lo(z1g[n][2*m]), bf_hi(z1g[n][2*m])}*f32x2{acc[n][m][0], acc[n][m][1]});
          hp[n][1] = pack2(f32x2{bf_lo(z1g[n][2*m + 1]), bf_hi(z1g[n][2*m + 1])}*f32x2{acc[n][m][2], acc[n][m][3]});
        }
        #pragma unroll
        for (int q = 0; q < 4; q++)
          __builtin_amdgcn_raw_buffer_store_b128(u32x4{hp[2*q][0], hp[2*q][1], hp[2*q + 1][0], hp[2*q + 1][1]}, r_h, zoff + tt*HTS + (4*pj + q)*1024, 0, AUX_SC1);
        __builtin_amdgcn_raw_buffer_store_b64(u32x2{hp[8][0], hp[8][1]}, r_h, zoff + (pj & 1)*8 + tt*HTS + (64 + (pj >> 1))*1024, 0, AUX_SC1);
      }
    }
    STAMP(19);
    RSTAMP(29);
    if (p3act) {
      WAITL(0);
      BAR();
      f32x4 aS[5], aC[5];
      u32x2 xv[5];
      const int q4 = (l2 >> 2) & 3, p4 = l2 & 3, r0 = 4*g + q4;
      const int woff = 5120 + r0*128 + (((2*w + (p4 >> 1)) ^ (((r0 >> 1) & 3) << 1))*16) + (p4 & 1)*8;
      bf16x8 hA[3][5], wA[3], hB[3][5], wB[3];
      int slot = 2;
      auto rd3 = [&](bf16x8 (&h)[3][5], bf16x8 (&wv)[3]) {
        LAS char* p = lds + slot*P3_STAGE;
        #pragma unroll
        for (int kk = 0; kk < 3; kk++) {
          s16x4 lo = __builtin_amdgcn_ds_read_tr16_b64_v4i16((LAS s16x4*)(p + kk*9216 + woff));
          s16x4 hi = __builtin_amdgcn_ds_read_tr16_b64_v4i16((LAS s16x4*)(p + kk*9216 + woff + 2048));
          wv[kk] = __builtin_shufflevector(lo, hi, 0, 1, 2, 3, 4, 5, 6, 7);
          #pragma unroll
          for (int m5 = 0; m5 < 5; m5++) h[kk][m5] = *(LAS bf16x8*)(p + kk*9216 + m5*1024 + zoff);
        }
        slot = (slot == 4) ? 0 : slot + 1;
      };
      auto mm3 = [&](bf16x8 (&h)[3][5], bf16x8 (&wv)[3]) {
        #pragma unroll
        for (int kk = 0; kk < 3; kk++)
          #pragma unroll
          for (int m5 = 0; m5 < 5; m5++) aC[m5] = MFMA(wv[kk], h[kk][m5], aC[m5]);
        #pragma unroll
        for (int q = 0; q < 15; q++) { SG(0x8, 1); SG(0x100, 1); }
        SG(0x100, 6);
      };
      #pragma unroll
      for (int m5 = 0; m5 < 5; m5++) {
        int tt = 5*rg + m5; if (tt > 127) tt = 127;
        const int row = 32*(tt >> 1) + 8*(fl >> 2) + 4*(tt & 1) + (fl & 3);
        xv[m5] = *(const u32x2*)(x + (size_t)row*D_ + 64*pc + 16*w + 4*g);
      }
      BAR();
      #pragma unroll 1
      for (int ph = 0; ph < 2; ph++) {
        #pragma unroll
        for (int m5 = 0; m5 < 5; m5++) aC[m5] = f32x4{0,0,0,0};
        rd3(hA, wA); SB();
        const int nit = ph ? 12 : 3;
        #pragma unroll 1
        for (int it = 0; it < nit; it++) {
          CWAITL0(); SB(); rd3(hB, wB); mm3(hA, wA); SB(); PIN5(aC); BAR(); SB();
          CWAITL0(); SB(); rd3(hA, wA); mm3(hB, wB); SB(); PIN5(aC);
          if (ph == 0 && it == 1) { WAITV(0); RSTAMP(30); }
          if (ph == 0 || it < 11) BAR();
          SB();
          if (ph == 0 && it == 1 && tid == 0) set_flag(ws, FL_H, pi, pj, T);
        }
        if (ph == 0) {
          #pragma unroll
          for (int m5 = 0; m5 < 5; m5++) aS[m5] = aC[m5];
          slot = 3;
          STAMP(10);
        }
      }
      STAMP(11);
      #pragma unroll
      for (int m5 = 0; m5 < 5; m5++) {
        const int tt = 5*rg + m5;
        if (tt < 128) {
          const int row = 32*(tt >> 1) + 8*(fl >> 2) + 4*(tt & 1) + (fl & 3);
          const size_t o = (size_t)row*D_ + 64*pc + 16*w + 4*g;
          float r[4];
          #pragma unroll
          for (int e = 0; e < 4; e++) {
            float bv = aC[m5][e], sv = aS[m5][e];
            float sl = bv*__builtin_amdgcn_rcpf(1.f + __builtin_amdgcn_exp2f(-1.4426950408889634f*bv));
            unsigned xw = xv[m5][e >> 1];
            float xx = (e & 1) ? bf_hi(xw) : bf_lo(xw);
            r[e] = __builtin_fmaf(sv, sl, xx);
          }
          *(u32x2*)(out + o) = u32x2{pack_bf16(r[0], r[1]), pack_bf16(r[2], r[3])};
        }
      }
    } else {
      WAITV(0);
      BAR();
      if (tid == 0) set_flag(ws, FL_H, pi, pj, T);
    }
    if (b == 0 && tid == 0) __hip_atomic_store((gu32*)ws, T, RLX_AGENT);
    STAMP(12);
  }
  TDUMP();
  asm volatile(".Lpf_end:");
}

extern "C" void kernel_launch(void* const* d_in, const int* in_sizes, int n_in, void* d_out, int out_size, void* d_ws, size_t ws_size, hipStream_t stream) {
  (void)hipFuncSetAttribute((const void*)fused_kernel, hipFuncAttributeMaxDynamicSharedMemorySize, LDS_BYTES);
  fused_kernel<<<256, 512, LDS_BYTES, stream>>>((const u16*)d_in[0], (const u16*)d_in[1], (const u16*)d_in[2], (const u16*)d_in[4],
      (const u16*)d_in[5], (const u16*)d_in[6], (const u16*)d_in[7], (u16*)d_out, (char*)d_ws);
}
```

```cpp
#include <hip/hip_runtime.h>
#include <stdint.h>

typedef unsigned short u16;
typedef __attribute__((ext_vector_type(8))) short bf16x8;
typedef __attribute__((ext_vector_type(4))) short s16x4;
typedef __attribute__((ext_vector_type(4))) float f32x4;
typedef __attribute__((ext_vector_type(16))) float f32x16;
typedef __attribute__((ext_vector_type(2))) float f32x2;
typedef __attribute__((ext_vector_type(4))) unsigned u32x4;
typedef __attribute__((ext_vector_type(2))) unsigned u32x2;
typedef __attribute__((ext_vector_type(2))) __bf16 bf16x2;
#define GAS __attribute__((address_space(1)))
#define LAS __attribute__((address_space(3)))
typedef GAS unsigned gu32;
#define RLX_AGENT __ATOMIC_RELAXED, __HIP_MEMORY_SCOPE_AGENT
#define BAR() asm volatile("s_barrier" ::: "memory")
#define SB() __builtin_amdgcn_sched_barrier(0)
#define SG(mask, n) __builtin_amdgcn_sched_group_barrier(mask, n, 0)
#define WAITV(n) asm volatile("s_waitcnt vmcnt(%0)" :: "i"(n) : "memory")
#define WAITL(n) asm volatile("s_waitcnt lgkmcnt(%0)" :: "i"(n) : "memory")
#define CWAITL0() __builtin_amdgcn_s_waitcnt(0xC07F)
#define RSRC __amdgpu_buffer_rsrc_t
#define MKRSRC(p, bytes) __builtin_amdgcn_make_buffer_rsrc((void*)(p), 0, (bytes), 0x00020000)
#define AUX_SC1 16
#define OPAQUE(x) asm volatile("" : "+v"(x))

constexpr int D_ = 576, FH = 2304;
constexpr size_t WS_FLAGS = 4096;
constexpr size_t WS_STAT  = 32768;
constexpr size_t WS_HALO  = WS_STAT + 2048*256;
constexpr int ZJS = 64*9*1024 + 256, HTS = 72*1024 + 256, OTS = 18*1024 + 256;
constexpr size_t WS_Z2N   = WS_HALO + 16*16*4096;
constexpr size_t WS_H     = WS_Z2N + (size_t)16*ZJS;
constexpr size_t WS_O     = WS_H + (size_t)128*HTS;
constexpr int FL_Z = 1, FL_HALO = 2, FL_O = 3, FL_H = 4;
constexpr int LDS_BYTES = 163840;
constexpr int P1_STAGE = 18432, P1_NS = 5;
constexpr int XCH_OFF = 4*P1_STAGE;
constexpr int ZB_OFF = P1_NS*P1_STAGE;
constexpr int QB_PITCH = 248;
constexpr int IMG_PITCH = 296, IMG_W = 32*IMG_PITCH;
constexpr int QB_OFF = ZB_OFF + 4*IMG_W - 16*QB_PITCH;
constexpr int MISC_OFF = QB_OFF + 144*QB_PITCH;
constexpr int Z1B_OFF = 4*P1_STAGE;
constexpr int P2_STAGE = 34816;
constexpr int P3_STAGE = 27648;
constexpr int SPIN_MAX = 1 << 18;

template<int N> struct IC { static constexpr int value = N; };
template<int B, int E, class F> __device__ __forceinline__ void static_for(F&& f) {
  if constexpr (B < E) { f(IC<B>{}); static_for<B + 1, E>(f); }
}

__device__ __forceinline__ unsigned pack2(f32x2 v) { return __builtin_bit_cast(unsigned, __builtin_convertvector(v, bf16x2)); }
__device__ __forceinline__ float opq(float x) { asm("" : "+v"(x)); return x; }
__device__ __forceinline__ unsigned pack_bf16(float a, float b) { return pack2(f32x2{a, b}); }
__device__ __forceinline__ float asf(unsigned u) { return __builtin_bit_cast(float, u); }
__device__ __forceinline__ float bf_lo(unsigned u) { return __builtin_bit_cast(float, u << 16); }
__device__ __forceinline__ float bf_hi(unsigned u) { return __builtin_bit_cast(float, u & 0xffff0000u); }
__device__ __forceinline__ float dot2(unsigned a, unsigned b, float c) {
  return __builtin_amdgcn_fdot2_f32_bf16(__builtin_bit_cast(bf16x2, a), __builtin_bit_cast(bf16x2, b), c, false);
}
__device__ __forceinline__ float gelu_f(float x) {
  const float c1 = -2.302208198f, c2 = -0.1029432397f;
  float t = __builtin_fmaf(x * x, c2, c1);
  float e = __builtin_amdgcn_exp2f(x * t);
  return x * __builtin_amdgcn_rcpf(1.f + e);
}
__device__ __forceinline__ f32x2 gelu2(f32x2 x) {
  const f32x2 c1 = {-2.302208198f, -2.302208198f}, c2 = {-0.1029432397f, -0.1029432397f}, one = {1.f, 1.f};
  f32x2 t = __builtin_elementwise_fma(x * x, c2, c1);
  f32x2 a = x * t;
  f32x2 e = {__builtin_amdgcn_exp2f(a[0]), __builtin_amdgcn_exp2f(a[1])};
  f32x2 d = e + one;
  f32x2 r = {__builtin_amdgcn_rcpf(d[0]), __builtin_amdgcn_rcpf(d[1])};
  return x * r;
}
template<int CTRL> __device__ __forceinline__ float dpp_f(float v) {
  return __builtin_bit_cast(float, __builtin_amdgcn_update_dpp(0, __builtin_bit_cast(int, v), CTRL, 0xf, 0xf, true));
}
__device__ __forceinline__ float row16_allsum(float v) {
  v += dpp_f<0x128>(v); v += dpp_f<0x124>(v); v += dpp_f<0x122>(v); v += dpp_f<0x121>(v); return v;
}
__device__ __forceinline__ bool poll_flags(const char* ws, int group, int idx, int nflags, unsigned T, int lane) {
  gu32* p = (gu32*)(ws + WS_FLAGS + (size_t)(group*16 + idx)*256) + (lane & (nflags - 1));
  unsigned v = __hip_atomic_load(p, RLX_AGENT);
  return __all(v == T);
}
__device__ __forceinline__ void wait_flags(const char* ws, int group, int idx, int nflags, unsigned T, int lane) {
  for (int spin = 0; spin < SPIN_MAX; ++spin) {
    if (poll_flags(ws, group, idx, nflags, T, lane)) break;
    __builtin_amdgcn_s_sleep(1);
  }
}
__device__ __forceinline__ void set_flag(char* ws, int group, int idx, int k, unsigned T) {
  __hip_atomic_store((gu32*)(ws + WS_FLAGS + (size_t)(group*16 + idx)*256) + k, T, RLX_AGENT);
}
__device__ __forceinline__ int kperm(int r) { return 8*(r >> 3) + 4*(r & 1) + ((r >> 1) & 3); }
__device__ __forceinline__ u32x4 make_srd(const void* p, unsigned bytes) {
  unsigned long long a = (unsigned long long)p;
  return u32x4{(unsigned)a, (unsigned)(a >> 32) & 0xffffu, bytes, 0x00020000u};
}
__device__ __forceinline__ void SETM0(int v) { asm volatile("s_mov_b32 m0, %0\n\ts_nop 0" :: "s"(v) : "memory"); }
template<int OFF> __device__ __forceinline__ void dma_o(int voff, u32x4 srd, int soff) {
  asm volatile("buffer_load_dwordx4 %0, %1, %2 offen offset:%3 lds" :: "v"(voff), "s"(srd), "s"(soff), "i"(OFF) : "memory");
}
template<int OFF> __device__ __forceinline__ void dma_s(int voff, u32x4 srd, int soff) {
  asm volatile("buffer_load_dwordx4 %0, %1, %2 offen offset:%3 sc1 lds" :: "v"(voff), "s"(srd), "s"(soff), "i"(OFF) : "memory");
}
#define DMA(voff, srd, soff, OFF) dma_o<OFF>((voff), (srd), (soff))
#define DMAS(voff, srd, soff, OFF) dma_s<OFF>((voff), (srd), (soff))

#define PIN4(a) asm volatile("" : "+v"(a))
#define PIN_ACC(acc) asm volatile("" : "+v"(acc[0][0]), "+v"(acc[0][1]), "+v"(acc[1][0]), "+v"(acc[1][1]), "+v"(acc[2][0]), "+v"(acc[2][1]), \
  "+v"(acc[3][0]), "+v"(acc[3][1]), "+v"(acc[4][0]), "+v"(acc[4][1]), "+v"(acc[5][0]), "+v"(acc[5][1]), "+v"(acc[6][0]), "+v"(acc[6][1]), \
  "+v"(acc[7][0]), "+v"(acc[7][1]), "+v"(acc[8][0]), "+v"(acc[8][1]))
#define PIN5(a) asm volatile("" : "+v"(a[0]), "+v"(a[1]), "+v"(a[2]), "+v"(a[3]), "+v"(a[4]))
#define MFMA32(a, b, c) __builtin_amdgcn_mfma_f32_32x32x16_bf16((a), (b), (c), 0, 0, 0)
#define MFMA(a, b, c) __builtin_amdgcn_mfma_f32_16x16x32_bf16((a), (b), (c), 0, 0, 0)


extern "C" __global__ __launch_bounds__(512, 2) void fused_kernel(const u16* __restrict__ x, const u16* __restrict__ wqkv, const u16* __restrict__ wo,
    const u16* __restrict__ w_in, const u16* __restrict__ w_sp, const u16* __restrict__ b_sp, const u16* __restrict__ w_out,
    u16* __restrict__ out, char* __restrict__ ws)
{
  extern __shared__ __attribute__((aligned(16))) char smem[];
  LAS char* lds = (LAS char*)smem;
  const int tid = threadIdx.x, lane = tid & 63;
  const int wave = __builtin_amdgcn_readfirstlane(tid >> 6);
  const int w = wave & 3;
  const int b = blockIdx.x, xcd = b & 7, rr = b >> 3;
  const int pi = 4*(xcd & 3) + (rr & 3), pj = 8*(xcd >> 2) + (rr >> 2);
  if (wave == 7) {
    unsigned long long pc; unsigned off, sz;
    asm volatile("s_getpc_b64 %0\n.Lpf_here:\n\ts_mov_b32 %1, .Lpf_here-fused_kernel\n\ts_mov_b32 %2, .Lpf_end-fused_kernel" : "=s"(pc), "=s"(off), "=s"(sz));
    pc -= off;
    const u32x4 s_code = u32x4{(unsigned)pc, (unsigned)(pc >> 32) & 0xffffu, sz, 0x00020000u};
    const int per = 2048;
    SETM0(149504); DMA(lane*16, s_code, rr*per, 0); DMA(lane*16, s_code, rr*per, 1024);
  }
  unsigned T;

  int cv[9];
  u32x4 csrd = make_srd(w_in, 576*9216);
  int p1dst = (w & 1)*9216, p1so = 32*(w & 1)*9216 + (FH + 144*pj)*2, p1step = 64*9216;
  int cq[9];
  auto p1_offsets = [&](auto QKV, int (&dst)[9]) {
    int l3 = lane; OPAQUE(l3);
    int r = l3 / 18, ch = l3 % 18;
    #pragma unroll
    for (int t = 0; t < 9; t++) {
      const int kp = kperm(r);
      dst[t] = (QKV.value ? kp*3456 + (ch < 15 ? (ch / 5)*1152 + (ch % 5)*16 : 0) : kp*9216 + ch*16) - (t & 3)*1024;
      r += 3; ch += 10; if (ch >= 18) { ch -= 18; r += 1; }
    }
  };
  auto p1_issue = [&]() {
    SETM0(p1dst); DMA(cv[0], csrd, p1so, 0); DMA(cv[1], csrd, p1so, 1024); DMA(cv[2], csrd, p1so, 2048); DMA(cv[3], csrd, p1so, 3072);
    SETM0(p1dst + 4096); DMA(cv[4], csrd, p1so, 0); DMA(cv[5], csrd, p1so, 1024); DMA(cv[6], csrd, p1so, 2048); DMA(cv[7], csrd, p1so, 3072);
    SETM0(p1dst + 8192); DMA(cv[8], csrd, p1so, 0);
    p1dst = (p1dst >= 4*P1_STAGE) ? p1dst - 4*P1_STAGE : p1dst + P1_STAGE; p1so += p1step;
  };
  const bool isD = (wave == 4 || wave == 5);

  if (wave >= 4) __builtin_amdgcn_s_setprio(3);
  const int mt = wave >> 2;
  bf16x8 xn[36];
  {
    const u32x4 s_x = make_srd(x + (size_t)(128*pi + 32*w)*D_, 32*1152);
    const int a = lane >> 2, bq = lane & 3;
    unsigned tagv;
    asm volatile("global_load_dword %0, %1, %2 sc1" : "=v"(tagv) : "v"(0), "s"(ws) : "memory");
    u32x4 raw[18];
    {
      const int vx = (16*mt + a)*1152 + bq*16;
      static_for<0, 18>([&](auto C) { u32x4 t; asm volatile("buffer_load_dwordx4 %0, %1, %2, 0 offen offset:%3" : "=v"(t) : "v"(vx), "s"(s_x), "i"(C.value*64) : "memory"); raw[C.value] = t; });
    }
    if (isD) { p1_offsets(IC<0>{}, cv); p1_issue(); }
    if (isD) WAITV(9); else WAITV(0);
    asm volatile("" : "+v"(tagv));
    T = (unsigned)__builtin_amdgcn_readfirstlane(tagv) + 1u;
    asm volatile("" : "+v"(raw[0]), "+v"(raw[1]), "+v"(raw[2]), "+v"(raw[3]), "+v"(raw[4]), "+v"(raw[5]), "+v"(raw[6]), "+v"(raw[7]), "+v"(raw[8]),
                      "+v"(raw[9]), "+v"(raw[10]), "+v"(raw[11]), "+v"(raw[12]), "+v"(raw[13]), "+v"(raw[14]), "+v"(raw[15]), "+v"(raw[16]), "+v"(raw[17]));
    float s1 = 0.f, s2 = 0.f, s3 = 0.f, s4 = 0.f;
    unsigned ones = 0x3f803f80u; OPAQUE(ones);
    #pragma unroll
    for (int c = 0; c < 18; c++) {
      s1 = dot2(raw[c][0], ones, s1); s2 = dot2(raw[c][0], raw[c][0], s2); s3 = dot2(raw[c][1], ones, s3); s4 = dot2(raw[c][1], raw[c][1], s4);
      s1 = dot2(raw[c][2], ones, s1); s2 = dot2(raw[c][2], raw[c][2], s2); s3 = dot2(raw[c][3], ones, s3); s4 = dot2(raw[c][3], raw[c][3], s4);
    }
    s1 += s3; s2 += s4;
    s1 += dpp_f<0xB1>(s1); s1 += dpp_f<0x4E>(s1);
    s2 += dpp_f<0xB1>(s2); s2 += dpp_f<0x4E>(s2);
    const float mean = s1*(1.f/576.f), var = fmaxf(s2*(1.f/576.f) - mean*mean, 0.f);
    const float rs = __builtin_amdgcn_rsqf(var + 1e-5f), sh = -mean*rs;
    const f32x2 rs2 = {rs, rs}, sh2 = {sh, sh};
    int xw = XCH_OFF + w*18432 + (bq >> 1)*1024 + (32*(bq & 1) + ((16*mt + a) ^ (2*bq)))*16;
    int xr0 = XCH_OFF + w*18432 + (32*(lane >> 5) + ((lane & 31) ^ (2*(lane >> 5))))*16;
    int xr1 = XCH_OFF + w*18432 + 1024 + (32*(lane >> 5) + ((lane & 31) ^ (4 + 2*(lane >> 5))))*16;
    OPAQUE(xw); OPAQUE(xr0); OPAQUE(xr1);
    u32x4 xo[18];
    #pragma unroll
    for (int c = 0; c < 18; c++)
      #pragma unroll
      for (int d = 0; d < 4; d++) xo[c][d] = pack2(__builtin_elementwise_fma(f32x2{bf_lo(raw[c][d]), bf_hi(raw[c][d])}, rs2, sh2));
    #pragma unroll
    for (int c = 0; c < 9; c++) *(LAS u32x4*)(lds + xw + c*2048) = xo[c];
    WAITL(0);
    if (wave >= 6) {
      const u32x4 s_a = make_srd(w_in, 576*9216), s_b = make_srd(wqkv, 576*3456);
      SETM0(149504);
      const int vl = lane*16;
      #pragma unroll
      for (int k = 0; k < 11; k++) DMA(vl, s_a, b*20736 + ((w - 2)*11 + k)*1024, 0);
      #pragma unroll
      for (int k = 0; k < 4; k++) DMA(vl, s_b, b*7776 + ((w - 2)*4 + k)*1024, 0);
    }
    BAR();
    if (isD) p1_issue();
    if (wave < 4) {
      #pragma unroll
      for (int c = 0; c < 9; c++) { xn[2*c] = *(LAS bf16x8*)(lds + xr0 + c*2048); xn[2*c + 1] = *(LAS bf16x8*)(lds + xr1 + c*2048); }
      WAITL(0);
    }
    BAR();
    #pragma unroll
    for (int c = 0; c < 9; c++) *(LAS u32x4*)(lds + xw + c*2048) = xo[9 + c];
    if (isD) WAITV(9);
    WAITL(0);
    BAR();
    if (isD) { p1_issue(); p1_issue(); p1_offsets(IC<1>{}, cq); }
    if (wave < 4) {
      #pragma unroll
      for (int c = 0; c < 9; c++) { xn[18 + 2*c] = *(LAS bf16x8*)(lds + xr0 + c*2048); xn[18 + 2*c + 1] = *(LAS bf16x8*)(lds + xr1 + c*2048); }
    }
  }

  unsigned z1p[9][4];
  f32x4 acc[9][2];

  if (wave < 4) {
    constexpr int D = 3, NB = 4;
    int wl = (8*(lane >> 5) + 2*((lane >> 2) & 3))*288 + ((lane >> 4) & 1)*32 + (lane & 3)*8;
    int slot_off = 0;
    f32x16 a32[5];
    auto body = [&](auto NTC) {
      constexpr int NT = NTC.value, FS = 4*NT;
      bf16x8 wf[NB];
      int wc = wl + slot_off, wn;
      OPAQUE(wc);
      auto rdw = [&](auto G, int base) {
        constexpr int i = G.value % FS, k16 = i / NT, n = i % NT;
        LAS char* p = lds + base + (k16 >> 1)*9216 + (k16 & 1)*16*288 + n*64;
        s16x4 lo = __builtin_amdgcn_ds_read_tr16_b64_v4i16((LAS s16x4*)p);
        s16x4 hi = __builtin_amdgcn_ds_read_tr16_b64_v4i16((LAS s16x4*)(p + 288));
        wf[G.value % NB] = __builtin_shufflevector(lo, hi, 0, 1, 2, 3, 4, 5, 6, 7);
      };
      #pragma unroll
      for (int n = 0; n < NT; n++)
        #pragma unroll
        for (int e = 0; e < 16; e++) a32[n][e] = 0.f;
      static_for<0, D>([&](auto G) { rdw(G, wc); });
      SB();
            static_for<0, 9*FS>([&](auto G) {
        constexpr int st = G.value / FS, i = G.value % FS, k16 = i / NT, n = i % NT;
        if constexpr (i == 0) { slot_off = (slot_off >= 4*P1_STAGE) ? 0 : slot_off + P1_STAGE; wn = wl + slot_off; OPAQUE(wn); }
        if constexpr (i == FS - D) { SB(); if constexpr (NT == 5) PIN5(a32); else asm volatile("" : "+v"(a32[0]), "+v"(a32[1]), "+v"(a32[2]), "+v"(a32[3]));
          BAR(); SB(); }
        a32[n] = MFMA32(wf[G.value % NB], xn[4*st + k16], a32[n]);
        if constexpr (G.value + D < 9*FS) { if constexpr (i + D < FS) rdw(IC<G.value + D>{}, wc); else rdw(IC<G.value + D>{}, wn); }
        SG(0x8, 1); SG(0x100, 2);
        SB();
        if constexpr (i == FS - 1) wc = wn;
      });
      if constexpr (NT == 5) PIN5(a32); else asm volatile("" : "+v"(a32[0]), "+v"(a32[1]), "+v"(a32[2]), "+v"(a32[3]));
    };
    #pragma unroll 1
    for (int rep = 0; rep < 2; rep++) {
      asm volatile("" : "+s"(rep));
      body(IC<5>{});
      if (rep == 0) {
        {
      int l2 = lane; OPAQUE(l2);
      const int sr = l2 & 31, h4 = (l2 >> 5)*4;
      auto pk4 = [&](int n, int rq) { return u32x2{pack2(f32x2{a32[n][4*rq], a32[n][4*rq + 1]}), pack2(f32x2{a32[n][4*rq + 2], a32[n][4*rq + 3]})}; };
        f32x2 q1 = {0.f, 0.f}, q2 = {0.f, 0.f};
        LAS char* p = lds + ZB_OFF + w*IMG_W + sr*IMG_PITCH + h4*2;
        #pragma unroll
        for (int n = 0; n < 5; n++)
          #pragma unroll
          for (int rq = 0; rq < 4; rq++) if (n < 4 || rq < 2) {
            const f32x2 ya = gelu2(f32x2{a32[n][4*rq], a32[n][4*rq + 1]}), yb = gelu2(f32x2{a32[n][4*rq + 2], a32[n][4*rq + 3]});
            q1 += ya; q2 = __builtin_elementwise_fma(ya, ya, q2);
            q1 += yb; q2 = __builtin_elementwise_fma(yb, yb, q2);
            *(LAS u32x2*)(p + n*64 + rq*16) = u32x2{pack2(ya), pack2(yb)};
          }
        float t1 = q1[0] + q1[1], t2 = q2[0] + q2[1];
        t1 += __shfl_xor(t1, 32); t2 += __shfl_xor(t2, 32);
        const u32x4 rec = {__builtin_bit_cast(unsigned, t1), T, __builtin_bit_cast(unsigned, t2), T};
        if (l2 < 32) __builtin_amdgcn_raw_buffer_store_b128(rec, MKRSRC(ws + WS_STAT, 2048*256), ((pi*16 + pj)*128 + 32*w + sr)*16, 0, AUX_SC1);
        WAITL(0);
        }
        body(IC<4>{});
        {
      int l2 = lane; OPAQUE(l2);
      const int sr = l2 & 31, h4 = (l2 >> 5)*4;
      auto pk4 = [&](int n, int rq) { return u32x2{pack2(f32x2{a32[n][4*rq], a32[n][4*rq + 1]}), pack2(f32x2{a32[n][4*rq + 2], a32[n][4*rq + 3]})}; };
        LAS char* p = lds + QB_OFF + (16 + 32*w + sr)*QB_PITCH + h4*2;
        #pragma unroll
        for (int n = 0; n < 4; n++)
          #pragma unroll
          for (int rq = 0; rq < 4; rq++) if (n < 3 || rq < 3 || h4 == 0) *(LAS u32x2*)(p + n*64 + rq*16) = pk4(n, rq);
        WAITL(0);
        }
      }
    }
    {
      int l2 = lane; OPAQUE(l2);
      const int sr = l2 & 31, h4 = (l2 >> 5)*4;
      LAS char* p = lds + Z1B_OFF + w*IMG_W + sr*IMG_PITCH + h4*2;
      #pragma unroll
      for (int n = 0; n < 5; n++)
        #pragma unroll
        for (int rq = 0; rq < 4; rq++) if (n < 4 || rq < 2)
          *(LAS u32x2*)(p + n*64 + rq*16) = u32x2{pack2(f32x2{a32[n][4*rq], a32[n][4*rq + 1]}), pack2(f32x2{a32[n][4*rq + 2], a32[n][4*rq + 3]})};
      WAITL(0);
    }
    BAR();
  } else {
    const int g = lane >> 4, fl = lane & 15;
    __builtin_amdgcn_s_setprio(3);
    if (isD) {
      WAITV(18); BAR();
      #pragma unroll 1
      for (int q = 1; q < 6; q++) { p1_issue(); WAITV(18); BAR(); }
      #pragma unroll
      for (int t = 0; t < 9; t++) { const int t_ = cv[t]; cv[t] = cq[t]; cq[t] = t_; }
      csrd = make_srd(wqkv, 576*3456); p1so = 32*(w & 1)*3456 + ((72*pj) & ~15); p1step = 64*3456;
      #pragma unroll 1
      for (int q = 6; q < 15; q++) { p1_issue(); WAITV(18); BAR(); }
      #pragma unroll
      for (int t = 0; t < 9; t++) cv[t] = cq[t];
      csrd = make_srd(w_in, 576*9216); p1so = 32*(w & 1)*9216 + 144*pj*2; p1step = 64*9216;
      #pragma unroll 1
      for (int q = 15; q < 24; q++) {
        if (q == 19 && wave == 4 && pi < 15) {
          const RSRC r_halo = MKRSRC(ws + WS_HALO + (size_t)(pi*16 + pj)*4096, 4096);
          #pragma unroll
          for (int c = 0; c < 4; c++) { int ch = c*64 + lane; if (ch > 247) ch = 247;
            u32x4 v = *(LAS u32x4*)(lds + QB_OFF + 128*QB_PITCH + ch*16);
            __builtin_amdgcn_raw_buffer_store_b128(v, r_halo, ch*16, 0, AUX_SC1); }
        }
        p1_issue(); WAITV(18); BAR();
        if (q == 20 && wave == 4 && lane == 0 && pi < 15) set_flag(ws, FL_HALO, pi, pj, T);
      }
      u32x4 hv[4]; unsigned hf = T;
      const bool hal = (wave == 5 && pi > 0);
      gu32* fp = (gu32*)(ws + WS_FLAGS + (size_t)(FL_HALO*16 + (pi > 0 ? pi - 1 : 0))*256) + pj;
      if (hal) asm volatile("global_load_dword %0, %1, %2 sc1" : "=v"(hf) : "v"(0), "s"(fp) : "memory");
      if (hal) WAITV(10); else WAITV(9);
      BAR();
      if (hal) {
        asm volatile("s_waitcnt vmcnt(0)" : "+v"(hf) :: "memory");
        if (__builtin_amdgcn_readfirstlane(hf) != T)
          for (int spin = 0; spin < SPIN_MAX; ++spin) { if (__builtin_amdgcn_readfirstlane(__hip_atomic_load(fp, RLX_AGENT)) == T) break; __builtin_amdgcn_s_sleep(1); }
        const u32x4 s_hal = make_srd(ws + WS_HALO + (size_t)((pi - 1)*16 + pj)*4096, 4096);
        static_for<0, 4>([&](auto C) { int ch = C.value*64 + lane; if (ch > 247) ch = 247; u32x4 t;
          asm volatile("buffer_load_dwordx4 %0, %1, %2, 0 offen sc1" : "=v"(t) : "v"(ch*16), "s"(s_hal) : "memory"); hv[C.value] = t; });
      } else WAITV(0);
      BAR();
      if (wave == 5) {
        if (pi > 0) asm volatile("s_waitcnt vmcnt(0)" : "+v"(hv[0]), "+v"(hv[1]), "+v"(hv[2]), "+v"(hv[3]) :: "memory");
        #pragma unroll
        for (int c = 0; c < 4; c++) { int ch = c*64 + lane; if (ch > 247) ch = 247; *(LAS u32x4*)(lds + QB_OFF + ch*16) = (pi > 0) ? hv[c] : u32x4{0, 0, 0, 0}; }
        WAITL(0);
      }
      BAR();
    } else {
      const int gA = 2*(w - 2);
      {
        const u32x4 s_a = make_srd(w_sp, 2048*4096), s_b = make_srd(w_out, 2304*1152), s_c = make_srd(wo, 576*1152);
        SETM0(149504);
        const int vl = lane*16;
        #pragma unroll 1
        for (int q = 0; q < 4; q++) BAR();
        #pragma unroll 1
        for (int q = 4; q < 16; q++) {
          const int k = q - 4;
          if (k < 8) { DMA(vl, s_a, b*32768 + ((w - 2)*16 + 2*k)*1024, 0); DMA(vl, s_a, b*32768 + ((w - 2)*16 + 2*k + 1)*1024, 0); }
          else if (k < 11) { DMA(vl, s_b, b*10368 + ((w - 2)*6 + 2*(k - 8))*1024, 0); DMA(vl, s_b, b*10368 + ((w - 2)*6 + 2*(k - 8) + 1)*1024, 0); }
          else { DMA(vl, s_c, b*2592 + ((w - 2)*2)*1024, 0); DMA(vl, s_c, b*2592 + ((w - 2)*2 + 1)*1024, 0); }
          BAR();
        }
      }
      LAS f32x2* st = (LAS f32x2*)(lds + MISC_OFF + (w - 2)*512);
      {
        const u32x4 s_st = make_srd(ws + WS_STAT, 2048*256);
        const int vo = (pi*16*128 + 32*gA + lane)*16;
        u32x4 rec[16];
        static_for<0, 16>([&](auto C) { u32x4 t; asm volatile("buffer_load_dwordx4 %0, %1, %2, %3 offen sc1" : "=v"(t) : "v"(vo), "s"(s_st), "s"(C.value*2048) : "memory"); rec[C.value] = t; });
        WAITV(16);
        BAR();
        BAR();
        asm volatile("s_waitcnt vmcnt(0)" : "+v"(rec[0]), "+v"(rec[1]), "+v"(rec[2]), "+v"(rec[3]), "+v"(rec[4]), "+v"(rec[5]), "+v"(rec[6]), "+v"(rec[7]),
                          "+v"(rec[8]), "+v"(rec[9]), "+v"(rec[10]), "+v"(rec[11]), "+v"(rec[12]), "+v"(rec[13]), "+v"(rec[14]), "+v"(rec[15]) :: "memory");
        float t1 = 0.f, t2 = 0.f; bool ok = true;
        #pragma unroll
        for (int c = 0; c < 16; c++) { t1 += asf(rec[c][0]); t2 += asf(rec[c][2]); ok = ok && rec[c][1] == T && rec[c][3] == T; }
        if (!__all(ok)) {
          const RSRC r_stat = MKRSRC(ws + WS_STAT, 2048*256);
          for (int spin = 0; spin < SPIN_MAX; ++spin) {
            t1 = 0.f; t2 = 0.f; ok = true;
            #pragma unroll
            for (int c = 0; c < 16; c++) {
              u32x4 v = __builtin_amdgcn_raw_buffer_load_b128(r_stat, vo + c*2048, 0, AUX_SC1);
              t1 += asf(v[0]); t2 += asf(v[2]); ok = ok && v[1] == T && v[3] == T;
            }
            if (__all(ok)) break;
            __builtin_amdgcn_s_sleep(1);
          }
        }
        const float mean = t1*(1.f/2304.f), var = fmaxf(t2*(1.f/2304.f) - mean*mean, 0.f);
        const float rs = __builtin_amdgcn_rsqf(var + 1e-5f);
        st[lane] = f32x2{rs, -mean*rs};
        WAITL(0);
        BAR();
      }
      const RSRC r_z2n = MKRSRC(ws + WS_Z2N, 16*ZJS);
      #pragma unroll 1
      for (int k = 0; k < 2; k++) {
        f32x2 r2[4], h2[4];
        #pragma unroll
        for (int d = 0; d < 4; d++) { f32x2 u0 = st[32*k + 8*g + 2*d], u1 = st[32*k + 8*g + 2*d + 1]; r2[d] = f32x2{u0[0], u1[0]}; h2[d] = f32x2{u0[1], u1[1]}; }
        LAS char* p = lds + ZB_OFF + (gA + k)*IMG_W + (8*g + ((lane >> 2) & 3))*IMG_PITCH + (lane & 3)*8;
        int zo = lane*16 + pj*ZJS + (4*pi + gA + k)*9216;
        #pragma unroll 1
        for (int j3 = 0; j3 < 3; j3++, p += 96, zo += 3072) {
          u32x4 v[3];
          #pragma unroll
          for (int u = 0; u < 3; u++) {
            s16x4 lo = __builtin_amdgcn_ds_read_tr16_b64_v4i16((LAS s16x4*)(p + u*32));
            s16x4 hi = __builtin_amdgcn_ds_read_tr16_b64_v4i16((LAS s16x4*)(p + u*32 + 4*IMG_PITCH));
            v[u] = __builtin_bit_cast(u32x4, __builtin_shufflevector(lo, hi, 0, 1, 2, 3, 4, 5, 6, 7));
          }
          #pragma unroll
          for (int u = 0; u < 3; u++) {
            u32x4 o;
            #pragma unroll
            for (int d = 0; d < 4; d++) o[d] = pack2(f32x2{opq(__builtin_fmaf(bf_lo(v[u][d]), r2[d][0], h2[d][0])), opq(__builtin_fmaf(bf_hi(v[u][d]), r2[d][1], h2[d][1]))});
            __builtin_amdgcn_raw_buffer_store_b128(o, r_z2n, zo + u*1024, 0, AUX_SC1);
          }
          BAR();
        }
      }
      BAR();
      BAR();
    }
    BAR();
    if (!isD) { WAITV(0); if (lane == 0) set_flag(ws, FL_Z, pj, 2*pi + (w - 2), T); }
    {
      #pragma unroll
      for (int m = 0; m < 2; m++) {
        LAS char* zx = lds + Z1B_OFF + w*IMG_W + (8*(fl >> 2) + 4*m + (fl & 3))*IMG_PITCH + g*8;
        #pragma unroll
        for (int n = 0; n < 9; n++) { u32x2 v = *(LAS u32x2*)(zx + n*32); z1p[n][2*m] = v[0]; z1p[n][2*m + 1] = v[1]; }
      }
    }
  }

  const u32x4 s_z2n = make_srd(ws + WS_Z2N + (size_t)pj*ZJS, 64*9*1024);
  const u32x4 s_wsp = make_srd(w_sp + (size_t)128*pi*2048, 128*4096);
  int vw[4];
  const int vlin = lane*16;
  auto p2_issue = [&](int s, auto ZR) {
    const int base = (s & 3)*P2_STAGE;
    if constexpr (ZR.value) {
      static_for<0, 9>([&](auto K) { SETM0(base + w*9216 + K.value*1024); DMAS(vlin, s_z2n, ((2*s + w)*9 + K.value)*1024, 0); });
    } else {
      static_for<0, 8>([&](auto K) { constexpr int k = K.value; const int wr = 2*(w - 2) + (k >> 2);
        SETM0(base + 18432 + wr*4096 + (k & 3)*1024); DMA(vw[k & 3], s_wsp, wr*32*4096 + s*128, 0); });
    }
  };
  if (wave >= 4) {
    { int l2 = lane; OPAQUE(l2);
      const int a = l2 >> 3, bb = l2 & 7;
      #pragma unroll
      for (int m = 0; m < 2; m++)
        #pragma unroll
        for (int hf = 0; hf < 2; hf++) { int rho = 8*hf + a; vw[2*m + hf] = (8*(rho >> 2) + 4*m + (rho & 3))*4096 + ((bb ^ ((rho >> 1) & 7))*16); } }
    if (w >= 2) {
      #pragma unroll 1
      for (int s = 0; s < 2; s++) p2_issue(s, IC<0>{});
    }
  }
  {
      int t2 = tid; OPAQUE(t2);
      const int u = t2 & 1, hh = (t2 >> 7) & 1, tl = ((t2 >> 1) & 63) + 64*(t2 >> 8);
      LAS char* rowp = lds + QB_OFF + (16 + tl)*QB_PITCH + 8*(pj & 1) + 36*hh;
      const int o8 = hh*4, o4 = hh ? 0 : 32;
      u32x2 qv[4]; unsigned q0;
      #pragma unroll
      for (int d = 0; d < 4; d++) qv[d] = *(LAS u32x2*)(rowp + o8 + d*8);
      q0 = *(LAS unsigned*)(rowp + o4);
      float lg[4];
      #pragma unroll
      for (int ww = 0; ww < 4; ww++) {
        LAS char* kr = rowp - (2*(4*u + ww))*QB_PITCH + 80;
        float sa = dot2(q0, *(LAS unsigned*)(kr + o4), 0.f), sb = 0.f;
        #pragma unroll
        for (int d = 0; d < 4; d++) { u32x2 kv = *(LAS u32x2*)(kr + o8 + d*8); sa = dot2(qv[d][0], kv[0], sa); sb = dot2(qv[d][1], kv[1], sb); }
        float s = (sa + sb)*0.23570226039551584f;
        if (128*pi + tl - 2*(4*u + ww) < 0) s = -1e9f;
        lg[ww] = s;
      }
      float l8[8];
      #pragma unroll
      for (int ww = 0; ww < 4; ww++) { float o = dpp_f<0xB1>(lg[ww]); l8[ww] = u ? o : lg[ww]; l8[4 + ww] = u ? lg[ww] : o; }
      float mx = l8[0];
      #pragma unroll
      for (int ww = 1; ww < 8; ww++) mx = fmaxf(mx, l8[ww]);
      float sum = 0.f;
      #pragma unroll
      for (int ww = 0; ww < 8; ww++) { l8[ww] = __builtin_amdgcn_exp2f((l8[ww] - mx)*1.4426950408889634f); sum += l8[ww]; }
      const float inv = __builtin_amdgcn_rcpf(sum);
      float olo[5], ohi[5];
      #pragma unroll
      for (int d = 0; d < 5; d++) { olo[d] = 0.f; ohi[d] = 0.f; }
      #pragma unroll
      for (int ww = 0; ww < 8; ww++) {
        const unsigned pb = pack_bf16(l8[ww]*inv, 0.f);
        const unsigned ph = pb << 16;
        LAS char* vr = rowp - (2*ww)*QB_PITCH + 160;
        u32x2 va = *(LAS u32x2*)(vr + o8 + 16*u), vb = *(LAS u32x2*)(vr + o8 + 16*u + 8); unsigned vc = *(LAS unsigned*)(vr + o4);
        olo[0] = dot2(va[0], pb, olo[0]); ohi[0] = dot2(va[0], ph, ohi[0]); olo[1] = dot2(va[1], pb, olo[1]); ohi[1] = dot2(va[1], ph, ohi[1]);
        olo[2] = dot2(vb[0], pb, olo[2]); ohi[2] = dot2(vb[0], ph, ohi[2]); olo[3] = dot2(vb[1], pb, olo[3]); ohi[3] = dot2(vb[1], ph, ohi[3]);
        olo[4] = dot2(vc, pb, olo[4]); ohi[4] = dot2(vc, ph, ohi[4]);
      }
      *(LAS u32x2*)(rowp + o8 + 16*u) = u32x2{pack_bf16(olo[0], ohi[0]), pack_bf16(olo[1], ohi[1])};
      *(LAS u32x2*)(rowp + o8 + 16*u + 8) = u32x2{pack_bf16(olo[2], ohi[2]), pack_bf16(olo[3], ohi[3])};
      if (u) *(LAS unsigned*)(rowp + o4) = pack_bf16(olo[4], ohi[4]);
      }
  WAITL(0);
  BAR();
  if (wave >= 4 && w < 2) { wait_flags(ws, FL_Z, pj, 32, T, lane); p2_issue(0, IC<1>{}); p2_issue(1, IC<1>{}); }
  if (wave >= 4) {
    #pragma unroll
    for (int n = 0; n < 3; n++) {
      #pragma unroll
      for (int d = 0; d < 4; d++) z1p[n][d] = pack2(gelu2(f32x2{bf_lo(z1p[n][d]), bf_hi(z1p[n][d])}));
      asm volatile("" : "+v"(z1p[n][0]), "+v"(z1p[n][1]), "+v"(z1p[n][2]), "+v"(z1p[n][3]));
    }
  }
  if (wave < 4) {
    const RSRC r_o = MKRSRC(ws + WS_O, 128*OTS);
    if (tid < 252) {
      const int r0 = tid / 9, p = tid % 9;
      const int col = 36*pj + 4*p, c32 = col & 31;
      const int cpart = ((col >> 5)*64 + 16*((c32 & 15) >> 2))*16 + (c32 >> 4)*8;
      LAS char* src = lds + QB_OFF + (16 + r0)*QB_PITCH + 8*(pj & 1) + 8*p;
      #pragma unroll
      for (int it = 0; it < 5; it++) {
        const int tl = r0 + 28*it;
        if (tl < 128) {
          u32x2 v = *(LAS u32x2*)(src + it*28*QB_PITCH);
          const int r32 = tl & 31, tt = 8*pi + 2*(tl >> 5) + ((r32 >> 2) & 1), rho = 4*(r32 >> 3) + (r32 & 3);
          __builtin_amdgcn_raw_buffer_store_b64(v, r_o, tt*OTS + rho*16 + cpart, 0, AUX_SC1);
        }
      }
    }
  }
  WAITL(0);
  BAR();
  const int p3cnt = (xcd < 2) ? 30 : 29;
  const int p3T = xcd*29 + (xcd < 2 ? xcd : 2) + rr;
  const bool p3act = rr < p3cnt;
  const int rg = p3T / 9, pc = p3T % 9;

  if (wave >= 4) {
    __builtin_amdgcn_s_setprio(0);
    auto p2_helper = [&](auto ZR) {
      constexpr int CNT = ZR.value ? 9 : 8;
      p2_issue(2, ZR); p2_issue(3, ZR);
      WAITV(3*CNT);
      BAR();
      auto piece = [&](int s, auto K) {
        constexpr int k = K.value; const int base = (s & 3)*P2_STAGE;
        if constexpr (ZR.value) { SETM0(base + w*9216 + k*1024); DMAS(vlin, s_z2n, ((2*s + w)*9 + k)*1024, 0); }
        else if constexpr (k < 8) { const int wr = 2*(w - 2) + (k >> 2); SETM0(base + 18432 + wr*4096 + (k & 3)*1024); DMA(vw[k & 3], s_wsp, wr*32*4096 + s*128, 0); }
      };
      auto step = [&](int s, auto PR) {
        WAITV(2*CNT);
        BAR();
        static_for<0, 9>([&](auto K) {
          piece(s + 4, K);
          if constexpr (PR.value >= 0 && K.value == 2) {
            constexpr int pr = PR.value;
            unsigned rr2 = pack2(f32x2{opq(gelu_f(opq(bf_lo(z1p[pr / 4][pr % 4])))), opq(gelu_f(opq(bf_hi(z1p[pr / 4][pr % 4]))))});
            asm volatile("" : "+v"(rr2));
            z1p[pr / 4][pr % 4] = rr2;
          }
        });
      };
      static_for<0, 24>([&](auto S) {
        step(S.value, IC<12 + S.value>{});
        if constexpr (S.value == 4) { if (wave == 4 && lane == 0) set_flag(ws, FL_O, pi, pj, T); }
      });
      #pragma unroll 1
      for (int s = 24; s < 28; s++) step(s, IC<-1>{});
      WAITV(2*CNT); BAR();
      WAITV(CNT); BAR();
      #pragma unroll
      for (int n = 0; n < 9; n++) *(LAS u32x4*)(lds + w*9216 + n*1024 + lane*16) = u32x4{z1p[n][0], z1p[n][1], z1p[n][2], z1p[n][3]};
      WAITV(0); WAITL(0); BAR();
      BAR();
    };
    if (w < 2) p2_helper(IC<1>{}); else p2_helper(IC<0>{});
    if (p3act) {
      const u32x4 s_o = make_srd(ws + WS_O, 128*OTS), s_h = make_srd(ws + WS_H, 128*HTS);
      const u32x4 s_wo = make_srd(wo, 576*1152), s_wout = make_srd(w_out, 2304*1152);
      int vW0, vW1, vA[5];
      int l2 = lane; OPAQUE(l2);
      { const int r = l2 >> 3;
        vW0 = r*1152 + (((l2 & 7) ^ (((r >> 1) & 3) << 1))*16);
        vW1 = (r + 8)*1152 + (((l2 & 7) ^ ((((r + 8) >> 1) & 3) << 1))*16);
        #pragma unroll
        for (int m5 = 0; m5 < 5; m5++) { int tt = 5*rg + m5; if (tt > 127) tt = 127; vA[m5] = l2*16 + tt*OTS; } }
      int slot = 2;
      auto p3_w = [&](int sl, u32x4 sw, int R0, int R1) {
        const int base = sl*P3_STAGE + w*9216, s0 = R0*1152 + pc*128, s1 = R1*1152 + pc*128;
        SETM0(base + 5120); DMA(vW0, sw, s0, 0); SETM0(base + 6144); DMA(vW1, sw, s0, 0);
        SETM0(base + 7168); DMA(vW0, sw, s1, 0); SETM0(base + 8192); DMA(vW1, sw, s1, 0);
      };
      auto p3_a = [&](int sl, u32x4 sa, int ks) {
        const int base = sl*P3_STAGE + w*9216;
        static_for<0, 5>([&](auto M) { SETM0(base + M.value*1024); DMAS(vA[M.value], sa, ks*1024, 0); });
      };
      auto adv = [&]() { slot = (slot == 4) ? 0 : slot + 1; };
      auto p3_wo = [&](int u) { const int ks = 3*u + w; if (w < 3) { p3_w(slot, s_wo, 32*ks, 32*ks + 16); p3_a(slot, s_o, ks); } adv(); };
      auto g3_w = [&](int sl, int v) { const int ks = 3*v + w;
        const int R0 = (ks < 64) ? 144*(ks >> 2) + 32*(ks & 3) : 288*(ks - 64) + 128;
        p3_w(sl, s_wout, R0, R0 + ((ks < 64) ? 16 : 144)); };
      auto p3_g3 = [&](int v) { if (w < 3) { g3_w(slot, v); p3_a(slot, s_h, 3*v + w); } adv(); };
      const int p0 = (5*rg) >> 3, p1 = ((5*rg + 4 > 127 ? 127 : 5*rg + 4)) >> 3;
      if (w < 3) { wait_flags(ws, FL_O, p0, 16, T, lane); wait_flags(ws, FL_O, p1, 16, T, lane); }
      #pragma unroll 1
      for (int u = 0; u < 3; u++) p3_wo(u);
      BAR();
      p3_wo(3); p3_wo(4);
      WAITV(27);
      BAR();
      WAITV(18); BAR();
      p3_wo(5); WAITV(18); BAR();
      WAITV(9); BAR();
      WAITV(0); BAR();
      BAR();
      if (w < 3) { g3_w(3, 0); g3_w(4, 1); g3_w(0, 2); g3_w(1, 3); }
      #pragma unroll
      for (int m5 = 0; m5 < 5; m5++) { int tt = 5*rg + m5; if (tt > 127) tt = 127; vA[m5] = l2*16 + tt*HTS; }
      if (w < 3) { wait_flags(ws, FL_H, p0, 16, T, lane); wait_flags(ws, FL_H, p1, 16, T, lane); }
      if (w < 3) { p3_a(3, s_h, w); p3_a(4, s_h, 3 + w); p3_a(0, s_h, 6 + w); p3_a(1, s_h, 9 + w); }
      slot = 2;
      WAITV(10); BAR();
      p3_g3(4); WAITV(14); BAR();
      #pragma unroll 1
      for (int u = 7; u < 26; u++) {
        p3_g3(u - 2);
        WAITV(18); BAR();
      }
      WAITV(9); BAR();
      WAITV(0); BAR();
      BAR();
    }
  } else {
    int l2 = lane; OPAQUE(l2);
    const int g = l2 >> 4, fl = l2 & 15;
    const int zoff = l2*16;
    const int woff0 = 18432 + (4*w + (fl >> 3))*1024 + (fl & 7)*128 + ((g ^ ((fl >> 1) & 7))*16);
    float bs[2];
    #pragma unroll
    for (int m = 0; m < 2; m++) bs[m] = bf_lo(b_sp[128*pi + 32*w + 8*(fl >> 2) + 4*m + (fl & 3)]);
    #pragma unroll
    for (int n = 0; n < 9; n++) { acc[n][0] = f32x4{bs[0], bs[0], bs[0], bs[0]}; acc[n][1] = f32x4{bs[1], bs[1], bs[1], bs[1]}; }
    bf16x8 z0[9], w0[2], z1[9], w1[2];
    auto rd = [&](bf16x8 (&z)[9], bf16x8 (&wv)[2], int s, int kk) {
      const int base = (s & 3)*P2_STAGE;
      LAS char* pw = lds + base + (woff0 ^ (kk*64));
      LAS char* pz = lds + base + kk*9216 + zoff;
      wv[0] = *(LAS bf16x8*)pw; wv[1] = *(LAS bf16x8*)(pw + 2048);
      #pragma unroll
      for (int n = 0; n < 9; n++) z[n] = *(LAS bf16x8*)(pz + n*1024);
    };
    auto mm = [&](bf16x8 (&z)[9], bf16x8 (&wv)[2]) {
      #pragma unroll
      for (int n = 0; n < 9; n++) { acc[n][0] = MFMA(z[n], wv[0], acc[n][0]); acc[n][1] = MFMA(z[n], wv[1], acc[n][1]); }
      #pragma unroll
      for (int q = 0; q < 11; q++) { SG(0x8, 1); SG(0x100, 1); }
      SG(0x8, 7);
    };
    BAR();
    rd(z0, w0, 0, 0);
    CWAITL0(); SB();
    #pragma unroll 1
    for (int s = 0; s < 32; s++) {
      CWAITL0(); SB();
      rd(z1, w1, s, 1);
      mm(z0, w0);
      SB(); CWAITL0(); PIN_ACC(acc);
      if (s == 4) WAITV(0);
      BAR(); SB();
      rd(z0, w0, (s + 1 < 32 ? s + 1 : s), 0);
      mm(z1, w1);
      SB();
    }
    {
      const RSRC r_h = MKRSRC(ws + WS_H, 128*HTS);
      u32x4 z1g[9];
      #pragma unroll
      for (int n = 0; n < 9; n++) z1g[n] = *(LAS u32x4*)(lds + w*9216 + n*1024 + zoff);
      #pragma unroll
      for (int m = 0; m < 2; m++) {
        const int tt = 8*pi + 2*w + m;
        unsigned hp[9][2];
        #pragma unroll
        for (int n = 0; n < 9; n++) {
          hp[n][0] = pack2(f32x2{bf_lo(z1g[n][2*m]), bf_hi(z1g[n][2*m])}*f32x2{acc[n][m][0], acc[n][m][1]});
          hp[n][1] = pack2(f32x2{bf_lo(z1g[n][2*m + 1]), bf_hi(z1g[n][2*m + 1])}*f32x2{acc[n][m][2], acc[n][m][3]});
        }
        #pragma unroll
        for (int q = 0; q < 4; q++)
          __builtin_amdgcn_raw_buffer_store_b128(u32x4{hp[2*q][0], hp[2*q][1], hp[2*q + 1][0], hp[2*q + 1][1]}, r_h, zoff + tt*HTS + (4*pj + q)*1024, 0, AUX_SC1);
        __builtin_amdgcn_raw_buffer_store_b64(u32x2{hp[8][0], hp[8][1]}, r_h, zoff + (pj & 1)*8 + tt*HTS + (64 + (pj >> 1))*1024, 0, AUX_SC1);
      }
    }
    if (p3act) {
      WAITL(0);
      BAR();
      f32x4 aS[5], aC[5];
      u32x2 xv[5];
      const int q4 = (l2 >> 2) & 3, p4 = l2 & 3, r0 = 4*g + q4;
      const int woff = 5120 + r0*128 + (((2*w + (p4 >> 1)) ^ (((r0 >> 1) & 3) << 1))*16) + (p4 & 1)*8;
      bf16x8 hA[3][5], wA[3], hB[3][5], wB[3];
      int slot = 2;
      auto rd3 = [&](bf16x8 (&h)[3][5], bf16x8 (&wv)[3]) {
        LAS char* p = lds + slot*P3_STAGE;
        #pragma unroll
        for (int kk = 0; kk < 3; kk++) {
          s16x4 lo = __builtin_amdgcn_ds_read_tr16_b64_v4i16((LAS s16x4*)(p + kk*9216 + woff));
          s16x4 hi = __builtin_amdgcn_ds_read_tr16_b64_v4i16((LAS s16x4*)(p + kk*9216 + woff + 2048));
          wv[kk] = __builtin_shufflevector(lo, hi, 0, 1, 2, 3, 4, 5, 6, 7);
          #pragma unroll
          for (int m5 = 0; m5 < 5; m5++) h[kk][m5] = *(LAS bf16x8*)(p + kk*9216 + m5*1024 + zoff);
        }
        slot = (slot == 4) ? 0 : slot + 1;
      };
      auto mm3 = [&](bf16x8 (&h)[3][5], bf16x8 (&wv)[3]) {
        #pragma unroll
        for (int kk = 0; kk < 3; kk++)
          #pragma unroll
          for (int m5 = 0; m5 < 5; m5++) aC[m5] = MFMA(wv[kk], h[kk][m5], aC[m5]);
        #pragma unroll
        for (int q = 0; q < 15; q++) { SG(0x8, 1); SG(0x100, 1); }
        SG(0x100, 6);
      };
      #pragma unroll
      for (int m5 = 0; m5 < 5; m5++) {
        int tt = 5*rg + m5; if (tt > 127) tt = 127;
        const int row = 32*(tt >> 1) + 8*(fl >> 2) + 4*(tt & 1) + (fl & 3);
        xv[m5] = *(const u32x2*)(x + (size_t)row*D_ + 64*pc + 16*w + 4*g);
      }
      BAR();
      #pragma unroll 1
      for (int ph = 0; ph < 2; ph++) {
        #pragma unroll
        for (int m5 = 0; m5 < 5; m5++) aC[m5] = f32x4{0,0,0,0};
        rd3(hA, wA); SB();
        const int nit = ph ? 12 : 3;
        #pragma unroll 1
        for (int it = 0; it < nit; it++) {
          CWAITL0(); SB(); rd3(hB, wB); mm3(hA, wA); SB(); PIN5(aC); BAR(); SB();
          CWAITL0(); SB(); rd3(hA, wA); mm3(hB, wB); SB(); PIN5(aC);
          if (ph == 0 && it == 1) { WAITV(0); }
          if (ph == 0 || it < 11) BAR();
          SB();
          if (ph == 0 && it == 1 && tid == 0) set_flag(ws, FL_H, pi, pj, T);
        }
        if (ph == 0) {
          #pragma unroll
          for (int m5 = 0; m5 < 5; m5++) aS[m5] = aC[m5];
          slot = 3;
        }
      }
      #pragma unroll
      for (int m5 = 0; m5 < 5; m5++) {
        const int tt = 5*rg + m5;
        if (tt < 128) {
          const int row = 32*(tt >> 1) + 8*(fl >> 2) + 4*(tt & 1) + (fl & 3);
          const size_t o = (size_t)row*D_ + 64*pc + 16*w + 4*g;
          float r[4];
          #pragma unroll
          for (int e = 0; e < 4; e++) {
            float bv = aC[m5][e], sv = aS[m5][e];
            float sl = bv*__builtin_amdgcn_rcpf(1.f + __builtin_amdgcn_exp2f(-1.4426950408889634f*bv));
            unsigned xw = xv[m5][e >> 1];
            float xx = (e & 1) ? bf_hi(xw) : bf_lo(xw);
            r[e] = __builtin_fmaf(sv, sl, xx);
          }
          *(u32x2*)(out + o) = u32x2{pack_bf16(r[0], r[1]), pack_bf16(r[2], r[3])};
        }
      }
    } else {
      WAITV(0);
      BAR();
      if (tid == 0) set_flag(ws, FL_H, pi, pj, T);
    }
    if (b == 0 && tid == 0) __hip_atomic_store((gu32*)ws, T, RLX_AGENT);
  }
  asm volatile(".Lpf_end:");
}

extern "C" void kernel_launch(void* const* d_in, const int* in_sizes, int n_in, void* d_out, int out_size, void* d_ws, size_t ws_size, hipStream_t stream) {
  (void)hipFuncSetAttribute((const void*)fused_kernel, hipFuncAttributeMaxDynamicSharedMemorySize, LDS_BYTES);
  fused_kernel<<<256, 512, LDS_BYTES, stream>>>((const u16*)d_in[0], (const u16*)d_in[1], (const u16*)d_in[2], (const u16*)d_in[4],
      (const u16*)d_in[5], (const u16*)d_in[6], (const u16*)d_in[7], (u16*)d_out, (char*)d_ws);
}
```

```cpp
#include <hip/hip_runtime.h>
#include <stdint.h>

typedef unsigned short u16;
typedef __attribute__((ext_vector_type(8))) short bf16x8;
typedef __attribute__((ext_vector_type(4))) short s16x4;
typedef __attribute__((ext_vector_type(4))) float f32x4;
typedef __attribute__((ext_vector_type(16))) float f32x16;
typedef __attribute__((ext_vector_type(2))) float f32x2;
typedef __attribute__((ext_vector_type(4))) unsigned u32x4;
typedef __attribute__((ext_vector_type(2))) unsigned u32x2;
typedef __attribute__((ext_vector_type(2))) __bf16 bf16x2;
#define GAS __attribute__((address_space(1)))
#define LAS __attribute__((address_space(3)))
typedef GAS unsigned gu32;
#define RLX_AGENT __ATOMIC_RELAXED, __HIP_MEMORY_SCOPE_AGENT
#define BAR() asm volatile("s_barrier" ::: "memory")
#define SB() __builtin_amdgcn_sched_barrier(0)
#define SG(mask, n) __builtin_amdgcn_sched_group_barrier(mask, n, 0)
#define WAITV(n) asm volatile("s_waitcnt vmcnt(%0)" :: "i"(n) : "memory")
#define WAITL(n) asm volatile("s_waitcnt lgkmcnt(%0)" :: "i"(n) : "memory")
#define CWAITL0() __builtin_amdgcn_s_waitcnt(0xC07F)
#define RSRC __amdgpu_buffer_rsrc_t
#define MKRSRC(p, bytes) __builtin_amdgcn_make_buffer_rsrc((void*)(p), 0, (bytes), 0x00020000)
#define AUX_SC1 16
#define OPAQUE(x) asm volatile("" : "+v"(x))

constexpr int D_ = 576, FH = 2304;
constexpr size_t WS_FLAGS = 4096;
constexpr size_t WS_STAT  = 32768;
constexpr size_t WS_HALO  = WS_STAT + 2048*256;
constexpr int ZJS = 64*9*1024 + 256, HTS = 72*1024 + 256, OTS = 18*1024 + 256;
constexpr size_t WS_Z2N   = WS_HALO + 16*16*4096;
constexpr size_t WS_H     = WS_Z2N + (size_t)16*ZJS;
constexpr size_t WS_O     = WS_H + (size_t)128*HTS;
constexpr int FL_Z = 1, FL_HALO = 2, FL_O = 3, FL_H = 4;
constexpr int LDS_BYTES = 163840;
constexpr int P1_STAGE = 18432, P1_NS = 5;
constexpr int XCH_OFF = 4*P1_STAGE;
constexpr int ZB_OFF = P1_NS*P1_STAGE;
constexpr int QB_PITCH = 248;
constexpr int IMG_PITCH = 296, IMG_W = 32*IMG_PITCH;
constexpr int QB_OFF = ZB_OFF + 4*IMG_W - 16*QB_PITCH;
constexpr int MISC_OFF = QB_OFF + 144*QB_PITCH;
constexpr int Z1B_OFF = 4*P1_STAGE;
constexpr int P2_STAGE = 34816;
constexpr int P3_STAGE = 27648;
constexpr int SPIN_MAX = 1 << 18;

template<int N> struct IC { static constexpr int value = N; };
template<int B, int E, class F> __device__ __forceinline__ void static_for(F&& f) {
  if constexpr (B < E) { f(IC<B>{}); static_for<B + 1, E>(f); }
}

__device__ __forceinline__ unsigned pack2(f32x2 v) { return __builtin_bit_cast(unsigned, __builtin_convertvector(v, bf16x2)); }
__device__ __forceinline__ float opq(float x) { asm("" : "+v"(x)); return x; }
__device__ __forceinline__ unsigned pack_bf16(float a, float b) { return pack2(f32x2{a, b}); }
__device__ __forceinline__ float asf(unsigned u) { return __builtin_bit_cast(float, u); }
__device__ __forceinline__ float bf_lo(unsigned u) { return __builtin_bit_cast(float, u << 16); }
__device__ __forceinline__ float bf_hi(unsigned u) { return __builtin_bit_cast(float, u & 0xffff0000u); }
__device__ __forceinline__ float dot2(unsigned a, unsigned b, float c) {
  return __builtin_amdgcn_fdot2_f32_bf16(__builtin_bit_cast(bf16x2, a), __builtin_bit_cast(bf16x2, b), c, false);
}
__device__ __forceinline__ float gelu_f(float x) {
  const float c1 = -2.302208198f, c2 = -0.1029432397f;
  float t = __builtin_fmaf(x * x, c2, c1);
  float e = __builtin_amdgcn_exp2f(x * t);
  return x * __builtin_amdgcn_rcpf(1.f + e);
}
__device__ __forceinline__ f32x2 gelu2(f32x2 x) {
  const f32x2 c1 = {-2.302208198f, -2.302208198f}, c2 = {-0.1029432397f, -0.1029432397f}, one = {1.f, 1.f};
  f32x2 t = __builtin_elementwise_fma(x * x, c2, c1);
  f32x2 a = x * t;
  f32x2 e = {__builtin_amdgcn_exp2f(a[0]), __builtin_amdgcn_exp2f(a[1])};
  f32x2 d = e + one;
  f32x2 r = {__builtin_amdgcn_rcpf(d[0]), __builtin_amdgcn_rcpf(d[1])};
  return x * r;
}
template<int CTRL> __device__ __forceinline__ float dpp_f(float v) {
  return __builtin_bit_cast(float, __builtin_amdgcn_update_dpp(0, __builtin_bit_cast(int, v), CTRL, 0xf, 0xf, true));
}
__device__ __forceinline__ float row16_allsum(float v) {
  v += dpp_f<0x128>(v); v += dpp_f<0x124>(v); v += dpp_f<0x122>(v); v += dpp_f<0x121>(v); return v;
}
__device__ __forceinline__ bool poll_flags(const char* ws, int group, int idx, int nflags, unsigned T, int lane) {
  gu32* p = (gu32*)(ws + WS_FLAGS + (size_t)(group*16 + idx)*256) + (lane & (nflags - 1));
  unsigned v = __hip_atomic_load(p, RLX_AGENT);
  return __all(v == T);
}
__device__ __forceinline__ void wait_flags(const char* ws, int group, int idx, int nflags, unsigned T, int lane) {
  for (int spin = 0; spin < SPIN_MAX; ++spin) {
    if (poll_flags(ws, group, idx, nflags, T, lane)) break;
    __builtin_amdgcn_s_sleep(1);
  }
}
__device__ __forceinline__ void set_flag(char* ws, int group, int idx, int k, unsigned T) {
  __hip_atomic_store((gu32*)(ws + WS_FLAGS + (size_t)(group*16 + idx)*256) + k, T, RLX_AGENT);
}
__device__ __forceinline__ int kperm(int r) { return 8*(r >> 3) + 4*(r & 1) + ((r >> 1) & 3); }
__device__ __forceinline__ u32x4 make_srd(const void* p, unsigned bytes) {
  unsigned long long a = (unsigned long long)p;
  return u32x4{(unsigned)a, (unsigned)(a >> 32) & 0xffffu, bytes, 0x00020000u};
}
__device__ __forceinline__ void SETM0(int v) { asm volatile("s_mov_b32 m0, %0\n\ts_nop 0" :: "s"(v) : "memory"); }
template<int OFF> __device__ __forceinline__ void dma_o(int voff, u32x4 srd, int soff) {
  asm volatile("buffer_load_dwordx4 %0, %1, %2 offen offset:%3 lds" :: "v"(voff), "s"(srd), "s"(soff), "i"(OFF) : "memory");
}
template<int OFF> __device__ __forceinline__ void dma_s(int voff, u32x4 srd, int soff) {
  asm volatile("buffer_load_dwordx4 %0, %1, %2 offen offset:%3 sc1 lds" :: "v"(voff), "s"(srd), "s"(soff), "i"(OFF) : "memory");
}
#define DMA(voff, srd, soff, OFF) dma_o<OFF>((voff), (srd), (soff))
#define DMAS(voff, srd, soff, OFF) dma_s<OFF>((voff), (srd), (soff))

#define PIN4(a) asm volatile("" : "+v"(a))
#define PIN_ACC(acc) asm volatile("" : "+v"(acc[0][0]), "+v"(acc[0][1]), "+v"(acc[1][0]), "+v"(acc[1][1]), "+v"(acc[2][0]), "+v"(acc[2][1]), \
  "+v"(acc[3][0]), "+v"(acc[3][1]), "+v"(acc[4][0]), "+v"(acc[4][1]), "+v"(acc[5][0]), "+v"(acc[5][1]), "+v"(acc[6][0]), "+v"(acc[6][1]), \
  "+v"(acc[7][0]), "+v"(acc[7][1]), "+v"(acc[8][0]), "+v"(acc[8][1]))
#define PIN5(a) asm volatile("" : "+v"(a[0]), "+v"(a[1]), "+v"(a[2]), "+v"(a[3]), "+v"(a[4]))
#define MFMA32(a, b, c) __builtin_amdgcn_mfma_f32_32x32x16_bf16((a), (b), (c), 0, 0, 0)
#define MFMA(a, b, c) __builtin_amdgcn_mfma_f32_16x16x32_bf16((a), (b), (c), 0, 0, 0)


extern "C" __global__ __launch_bounds__(512, 2) void fused_kernel(const u16* __restrict__ x, const u16* __restrict__ wqkv, const u16* __restrict__ wo,
    const u16* __restrict__ w_in, const u16* __restrict__ w_sp, const u16* __restrict__ b_sp, const u16* __restrict__ w_out,
    u16* __restrict__ out, char* __restrict__ ws)
{
  extern __shared__ __attribute__((aligned(16))) char smem[];
  LAS char* lds = (LAS char*)smem;
  const int tid = threadIdx.x, lane = tid & 63;
  const int wave = __builtin_amdgcn_readfirstlane(tid >> 6);
  const int w = wave & 3;
  const int b = blockIdx.x, xcd = b & 7, rr = b >> 3;
  const int pi = 4*(xcd & 3) + (rr & 3), pj = 8*(xcd >> 2) + (rr >> 2);
  if (wave == 7) {
    unsigned long long pc; unsigned off, sz;
    asm volatile("s_getpc_b64 %0\n.Lpf_here:\n\ts_mov_b32 %1, .Lpf_here-fused_kernel\n\ts_mov_b32 %2, .Lpf_end-fused_kernel" : "=s"(pc), "=s"(off), "=s"(sz));
    pc -= off;
    const u32x4 s_code = u32x4{(unsigned)pc, (unsigned)(pc >> 32) & 0xffffu, sz, 0x00020000u};
    const int per = 2048;
    SETM0(149504); DMA(lane*16, s_code, rr*per, 0); DMA(lane*16, s_code, rr*per, 1024);
  }
  unsigned T;

  int cv[9];
  u32x4 csrd = make_srd(w_in, 576*9216);
  int p1dst = (w & 1)*9216, p1so = 32*(w & 1)*9216 + (FH + 144*pj)*2, p1step = 64*9216;
  int cq[9];
  auto p1_offsets = [&](auto QKV, int (&dst)[9]) {
    int l3 = lane; OPAQUE(l3);
    int r = l3 / 18, ch = l3 % 18;
    #pragma unroll
    for (int t = 0; t < 9; t++) {
      const int kp = kperm(r);
      dst[t] = (QKV.value ? kp*3456 + (ch < 15 ? (ch / 5)*1152 + (ch % 5)*16 : 0) : kp*9216 + ch*16) - (t & 3)*1024;
      r += 3; ch += 10; if (ch >= 18) { ch -= 18; r += 1; }
    }
  };
  auto p1_issue = [&]() {
    SETM0(p1dst); DMA(cv[0], csrd, p1so, 0); DMA(cv[1], csrd, p1so, 1024); DMA(cv[2], csrd, p1so, 2048); DMA(cv[3], csrd, p1so, 3072);
    SETM0(p1dst + 4096); DMA(cv[4], csrd, p1so, 0); DMA(cv[5], csrd, p1so, 1024); DMA(cv[6], csrd, p1so, 2048); DMA(cv[7], csrd, p1so, 3072);
    SETM0(p1dst + 8192); DMA(cv[8], csrd, p1so, 0);
    p1dst = (p1dst >= 4*P1_STAGE) ? p1dst - 4*P1_STAGE : p1dst + P1_STAGE; p1so += p1step;
  };
  const bool isD = (wave == 4 || wave == 5);

  if (wave >= 4) __builtin_amdgcn_s_setprio(3);
  const int mt = wave >> 2;
  bf16x8 xn[36];
  {
    const u32x4 s_x = make_srd(x + (size_t)(128*pi + 32*w)*D_, 32*1152);
    const int a = lane >> 2, bq = lane & 3;
    unsigned tagv;
    asm volatile("global_load_dword %0, %1, %2 sc1" : "=v"(tagv) : "v"(0), "s"(ws) : "memory");
    u32x4 raw[18];
    {
      const int vx = (16*mt + a)*1152 + bq*16;
      static_for<0, 18>([&](auto C) { u32x4 t; asm volatile("buffer_load_dwordx4 %0, %1, %2, 0 offen offset:%3" : "=v"(t) : "v"(vx), "s"(s_x), "i"(C.value*64) : "memory"); raw[C.value] = t; });
    }
    if (isD) { p1_offsets(IC<0>{}, cv); p1_issue(); }
    if (isD) WAITV(9); else WAITV(0);
    asm volatile("" : "+v"(tagv));
    T = (unsigned)__builtin_amdgcn_readfirstlane(tagv) + 1u;
    asm volatile("" : "+v"(raw[0]), "+v"(raw[1]), "+v"(raw[2]), "+v"(raw[3]), "+v"(raw[4]), "+v"(raw[5]), "+v"(raw[6]), "+v"(raw[7]), "+v"(raw[8]),
                      "+v"(raw[9]), "+v"(raw[10]), "+v"(raw[11]), "+v"(raw[12]), "+v"(raw[13]), "+v"(raw[14]), "+v"(raw[15]), "+v"(raw[16]), "+v"(raw[17]));
    float s1 = 0.f, s2 = 0.f, s3 = 0.f, s4 = 0.f;
    unsigned ones = 0x3f803f80u; OPAQUE(ones);
    #pragma unroll
    for (int c = 0; c < 18; c++) {
      s1 = dot2(raw[c][0], ones, s1); s2 = dot2(raw[c][0], raw[c][0], s2); s3 = dot2(raw[c][1], ones, s3); s4 = dot2(raw[c][1], raw[c][1], s4);
      s1 = dot2(raw[c][2], ones, s1); s2 = dot2(raw[c][2], raw[c][2], s2); s3 = dot2(raw[c][3], ones, s3); s4 = dot2(raw[c][3], raw[c][3], s4);
    }
    s1 += s3; s2 += s4;
    s1 += dpp_f<0xB1>(s1); s1 += dpp_f<0x4E>(s1);
    s2 += dpp_f<0xB1>(s2); s2 += dpp_f<0x4E>(s2);
    const float mean = s1*(1.f/576.f), var = fmaxf(s2*(1.f/576.f) - mean*mean, 0.f);
    const float rs = __builtin_amdgcn_rsqf(var + 1e-5f), sh = -mean*rs;
    const f32x2 rs2 = {rs, rs}, sh2 = {sh, sh};
    int xw = XCH_OFF + w*18432 + (bq >> 1)*1024 + (32*(bq & 1) + ((16*mt + a) ^ (2*bq)))*16;
    int xr0 = XCH_OFF + w*18432 + (32*(lane >> 5) + ((lane & 31) ^ (2*(lane >> 5))))*16;
    int xr1 = XCH_OFF + w*18432 + 1024 + (32*(lane >> 5) + ((lane & 31) ^ (4 + 2*(lane >> 5))))*16;
    OPAQUE(xw); OPAQUE(xr0); OPAQUE(xr1);
    u32x4 xo[18];
    #pragma unroll
    for (int c = 0; c < 18; c++)
      #pragma unroll
      for (int d = 0; d < 4; d++) xo[c][d] = pack2(__builtin_elementwise_fma(f32x2{bf_lo(raw[c][d]), bf_hi(raw[c][d])}, rs2, sh2));
    #pragma unroll
    for (int c = 0; c < 9; c++) *(LAS u32x4*)(lds + xw + c*2048) = xo[c];
    WAITL(0);
    if (wave >= 6) {
      const u32x4 s_a = make_srd(w_in, 576*9216), s_b = make_srd(wqkv, 576*3456);
      SETM0(149504);
      const int vl = lane*16;
      #pragma unroll
      for (int k = 0; k < 11; k++) DMA(vl, s_a, b*20736 + ((w - 2)*11 + k)*1024, 0);
      #pragma unroll
      for (int k = 0; k < 4; k++) DMA(vl, s_b, b*7776 + ((w - 2)*4 + k)*1024, 0);
    }
    BAR();
    if (isD) p1_issue();
    if (wave < 4) {
      #pragma unroll
      for (int c = 0; c < 9; c++) { xn[2*c] = *(LAS bf16x8*)(lds + xr0 + c*2048); xn[2*c + 1] = *(LAS bf16x8*)(lds + xr1 + c*2048); }
      WAITL(0);
    }
    BAR();
    #pragma unroll
    for (int c = 0; c < 9; c++) *(LAS u32x4*)(lds + xw + c*2048) = xo[9 + c];
    if (isD) WAITV(9);
    WAITL(0);
    BAR();
    if (isD) { p1_issue(); p1_issue(); p1_offsets(IC<1>{}, cq); }
    if (wave < 4) {
      #pragma unroll
      for (int c = 0; c < 9; c++) { xn[18 + 2*c] = *(LAS bf16x8*)(lds + xr0 + c*2048); xn[18 + 2*c + 1] = *(LAS bf16x8*)(lds + xr1 + c*2048); }
    }
  }

  unsigned z1p[9][4];
  f32x4 acc[9][2];

  if (wave < 4) {
    constexpr int D = 3, NB = 4;
    int wl = (8*(lane >> 5) + 2*((lane >> 2) & 3))*288 + ((lane >> 4) & 1)*32 + (lane & 3)*8;
    int slot_off = 0;
    f32x16 a32[5];
    auto body = [&](auto NTC) {
      constexpr int NT = NTC.value, FS = 4*NT;
      bf16x8 wf[NB];
      int wc = wl + slot_off, wn;
      OPAQUE(wc);
      auto rdw = [&](auto G, int base) {
        constexpr int i = G.value % FS, k16 = i / NT, n = i % NT;
        LAS char* p = lds + base + (k16 >> 1)*9216 + (k16 & 1)*16*288 + n*64;
        s16x4 lo = __builtin_amdgcn_ds_read_tr16_b64_v4i16((LAS s16x4*)p);
        s16x4 hi = __builtin_amdgcn_ds_read_tr16_b64_v4i16((LAS s16x4*)(p + 288));
        wf[G.value % NB] = __builtin_shufflevector(lo, hi, 0, 1, 2, 3, 4, 5, 6, 7);
      };
      #pragma unroll
      for (int n = 0; n < NT; n++)
        #pragma unroll
        for (int e = 0; e < 16; e++) a32[n][e] = 0.f;
      static_for<0, D>([&](auto G) { rdw(G, wc); });
      SB();
            static_for<0, 9*FS>([&](auto G) {
        constexpr int st = G.value / FS, i = G.value % FS, k16 = i / NT, n = i % NT;
        if constexpr (i == 0) { slot_off = (slot_off >= 4*P1_STAGE) ? 0 : slot_off + P1_STAGE; wn = wl + slot_off; OPAQUE(wn); }
        if constexpr (i == FS - D) { SB(); if constexpr (NT == 5) PIN5(a32); else asm volatile("" : "+v"(a32[0]), "+v"(a32[1]), "+v"(a32[2]), "+v"(a32[3]));
          BAR(); SB(); }
        a32[n] = MFMA32(wf[G.value % NB], xn[4*st + k16], a32[n]);
        if constexpr (G.value + D < 9*FS) { if constexpr (i + D < FS) rdw(IC<G.value + D>{}, wc); else rdw(IC<G.value + D>{}, wn); }
        SG(0x8, 1); SG(0x100, 2);
        SB();
        if constexpr (i == FS - 1) wc = wn;
      });
      if constexpr (NT == 5) PIN5(a32); else asm volatile("" : "+v"(a32[0]), "+v"(a32[1]), "+v"(a32[2]), "+v"(a32[3]));
    };
    #pragma unroll 1
    for (int rep = 0; rep < 2; rep++) {
      asm volatile("" : "+s"(rep));
      body(IC<5>{});
      if (rep == 0) {
        {
      int l2 = lane; OPAQUE(l2);
      const int sr = l2 & 31, h4 = (l2 >> 5)*4;
      auto pk4 = [&](int n, int rq) { return u32x2{pack2(f32x2{a32[n][4*rq], a32[n][4*rq + 1]}), pack2(f32x2{a32[n][4*rq + 2], a32[n][4*rq + 3]})}; };
        f32x2 q1 = {0.f, 0.f}, q2 = {0.f, 0.f};
        LAS char* p = lds + ZB_OFF + w*IMG_W + sr*IMG_PITCH + h4*2;
        #pragma unroll
        for (int n = 0; n < 5; n++)
          #pragma unroll
          for (int rq = 0; rq < 4; rq++) if (n < 4 || rq < 2) {
            const f32x2 ya = gelu2(f32x2{a32[n][4*rq], a32[n][4*rq + 1]}), yb = gelu2(f32x2{a32[n][4*rq + 2], a32[n][4*rq + 3]});
            q1 += ya; q2 = __builtin_elementwise_fma(ya, ya, q2);
            q1 += yb; q2 = __builtin_elementwise_fma(yb, yb, q2);
            *(LAS u32x2*)(p + n*64 + rq*16) = u32x2{pack2(ya), pack2(yb)};
          }
        float t1 = q1[0] + q1[1], t2 = q2[0] + q2[1];
        t1 += __shfl_xor(t1, 32); t2 += __shfl_xor(t2, 32);
        const u32x4 rec = {__builtin_bit_cast(unsigned, t1), T, __builtin_bit_cast(unsigned, t2), T};
        if (l2 < 32) __builtin_amdgcn_raw_buffer_store_b128(rec, MKRSRC(ws + WS_STAT, 2048*256), ((pi*16 + pj)*128 + 32*w + sr)*16, 0, AUX_SC1);
        WAITL(0);
        }
        body(IC<4>{});
        {
      int l2 = lane; OPAQUE(l2);
      const int sr = l2 & 31, h4 = (l2 >> 5)*4;
      auto pk4 = [&](int n, int rq) { return u32x2{pack2(f32x2{a32[n][4*rq], a32[n][4*rq + 1]}), pack2(f32x2{a32[n][4*rq + 2], a32[n][4*rq + 3]})}; };
        LAS char* p = lds + QB_OFF + (16 + 32*w + sr)*QB_PITCH + h4*2;
        #pragma unroll
        for (int n = 0; n < 4; n++)
          #pragma unroll
          for (int rq = 0; rq < 4; rq++) if (n < 3 || rq < 3 || h4 == 0) *(LAS u32x2*)(p + n*64 + rq*16) = pk4(n, rq);
        WAITL(0);
        }
      }
    }
    {
      int l2 = lane; OPAQUE(l2);
      const int sr = l2 & 31, h4 = (l2 >> 5)*4;
      LAS char* p = lds + Z1B_OFF + w*IMG_W + sr*IMG_PITCH + h4*2;
      #pragma unroll
      for (int n = 0; n < 5; n++)
        #pragma unroll
        for (int rq = 0; rq < 4; rq++) if (n < 4 || rq < 2)
          *(LAS u32x2*)(p + n*64 + rq*16) = u32x2{pack2(f32x2{a32[n][4*rq], a32[n][4*rq + 1]}), pack2(f32x2{a32[n][4*rq + 2], a32[n][4*rq + 3]})};
      WAITL(0);
    }
    BAR();
  } else {
    const int g = lane >> 4, fl = lane & 15;
    __builtin_amdgcn_s_setprio(3);
    if (isD) {
      WAITV(18); BAR();
      #pragma unroll 1
      for (int q = 1; q < 6; q++) { p1_issue(); WAITV(18); BAR(); }
      #pragma unroll
      for (int t = 0; t < 9; t++) { const int t_ = cv[t]; cv[t] = cq[t]; cq[t] = t_; }
      csrd = make_srd(wqkv, 576*3456); p1so = 32*(w & 1)*3456 + ((72*pj) & ~15); p1step = 64*3456;
      #pragma unroll 1
      for (int q = 6; q < 15; q++) { p1_issue(); WAITV(18); BAR(); }
      #pragma unroll
      for (int t = 0; t < 9; t++) cv[t] = cq[t];
      csrd = make_srd(w_in, 576*9216); p1so = 32*(w & 1)*9216 + 144*pj*2; p1step = 64*9216;
      #pragma unroll 1
      for (int q = 15; q < 24; q++) {
        if (q == 19 && wave == 4 && pi < 15) {
          const RSRC r_halo = MKRSRC(ws + WS_HALO + (size_t)(pi*16 + pj)*4096, 4096);
          #pragma unroll
          for (int c = 0; c < 4; c++) { int ch = c*64 + lane; if (ch > 247) ch = 247;
            u32x4 v = *(LAS u32x4*)(lds + QB_OFF + 128*QB_PITCH + ch*16);
            __builtin_amdgcn_raw_buffer_store_b128(v, r_halo, ch*16, 0, AUX_SC1); }
        }
        p1_issue(); WAITV(18); BAR();
        if (q == 20 && wave == 4 && lane == 0 && pi < 15) set_flag(ws, FL_HALO, pi, pj, T);
      }
      u32x4 hv[4]; unsigned hf = T;
      const bool hal = (wave == 5 && pi > 0);
      gu32* fp = (gu32*)(ws + WS_FLAGS + (size_t)(FL_HALO*16 + (pi > 0 ? pi - 1 : 0))*256) + pj;
      if (hal) asm volatile("global_load_dword %0, %1, %2 sc1" : "=v"(hf) : "v"(0), "s"(fp) : "memory");
      if (hal) WAITV(10); else WAITV(9);
      BAR();
      if (hal) {
        asm volatile("s_waitcnt vmcnt(0)" : "+v"(hf) :: "memory");
        if (__builtin_amdgcn_readfirstlane(hf) != T)
          for (int spin = 0; spin < SPIN_MAX; ++spin) { if (__builtin_amdgcn_readfirstlane(__hip_atomic_load(fp, RLX_AGENT)) == T) break; __builtin_amdgcn_s_sleep(1); }
        const u32x4 s_hal = make_srd(ws + WS_HALO + (size_t)((pi - 1)*16 + pj)*4096, 4096);
        static_for<0, 4>([&](auto C) { int ch = C.value*64 + lane; if (ch > 247) ch = 247; u32x4 t;
          asm volatile("buffer_load_dwordx4 %0, %1, %2, 0 offen sc1" : "=v"(t) : "v"(ch*16), "s"(s_hal) : "memory"); hv[C.value] = t; });
      } else WAITV(0);
      BAR();
      if (wave == 5) {
        if (pi > 0) asm volatile("s_waitcnt vmcnt(0)" : "+v"(hv[0]), "+v"(hv[1]), "+v"(hv[2]), "+v"(hv[3]) :: "memory");
        #pragma unroll
        for (int c = 0; c < 4; c++) { int ch = c*64 + lane; if (ch > 247) ch = 247; *(LAS u32x4*)(lds + QB_OFF + ch*16) = (pi > 0) ? hv[c] : u32x4{0, 0, 0, 0}; }
        WAITL(0);
      }
      BAR();
    } else {
      const int gA = 2*(w - 2);
      {
        const u32x4 s_a = make_srd(w_sp, 2048*4096), s_b = make_srd(w_out, 2304*1152), s_c = make_srd(wo, 576*1152);
        SETM0(149504);
        const int vl = lane*16;
        {
          const u32x4 s_w = make_srd(ws, (unsigned)(WS_O + (size_t)128*OTS)), s_bs = make_srd(b_sp, 4096);
          if (w == 2) { if (rr < 20) DMAS(vl, s_w, (int)WS_FLAGS + rr*1024, 0); } else { if (rr < 4) DMAS(vl, s_bs, rr*1024, 0); }
          BAR();
          const int tt0 = 8*pi + 4*(w - 2);
          #pragma unroll
          for (int k = 0; k < 4; k++) DMAS(vl, s_w, (int)WS_H + (tt0 + k)*HTS + (64 + (pj >> 1))*1024, 0);
          BAR();
          const int ks0 = (36*pj) >> 5, ks1 = (36*pj + 35) >> 5;
          #pragma unroll
          for (int k = 0; k < 4; k++) DMAS(vl, s_w, (int)WS_O + (tt0 + k)*OTS + ks0*1024, 0);
          BAR();
          if (ks1 != ks0) {
            #pragma unroll
            for (int k = 0; k < 4; k++) DMAS(vl, s_w, (int)WS_O + (tt0 + k)*OTS + ks1*1024, 0);
          }
          BAR();
        }
        #pragma unroll 1
        for (int q = 4; q < 16; q++) {
          const int k = q - 4;
          if (k < 8) { DMA(vl, s_a, b*32768 + ((w - 2)*16 + 2*k)*1024, 0); DMA(vl, s_a, b*32768 + ((w - 2)*16 + 2*k + 1)*1024, 0); }
          else if (k < 11) { DMA(vl, s_b, b*10368 + ((w - 2)*6 + 2*(k - 8))*1024, 0); DMA(vl, s_b, b*10368 + ((w - 2)*6 + 2*(k - 8) + 1)*1024, 0); }
          else { DMA(vl, s_c, b*2592 + ((w - 2)*2)*1024, 0); DMA(vl, s_c, b*2592 + ((w - 2)*2 + 1)*1024, 0); }
          BAR();
        }
      }
      LAS f32x2* st = (LAS f32x2*)(lds + MISC_OFF + (w - 2)*512);
      {
        const u32x4 s_st = make_srd(ws + WS_STAT, 2048*256);
        const int vo = (pi*16*128 + 32*gA + lane)*16;
        u32x4 rec[16];
        static_for<0, 16>([&](auto C) { u32x4 t; asm volatile("buffer_load_dwordx4 %0, %1, %2, %3 offen sc1" : "=v"(t) : "v"(vo), "s"(s_st), "s"(C.value*2048) : "memory"); rec[C.value] = t; });
        WAITV(16);
        BAR();
        BAR();
        asm volatile("s_waitcnt vmcnt(0)" : "+v"(rec[0]), "+v"(rec[1]), "+v"(rec[2]), "+v"(rec[3]), "+v"(rec[4]), "+v"(rec[5]), "+v"(rec[6]), "+v"(rec[7]),
                          "+v"(rec[8]), "+v"(rec[9]), "+v"(rec[10]), "+v"(rec[11]), "+v"(rec[12]), "+v"(rec[13]), "+v"(rec[14]), "+v"(rec[15]) :: "memory");
        float t1 = 0.f, t2 = 0.f; bool ok = true;
        #pragma unroll
        for (int c = 0; c < 16; c++) { t1 += asf(rec[c][0]); t2 += asf(rec[c][2]); ok = ok && rec[c][1] == T && rec[c][3] == T; }
        if (!__all(ok)) {
          const RSRC r_stat = MKRSRC(ws + WS_STAT, 2048*256);
          for (int spin = 0; spin < SPIN_MAX; ++spin) {
            t1 = 0.f; t2 = 0.f; ok = true;
            #pragma unroll
            for (int c = 0; c < 16; c++) {
              u32x4 v = __builtin_amdgcn_raw_buffer_load_b128(r_stat, vo + c*2048, 0, AUX_SC1);
              t1 += asf(v[0]); t2 += asf(v[2]); ok = ok && v[1] == T && v[3] == T;
            }
            if (__all(ok)) break;
            __builtin_amdgcn_s_sleep(1);
          }
        }
        const float mean = t1*(1.f/2304.f), var = fmaxf(t2*(1.f/2304.f) - mean*mean, 0.f);
        const float rs = __builtin_amdgcn_rsqf(var + 1e-5f);
        st[lane] = f32x2{rs, -mean*rs};
        WAITL(0);
        BAR();
      }
      const RSRC r_z2n = MKRSRC(ws + WS_Z2N, 16*ZJS);
      #pragma unroll 1
      for (int k = 0; k < 2; k++) {
        f32x2 r2[4], h2[4];
        #pragma unroll
        for (int d = 0; d < 4; d++) { f32x2 u0 = st[32*k + 8*g + 2*d], u1 = st[32*k + 8*g + 2*d + 1]; r2[d] = f32x2{u0[0], u1[0]}; h2[d] = f32x2{u0[1], u1[1]}; }
        LAS char* p = lds + ZB_OFF + (gA + k)*IMG_W + (8*g + ((lane >> 2) & 3))*IMG_PITCH + (lane & 3)*8;
        int zo = lane*16 + pj*ZJS + (4*pi + gA + k)*9216;
        #pragma unroll 1
        for (int j3 = 0; j3 < 3; j3++, p += 96, zo += 3072) {
          u32x4 v[3];
          #pragma unroll
          for (int u = 0; u < 3; u++) {
            s16x4 lo = __builtin_amdgcn_ds_read_tr16_b64_v4i16((LAS s16x4*)(p + u*32));
            s16x4 hi = __builtin_amdgcn_ds_read_tr16_b64_v4i16((LAS s16x4*)(p + u*32 + 4*IMG_PITCH));
            v[u] = __builtin_bit_cast(u32x4, __builtin_shufflevector(lo, hi, 0, 1, 2, 3, 4, 5, 6, 7));
          }
          #pragma unroll
          for (int u = 0; u < 3; u++) {
            u32x4 o;
            #pragma unroll
            for (int d = 0; d < 4; d++) o[d] = pack2(f32x2{opq(__builtin_fmaf(bf_lo(v[u][d]), r2[d][0], h2[d][0])), opq(__builtin_fmaf(bf_hi(v[u][d]), r2[d][1], h2[d][1]))});
            __builtin_amdgcn_raw_buffer_store_b128(o, r_z2n, zo + u*1024, 0, AUX_SC1);
          }
          BAR();
        }
      }
      BAR();
      BAR();
    }
    BAR();
    if (!isD) { WAITV(0); if (lane == 0) set_flag(ws, FL_Z, pj, 2*pi + (w - 2), T); }
    {
      #pragma unroll
      for (int m = 0; m < 2; m++) {
        LAS char* zx = lds + Z1B_OFF + w*IMG_W + (8*(fl >> 2) + 4*m + (fl & 3))*IMG_PITCH + g*8;
        #pragma unroll
        for (int n = 0; n < 9; n++) { u32x2 v = *(LAS u32x2*)(zx + n*32); z1p[n][2*m] = v[0]; z1p[n][2*m + 1] = v[1]; }
      }
    }
  }

  const u32x4 s_z2n = make_srd(ws + WS_Z2N + (size_t)pj*ZJS, 64*9*1024);
  const u32x4 s_wsp = make_srd(w_sp + (size_t)128*pi*2048, 128*4096);
  int vw[4];
  const int vlin = lane*16;
  auto p2_issue = [&](int s, auto ZR) {
    const int base = (s & 3)*P2_STAGE;
    if constexpr (ZR.value) {
      static_for<0, 9>([&](auto K) { SETM0(base + w*9216 + K.value*1024); DMAS(vlin, s_z2n, ((2*s + w)*9 + K.value)*1024, 0); });
    } else {
      static_for<0, 8>([&](auto K) { constexpr int k = K.value; const int wr = 2*(w - 2) + (k >> 2);
        SETM0(base + 18432 + wr*4096 + (k & 3)*1024); DMA(vw[k & 3], s_wsp, wr*32*4096 + s*128, 0); });
    }
  };
  if (wave >= 4) {
    { int l2 = lane; OPAQUE(l2);
      const int a = l2 >> 3, bb = l2 & 7;
      #pragma unroll
      for (int m = 0; m < 2; m++)
        #pragma unroll
        for (int hf = 0; hf < 2; hf++) { int rho = 8*hf + a; vw[2*m + hf] = (8*(rho >> 2) + 4*m + (rho & 3))*4096 + ((bb ^ ((rho >> 1) & 7))*16); } }
    if (w >= 2) {
      #pragma unroll 1
      for (int s = 0; s < 2; s++) p2_issue(s, IC<0>{});
    }
  }
  {
      int t2 = tid; OPAQUE(t2);
      const int u = t2 & 1, hh = (t2 >> 7) & 1, tl = ((t2 >> 1) & 63) + 64*(t2 >> 8);
      LAS char* rowp = lds + QB_OFF + (16 + tl)*QB_PITCH + 8*(pj & 1) + 36*hh;
      const int o8 = hh*4, o4 = hh ? 0 : 32;
      u32x2 qv[4]; unsigned q0;
      #pragma unroll
      for (int d = 0; d < 4; d++) qv[d] = *(LAS u32x2*)(rowp + o8 + d*8);
      q0 = *(LAS unsigned*)(rowp + o4);
      float lg[4];
      #pragma unroll
      for (int ww = 0; ww < 4; ww++) {
        LAS char* kr = rowp - (2*(4*u + ww))*QB_PITCH + 80;
        float sa = dot2(q0, *(LAS unsigned*)(kr + o4), 0.f), sb = 0.f;
        #pragma unroll
        for (int d = 0; d < 4; d++) { u32x2 kv = *(LAS u32x2*)(kr + o8 + d*8); sa = dot2(qv[d][0], kv[0], sa); sb = dot2(qv[d][1], kv[1], sb); }
        float s = (sa + sb)*0.23570226039551584f;
        if (128*pi + tl - 2*(4*u + ww) < 0) s = -1e9f;
        lg[ww] = s;
      }
      float l8[8];
      #pragma unroll
      for (int ww = 0; ww < 4; ww++) { float o = dpp_f<0xB1>(lg[ww]); l8[ww] = u ? o : lg[ww]; l8[4 + ww] = u ? lg[ww] : o; }
      float mx = l8[0];
      #pragma unroll
      for (int ww = 1; ww < 8; ww++) mx = fmaxf(mx, l8[ww]);
      float sum = 0.f;
      #pragma unroll
      for (int ww = 0; ww < 8; ww++) { l8[ww] = __builtin_amdgcn_exp2f((l8[ww] - mx)*1.4426950408889634f); sum += l8[ww]; }
      const float inv = __builtin_amdgcn_rcpf(sum);
      float olo[5], ohi[5];
      #pragma unroll
      for (int d = 0; d < 5; d++) { olo[d] = 0.f; ohi[d] = 0.f; }
      #pragma unroll
      for (int ww = 0; ww < 8; ww++) {
        const unsigned pb = pack_bf16(l8[ww]*inv, 0.f);
        const unsigned ph = pb << 16;
        LAS char* vr = rowp - (2*ww)*QB_PITCH + 160;
        u32x2 va = *(LAS u32x2*)(vr + o8 + 16*u), vb = *(LAS u32x2*)(vr + o8 + 16*u + 8); unsigned vc = *(LAS unsigned*)(vr + o4);
        olo[0] = dot2(va[0], pb, olo[0]); ohi[0] = dot2(va[0], ph, ohi[0]); olo[1] = dot2(va[1], pb, olo[1]); ohi[1] = dot2(va[1], ph, ohi[1]);
        olo[2] = dot2(vb[0], pb, olo[2]); ohi[2] = dot2(vb[0], ph, ohi[2]); olo[3] = dot2(vb[1], pb, olo[3]); ohi[3] = dot2(vb[1], ph, ohi[3]);
        olo[4] = dot2(vc, pb, olo[4]); ohi[4] = dot2(vc, ph, ohi[4]);
      }
      *(LAS u32x2*)(rowp + o8 + 16*u) = u32x2{pack_bf16(olo[0], ohi[0]), pack_bf16(olo[1], ohi[1])};
      *(LAS u32x2*)(rowp + o8 + 16*u + 8) = u32x2{pack_bf16(olo[2], ohi[2]), pack_bf16(olo[3], ohi[3])};
      if (u) *(LAS unsigned*)(rowp + o4) = pack_bf16(olo[4], ohi[4]);
      }
  WAITL(0);
  BAR();
  if (wave >= 4 && w < 2) { wait_flags(ws, FL_Z, pj, 32, T, lane); p2_issue(0, IC<1>{}); p2_issue(1, IC<1>{}); }
  if (wave >= 4) {
    #pragma unroll
    for (int n = 0; n < 3; n++) {
      #pragma unroll
      for (int d = 0; d < 4; d++) z1p[n][d] = pack2(gelu2(f32x2{bf_lo(z1p[n][d]), bf_hi(z1p[n][d])}));
      asm volatile("" : "+v"(z1p[n][0]), "+v"(z1p[n][1]), "+v"(z1p[n][2]), "+v"(z1p[n][3]));
    }
  }
  if (wave < 4) {
    const RSRC r_o = MKRSRC(ws + WS_O, 128*OTS);
    if (tid < 252) {
      const int r0 = tid / 9, p = tid % 9;
      const int col = 36*pj + 4*p, c32 = col & 31;
      const int cpart = ((col >> 5)*64 + 16*((c32 & 15) >> 2))*16 + (c32 >> 4)*8;
      LAS char* src = lds + QB_OFF + (16 + r0)*QB_PITCH + 8*(pj & 1) + 8*p;
      #pragma unroll
      for (int it = 0; it < 5; it++) {
        const int tl = r0 + 28*it;
        if (tl < 128) {
          u32x2 v = *(LAS u32x2*)(src + it*28*QB_PITCH);
          const int r32 = tl & 31, tt = 8*pi + 2*(tl >> 5) + ((r32 >> 2) & 1), rho = 4*(r32 >> 3) + (r32 & 3);
          __builtin_amdgcn_raw_buffer_store_b64(v, r_o, tt*OTS + rho*16 + cpart, 0, AUX_SC1);
        }
      }
    }
  }
  WAITL(0);
  BAR();
  const int p3cnt = (xcd < 2) ? 30 : 29;
  const int p3T = xcd*29 + (xcd < 2 ? xcd : 2) + rr;
  const bool p3act = rr < p3cnt;
  const int rg = p3T / 9, pc = p3T % 9;

  if (wave >= 4) {
    __builtin_amdgcn_s_setprio(0);
    auto p2_helper = [&](auto ZR) {
      constexpr int CNT = ZR.value ? 9 : 8;
      p2_issue(2, ZR); p2_issue(3, ZR);
      WAITV(3*CNT);
      BAR();
      auto piece = [&](int s, auto K) {
        constexpr int k = K.value; const int base = (s & 3)*P2_STAGE;
        if constexpr (ZR.value) { SETM0(base + w*9216 + k*1024); DMAS(vlin, s_z2n, ((2*s + w)*9 + k)*1024, 0); }
        else if constexpr (k < 8) { const int wr = 2*(w - 2) + (k >> 2); SETM0(base + 18432 + wr*4096 + (k & 3)*1024); DMA(vw[k & 3], s_wsp, wr*32*4096 + s*128, 0); }
      };
      auto step = [&](int s, auto PR) {
        WAITV(2*CNT);
        BAR();
        static_for<0, 9>([&](auto K) {
          piece(s + 4, K);
          if constexpr (PR.value >= 0 && K.value == 2) {
            constexpr int pr = PR.value;
            unsigned rr2 = pack2(f32x2{opq(gelu_f(opq(bf_lo(z1p[pr / 4][pr % 4])))), opq(gelu_f(opq(bf_hi(z1p[pr / 4][pr % 4]))))});
            asm volatile("" : "+v"(rr2));
            z1p[pr / 4][pr % 4] = rr2;
          }
        });
      };
      static_for<0, 24>([&](auto S) {
        step(S.value, IC<12 + S.value>{});
        if constexpr (S.value == 4) { if (wave == 4 && lane == 0) set_flag(ws, FL_O, pi, pj, T); }
      });
      #pragma unroll 1
      for (int s = 24; s < 28; s++) step(s, IC<-1>{});
      WAITV(2*CNT); BAR();
      WAITV(CNT); BAR();
      #pragma unroll
      for (int n = 0; n < 9; n++) *(LAS u32x4*)(lds + w*9216 + n*1024 + lane*16) = u32x4{z1p[n][0], z1p[n][1], z1p[n][2], z1p[n][3]};
      WAITV(0); WAITL(0); BAR();
      BAR();
    };
    if (w < 2) p2_helper(IC<1>{}); else p2_helper(IC<0>{});
    if (p3act) {
      const u32x4 s_o = make_srd(ws + WS_O, 128*OTS), s_h = make_srd(ws + WS_H, 128*HTS);
      const u32x4 s_wo = make_srd(wo, 576*1152), s_wout = make_srd(w_out, 2304*1152);
      int vW0, vW1, vA[5];
      int l2 = lane; OPAQUE(l2);
      { const int r = l2 >> 3;
        vW0 = r*1152 + (((l2 & 7) ^ (((r >> 1) & 3) << 1))*16);
        vW1 = (r + 8)*1152 + (((l2 & 7) ^ ((((r + 8) >> 1) & 3) << 1))*16);
        #pragma unroll
        for (int m5 = 0; m5 < 5; m5++) { int tt = 5*rg + m5; if (tt > 127) tt = 127; vA[m5] = l2*16 + tt*OTS; } }
      int slot = 2;
      auto p3_w = [&](int sl, u32x4 sw, int R0, int R1) {
        const int base = sl*P3_STAGE + w*9216, s0 = R0*1152 + pc*128, s1 = R1*1152 + pc*128;
        SETM0(base + 5120); DMA(vW0, sw, s0, 0); SETM0(base + 6144); DMA(vW1, sw, s0, 0);
        SETM0(base + 7168); DMA(vW0, sw, s1, 0); SETM0(base + 8192); DMA(vW1, sw, s1, 0);
      };
      auto p3_a = [&](int sl, u32x4 sa, int ks) {
        const int base = sl*P3_STAGE + w*9216;
        static_for<0, 5>([&](auto M) { SETM0(base + M.value*1024); DMAS(vA[M.value], sa, ks*1024, 0); });
      };
      auto adv = [&]() { slot = (slot == 4) ? 0 : slot + 1; };
      auto p3_wo = [&](int u) { const int ks = 3*u + w; if (w < 3) { p3_w(slot, s_wo, 32*ks, 32*ks + 16); p3_a(slot, s_o, ks); } adv(); };
      auto g3_w = [&](int sl, int v) { const int ks = 3*v + w;
        const int R0 = (ks < 64) ? 144*(ks >> 2) + 32*(ks & 3) : 288*(ks - 64) + 128;
        p3_w(sl, s_wout, R0, R0 + ((ks < 64) ? 16 : 144)); };
      auto p3_g3 = [&](int v) { if (w < 3) { g3_w(slot, v); p3_a(slot, s_h, 3*v + w); } adv(); };
      const int p0 = (5*rg) >> 3, p1 = ((5*rg + 4 > 127 ? 127 : 5*rg + 4)) >> 3;
      if (w < 3) { wait_flags(ws, FL_O, p0, 16, T, lane); wait_flags(ws, FL_O, p1, 16, T, lane); }
      #pragma unroll 1
      for (int u = 0; u < 3; u++) p3_wo(u);
      BAR();
      p3_wo(3); p3_wo(4);
      WAITV(27);
      BAR();
      WAITV(18); BAR();
      p3_wo(5); WAITV(18); BAR();
      WAITV(9); BAR();
      WAITV(0); BAR();
      BAR();
      if (w < 3) { g3_w(3, 0); g3_w(4, 1); g3_w(0, 2); g3_w(1, 3); }
      #pragma unroll
      for (int m5 = 0; m5 < 5; m5++) { int tt = 5*rg + m5; if (tt > 127) tt = 127; vA[m5] = l2*16 + tt*HTS; }
      if (w < 3) { wait_flags(ws, FL_H, p0, 16, T, lane); wait_flags(ws, FL_H, p1, 16, T, lane); }
      if (w < 3) { p3_a(3, s_h, w); p3_a(4, s_h, 3 + w); p3_a(0, s_h, 6 + w); p3_a(1, s_h, 9 + w); }
      slot = 2;
      WAITV(10); BAR();
      p3_g3(4); WAITV(14); BAR();
      #pragma unroll 1
      for (int u = 7; u < 26; u++) {
        p3_g3(u - 2);
        WAITV(18); BAR();
      }
      WAITV(9); BAR();
      WAITV(0); BAR();
      BAR();
    }
  } else {
    int l2 = lane; OPAQUE(l2);
    const int g = l2 >> 4, fl = l2 & 15;
    const int zoff = l2*16;
    const int woff0 = 18432 + (4*w + (fl >> 3))*1024 + (fl & 7)*128 + ((g ^ ((fl >> 1) & 7))*16);
    float bs[2];
    #pragma unroll
    for (int m = 0; m < 2; m++) bs[m] = bf_lo(b_sp[128*pi + 32*w + 8*(fl >> 2) + 4*m + (fl & 3)]);
    #pragma unroll
    for (int n = 0; n < 9; n++) { acc[n][0] = f32x4{bs[0], bs[0], bs[0], bs[0]}; acc[n][1] = f32x4{bs[1], bs[1], bs[1], bs[1]}; }
    bf16x8 z0[9], w0[2], z1[9], w1[2];
    auto rd = [&](bf16x8 (&z)[9], bf16x8 (&wv)[2], int s, int kk) {
      const int base = (s & 3)*P2_STAGE;
      LAS char* pw = lds + base + (woff0 ^ (kk*64));
      LAS char* pz = lds + base + kk*9216 + zoff;
      wv[0] = *(LAS bf16x8*)pw; wv[1] = *(LAS bf16x8*)(pw + 2048);
      #pragma unroll
      for (int n = 0; n < 9; n++) z[n] = *(LAS bf16x8*)(pz + n*1024);
    };
    auto mm = [&](bf16x8 (&z)[9], bf16x8 (&wv)[2]) {
      #pragma unroll
      for (int n = 0; n < 9; n++) { acc[n][0] = MFMA(z[n], wv[0], acc[n][0]); acc[n][1] = MFMA(z[n], wv[1], acc[n][1]); }
      #pragma unroll
      for (int q = 0; q < 11; q++) { SG(0x8, 1); SG(0x100, 1); }
      SG(0x8, 7);
    };
    BAR();
    rd(z0, w0, 0, 0);
    CWAITL0(); SB();
    #pragma unroll 1
    for (int s = 0; s < 32; s++) {
      CWAITL0(); SB();
      rd(z1, w1, s, 1);
      mm(z0, w0);
      SB(); CWAITL0(); PIN_ACC(acc);
      if (s == 4) WAITV(0);
      BAR(); SB();
      rd(z0, w0, (s + 1 < 32 ? s + 1 : s), 0);
      mm(z1, w1);
      SB();
    }
    {
      const RSRC r_h = MKRSRC(ws + WS_H, 128*HTS);
      u32x4 z1g[9];
      #pragma unroll
      for (int n = 0; n < 9; n++) z1g[n] = *(LAS u32x4*)(lds + w*9216 + n*1024 + zoff);
      #pragma unroll
      for (int m = 0; m < 2; m++) {
        const int tt = 8*pi + 2*w + m;
        unsigned hp[9][2];
        #pragma unroll
        for (int n = 0; n < 9; n++) {
          hp[n][0] = pack2(f32x2{bf_lo(z1g[n][2*m]), bf_hi(z1g[n][2*m])}*f32x2{acc[n][m][0], acc[n][m][1]});
          hp[n][1] = pack2(f32x2{bf_lo(z1g[n][2*m + 1]), bf_hi(z1g[n][2*m + 1])}*f32x2{acc[n][m][2], acc[n][m][3]});
        }
        #pragma unroll
        for (int q = 0; q < 4; q++)
          __builtin_amdgcn_raw_buffer_store_b128(u32x4{hp[2*q][0], hp[2*q][1], hp[2*q + 1][0], hp[2*q + 1][1]}, r_h, zoff + tt*HTS + (4*pj + q)*1024, 0, AUX_SC1);
        __builtin_amdgcn_raw_buffer_store_b64(u32x2{hp[8][0], hp[8][1]}, r_h, zoff + (pj & 1)*8 + tt*HTS + (64 + (pj >> 1))*1024, 0, AUX_SC1);
      }
    }
    if (p3act) {
      WAITL(0);
      BAR();
      f32x4 aS[5], aC[5];
      u32x2 xv[5];
      const int q4 = (l2 >> 2) & 3, p4 = l2 & 3, r0 = 4*g + q4;
      const int woff = 5120 + r0*128 + (((2*w + (p4 >> 1)) ^ (((r0 >> 1) & 3) << 1))*16) + (p4 & 1)*8;
      bf16x8 hA[3][5], wA[3], hB[3][5], wB[3];
      int slot = 2;
      auto rd3 = [&](bf16x8 (&h)[3][5], bf16x8 (&wv)[3]) {
        LAS char* p = lds + slot*P3_STAGE;
        #pragma unroll
        for (int kk = 0; kk < 3; kk++) {
          s16x4 lo = __builtin_amdgcn_ds_read_tr16_b64_v4i16((LAS s16x4*)(p + kk*9216 + woff));
          s16x4 hi = __builtin_amdgcn_ds_read_tr16_b64_v4i16((LAS s16x4*)(p + kk*9216 + woff + 2048));
          wv[kk] = __builtin_shufflevector(lo, hi, 0, 1, 2, 3, 4, 5, 6, 7);
          #pragma unroll
          for (int m5 = 0; m5 < 5; m5++) h[kk][m5] = *(LAS bf16x8*)(p + kk*9216 + m5*1024 + zoff);
        }
        slot = (slot == 4) ? 0 : slot + 1;
      };
      auto mm3 = [&](bf16x8 (&h)[3][5], bf16x8 (&wv)[3]) {
        #pragma unroll
        for (int kk = 0; kk < 3; kk++)
          #pragma unroll
          for (int m5 = 0; m5 < 5; m5++) aC[m5] = MFMA(wv[kk], h[kk][m5], aC[m5]);
        #pragma unroll
        for (int q = 0; q < 15; q++) { SG(0x8, 1); SG(0x100, 1); }
        SG(0x100, 6);
      };
      #pragma unroll
      for (int m5 = 0; m5 < 5; m5++) {
        int tt = 5*rg + m5; if (tt > 127) tt = 127;
        const int row = 32*(tt >> 1) + 8*(fl >> 2) + 4*(tt & 1) + (fl & 3);
        xv[m5] = *(const u32x2*)(x + (size_t)row*D_ + 64*pc + 16*w + 4*g);
      }
      BAR();
      #pragma unroll 1
      for (int ph = 0; ph < 2; ph++) {
        #pragma unroll
        for (int m5 = 0; m5 < 5; m5++) aC[m5] = f32x4{0,0,0,0};
        rd3(hA, wA); SB();
        const int nit = ph ? 12 : 3;
        #pragma unroll 1
        for (int it = 0; it < nit; it++) {
          CWAITL0(); SB(); rd3(hB, wB); mm3(hA, wA); SB(); PIN5(aC); BAR(); SB();
          CWAITL0(); SB(); rd3(hA, wA); mm3(hB, wB); SB(); PIN5(aC);
          if (ph == 0 && it == 1) { WAITV(0); }
          if (ph == 0 || it < 11) BAR();
          SB();
          if (ph == 0 && it == 1 && tid == 0) set_flag(ws, FL_H, pi, pj, T);
        }
        if (ph == 0) {
          #pragma unroll
          for (int m5 = 0; m5 < 5; m5++) aS[m5] = aC[m5];
          slot = 3;
        }
      }
      #pragma unroll
      for (int m5 = 0; m5 < 5; m5++) {
        const int tt = 5*rg + m5;
        if (tt < 128) {
          const int row = 32*(tt >> 1) + 8*(fl >> 2) + 4*(tt & 1) + (fl & 3);
          const size_t o = (size_t)row*D_ + 64*pc + 16*w + 4*g;
          float r[4];
          #pragma unroll
          for (int e = 0; e < 4; e++) {
            float bv = aC[m5][e], sv = aS[m5][e];
            float sl = bv*__builtin_amdgcn_rcpf(1.f + __builtin_amdgcn_exp2f(-1.4426950408889634f*bv));
            unsigned xw = xv[m5][e >> 1];
            float xx = (e & 1) ? bf_hi(xw) : bf_lo(xw);
            r[e] = __builtin_fmaf(sv, sl, xx);
          }
          *(u32x2*)(out + o) = u32x2{pack_bf16(r[0], r[1]), pack_bf16(r[2], r[3])};
        }
      }
    } else {
      WAITV(0);
      BAR();
      if (tid == 0) set_flag(ws, FL_H, pi, pj, T);
    }
    if (b == 0 && tid == 0) __hip_atomic_store((gu32*)ws, T, RLX_AGENT);
  }
  asm volatile(".Lpf_end:");
}

extern "C" void kernel_launch(void* const* d_in, const int* in_sizes, int n_in, void* d_out, int out_size, void* d_ws, size_t ws_size, hipStream_t stream) {
  (void)hipFuncSetAttribute((const void*)fused_kernel, hipFuncAttributeMaxDynamicSharedMemorySize, LDS_BYTES);
  fused_kernel<<<256, 512, LDS_BYTES, stream>>>((const u16*)d_in[0], (const u16*)d_in[1], (const u16*)d_in[2], (const u16*)d_in[4],
      (const u16*)d_in[5], (const u16*)d_in[6], (const u16*)d_in[7], (u16*)d_out, (char*)d_ws);
}
```

```cpp
#include <hip/hip_runtime.h>
#include <stdint.h>

typedef unsigned short u16;
typedef __attribute__((ext_vector_type(8))) short bf16x8;
typedef __attribute__((ext_vector_type(4))) short s16x4;
typedef __attribute__((ext_vector_type(4))) float f32x4;
typedef __attribute__((ext_vector_type(16))) float f32x16;
typedef __attribute__((ext_vector_type(2))) float f32x2;
typedef __attribute__((ext_vector_type(4))) unsigned u32x4;
typedef __attribute__((ext_vector_type(2))) unsigned u32x2;
typedef __attribute__((ext_vector_type(2))) __bf16 bf16x2;
#define GAS __attribute__((address_space(1)))
#define LAS __attribute__((address_space(3)))
typedef GAS unsigned gu32;
#define RLX_AGENT __ATOMIC_RELAXED, __HIP_MEMORY_SCOPE_AGENT
#define BAR() asm volatile("s_barrier" ::: "memory")
#define SB() __builtin_amdgcn_sched_barrier(0)
#define SG(mask, n) __builtin_amdgcn_sched_group_barrier(mask, n, 0)
#define WAITV(n) asm volatile("s_waitcnt vmcnt(%0)" :: "i"(n) : "memory")
#define WAITL(n) asm volatile("s_waitcnt lgkmcnt(%0)" :: "i"(n) : "memory")
#define CWAITL0() __builtin_amdgcn_s_waitcnt(0xC07F)
#define RSRC __amdgpu_buffer_rsrc_t
#define MKRSRC(p, bytes) __builtin_amdgcn_make_buffer_rsrc((void*)(p), 0, (bytes), 0x00020000)
#define AUX_SC1 16
#define OPAQUE(x) asm volatile("" : "+v"(x))

constexpr int D_ = 576, FH = 2304;
constexpr size_t WS_FLAGS = 4096;
constexpr size_t WS_STAT  = 32768;
constexpr size_t WS_HALO  = WS_STAT + 2048*256;
constexpr int ZJS = 64*9*1024 + 256, HTS = 72*1024 + 256, OTS = 18*1024 + 256;
constexpr size_t WS_Z2N   = WS_HALO + 16*16*4096;
constexpr size_t WS_H     = WS_Z2N + (size_t)16*ZJS;
constexpr size_t WS_O     = WS_H + (size_t)128*HTS;
constexpr int FL_Z = 1, FL_HALO = 2, FL_O = 3, FL_H = 4;
constexpr int LDS_BYTES = 163840;
constexpr int P1_STAGE = 18432, P1_NS = 5;
constexpr int XCH_OFF = 4*P1_STAGE;
constexpr int ZB_OFF = P1_NS*P1_STAGE;
constexpr int QB_PITCH = 248;
constexpr int IMG_PITCH = 296, IMG_W = 32*IMG_PITCH;
constexpr int QB_OFF = ZB_OFF + 4*IMG_W - 16*QB_PITCH;
constexpr int MISC_OFF = QB_OFF + 144*QB_PITCH;
constexpr int Z1B_OFF = 4*P1_STAGE;
constexpr int P2_STAGE = 34816;
constexpr int P3_STAGE = 27648;
constexpr int SPIN_MAX = 1 << 18;

template<int N> struct IC { static constexpr int value = N; };
template<int B, int E, class F> __device__ __forceinline__ void static_for(F&& f) {
  if constexpr (B < E) { f(IC<B>{}); static_for<B + 1, E>(f); }
}

__device__ __forceinline__ unsigned pack2(f32x2 v) { return __builtin_bit_cast(unsigned, __builtin_convertvector(v, bf16x2)); }
__device__ __forceinline__ float opq(float x) { asm("" : "+v"(x)); return x; }
__device__ __forceinline__ unsigned pack_bf16(float a, float b) { return pack2(f32x2{a, b}); }
__device__ __forceinline__ float asf(unsigned u) { return __builtin_bit_cast(float, u); }
__device__ __forceinline__ float bf_lo(unsigned u) { return __builtin_bit_cast(float, u << 16); }
__device__ __forceinline__ float bf_hi(unsigned u) { return __builtin_bit_cast(float, u & 0xffff0000u); }
__device__ __forceinline__ float dot2(unsigned a, unsigned b, float c) {
  return __builtin_amdgcn_fdot2_f32_bf16(__builtin_bit_cast(bf16x2, a), __builtin_bit_cast(bf16x2, b), c, false);
}
__device__ __forceinline__ float gelu_f(float x) {
  const float c1 = -2.302208198f, c2 = -0.1029432397f;
  float t = __builtin_fmaf(x * x, c2, c1);
  float e = __builtin_amdgcn_exp2f(x * t);
  return x * __builtin_amdgcn_rcpf(1.f + e);
}
__device__ __forceinline__ f32x2 gelu2(f32x2 x) {
  const f32x2 c1 = {-2.302208198f, -2.302208198f}, c2 = {-0.1029432397f, -0.1029432397f}, one = {1.f, 1.f};
  f32x2 t = __builtin_elementwise_fma(x * x, c2, c1);
  f32x2 a = x * t;
  f32x2 e = {__builtin_amdgcn_exp2f(a[0]), __builtin_amdgcn_exp2f(a[1])};
  f32x2 d = e + one;
  f32x2 r = {__builtin_amdgcn_rcpf(d[0]), __builtin_amdgcn_rcpf(d[1])};
  return x * r;
}
template<int CTRL> __device__ __forceinline__ float dpp_f(float v) {
  return __builtin_bit_cast(float, __builtin_amdgcn_update_dpp(0, __builtin_bit_cast(int, v), CTRL, 0xf, 0xf, true));
}
__device__ __forceinline__ float row16_allsum(float v) {
  v += dpp_f<0x128>(v); v += dpp_f<0x124>(v); v += dpp_f<0x122>(v); v += dpp_f<0x121>(v); return v;
}
__device__ __forceinline__ bool poll_flags(const char* ws, int group, int idx, int nflags, unsigned T, int lane) {
  gu32* p = (gu32*)(ws + WS_FLAGS + (size_t)(group*16 + idx)*256) + (lane & (nflags - 1));
  unsigned v = __hip_atomic_load(p, RLX_AGENT);
  return __all(v == T);
}
__device__ __forceinline__ void wait_flags(const char* ws, int group, int idx, int nflags, unsigned T, int lane) {
  for (int spin = 0; spin < SPIN_MAX; ++spin) {
    if (poll_flags(ws, group, idx, nflags, T, lane)) break;
    __builtin_amdgcn_s_sleep(1);
  }
}
__device__ __forceinline__ void set_flag(char* ws, int group, int idx, int k, unsigned T) {
  __hip_atomic_store((gu32*)(ws + WS_FLAGS + (size_t)(group*16 + idx)*256) + k, T, RLX_AGENT);
}
__device__ __forceinline__ int kperm(int r) { return 8*(r >> 3) + 4*(r & 1) + ((r >> 1) & 3); }
__device__ __forceinline__ u32x4 make_srd(const void* p, unsigned bytes) {
  unsigned long long a = (unsigned long long)p;
  return u32x4{(unsigned)a, (unsigned)(a >> 32) & 0xffffu, bytes, 0x00020000u};
}
__device__ __forceinline__ void SETM0(int v) { asm volatile("s_mov_b32 m0, %0\n\ts_nop 0" :: "s"(v) : "memory"); }
template<int OFF> __device__ __forceinline__ void dma_o(int voff, u32x4 srd, int soff) {
  asm volatile("buffer_load_dwordx4 %0, %1, %2 offen offset:%3 lds" :: "v"(voff), "s"(srd), "s"(soff), "i"(OFF) : "memory");
}
template<int OFF> __device__ __forceinline__ void dma_s(int voff, u32x4 srd, int soff) {
  asm volatile("buffer_load_dwordx4 %0, %1, %2 offen offset:%3 sc1 lds" :: "v"(voff), "s"(srd), "s"(soff), "i"(OFF) : "memory");
}
#define DMA(voff, srd, soff, OFF) dma_o<OFF>((voff), (srd), (soff))
#define DMAS(voff, srd, soff, OFF) dma_s<OFF>((voff), (srd), (soff))

#define PIN4(a) asm volatile("" : "+v"(a))
#define PIN_ACC(acc) asm volatile("" : "+v"(acc[0][0]), "+v"(acc[0][1]), "+v"(acc[1][0]), "+v"(acc[1][1]), "+v"(acc[2][0]), "+v"(acc[2][1]), \
  "+v"(acc[3][0]), "+v"(acc[3][1]), "+v"(acc[4][0]), "+v"(acc[4][1]), "+v"(acc[5][0]), "+v"(acc[5][1]), "+v"(acc[6][0]), "+v"(acc[6][1]), \
  "+v"(acc[7][0]), "+v"(acc[7][1]), "+v"(acc[8][0]), "+v"(acc[8][1]))
#define PIN5(a) asm volatile("" : "+v"(a[0]), "+v"(a[1]), "+v"(a[2]), "+v"(a[3]), "+v"(a[4]))
#define MFMA32(a, b, c) __builtin_amdgcn_mfma_f32_32x32x16_bf16((a), (b), (c), 0, 0, 0)
#define MFMA(a, b, c) __builtin_amdgcn_mfma_f32_16x16x32_bf16((a), (b), (c), 0, 0, 0)


extern "C" __global__ __launch_bounds__(512, 2) void fused_kernel(const u16* __restrict__ x, const u16* __restrict__ wqkv, const u16* __restrict__ wo,
    const u16* __restrict__ w_in, const u16* __restrict__ w_sp, const u16* __restrict__ b_sp, const u16* __restrict__ w_out,
    u16* __restrict__ out, char* __restrict__ ws)
{
  extern __shared__ __attribute__((aligned(16))) char smem[];
  LAS char* lds = (LAS char*)smem;
  const int tid = threadIdx.x, lane = tid & 63;
  const int wave = __builtin_amdgcn_readfirstlane(tid >> 6);
  const int w = wave & 3;
  const int b = blockIdx.x, xcd = b & 7, rr = b >> 3;
  const int pi = 4*(xcd & 3) + (rr & 3), pj = 8*(xcd >> 2) + (rr >> 2);
  if (wave == 7) {
    unsigned long long pc; unsigned off, sz;
    asm volatile("s_getpc_b64 %0\n.Lpf_here:\n\ts_mov_b32 %1, .Lpf_here-fused_kernel\n\ts_mov_b32 %2, .Lpf_end-fused_kernel" : "=s"(pc), "=s"(off), "=s"(sz));
    pc -= off;
    const u32x4 s_code = u32x4{(unsigned)pc, (unsigned)(pc >> 32) & 0xffffu, sz, 0x00020000u};
    const int per = 2048;
    SETM0(149504); DMA(lane*16, s_code, rr*per, 0); DMA(lane*16, s_code, rr*per, 1024);
  }
  unsigned T;

  int cv[9];
  u32x4 csrd = make_srd(w_in, 576*9216);
  int p1dst = (w & 1)*9216, p1so = 32*(w & 1)*9216 + (FH + 144*pj)*2, p1step = 64*9216;
  int cq[9];
  auto p1_offsets = [&](auto QKV, int (&dst)[9]) {
    int l3 = lane; OPAQUE(l3);
    int r = l3 / 18, ch = l3 % 18;
    #pragma unroll
    for (int t = 0; t < 9; t++) {
      const int kp = kperm(r);
      dst[t] = (QKV.value ? kp*3456 + (ch < 15 ? (ch / 5)*1152 + (ch % 5)*16 : 0) : kp*9216 + ch*16) - (t & 3)*1024;
      r += 3; ch += 10; if (ch >= 18) { ch -= 18; r += 1; }
    }
  };
  auto p1_issue = [&]() {
    SETM0(p1dst); DMA(cv[0], csrd, p1so, 0); DMA(cv[1], csrd, p1so, 1024); DMA(cv[2], csrd, p1so, 2048); DMA(cv[3], csrd, p1so, 3072);
    SETM0(p1dst + 4096); DMA(cv[4], csrd, p1so, 0); DMA(cv[5], csrd, p1so, 1024); DMA(cv[6], csrd, p1so, 2048); DMA(cv[7], csrd, p1so, 3072);
    SETM0(p1dst + 8192); DMA(cv[8], csrd, p1so, 0);
    p1dst = (p1dst >= 4*P1_STAGE) ? p1dst - 4*P1_STAGE : p1dst + P1_STAGE; p1so += p1step;
  };
  const bool isD = (wave == 4 || wave == 5);

  if (wave >= 4) __builtin_amdgcn_s_setprio(3);
  const int mt = wave >> 2;
  bf16x8 xn[36];
  {
    const u32x4 s_x = make_srd(x + (size_t)(128*pi + 32*w)*D_, 32*1152);
    const int a = lane >> 2, bq = lane & 3;
    unsigned tagv;
    asm volatile("global_load_dword %0, %1, %2 sc1" : "=v"(tagv) : "v"(0), "s"(ws) : "memory");
    u32x4 raw[18];
    {
      const int vx = (16*mt + a)*1152 + bq*16;
      static_for<0, 18>([&](auto C) { u32x4 t; asm volatile("buffer_load_dwordx4 %0, %1, %2, 0 offen offset:%3" : "=v"(t) : "v"(vx), "s"(s_x), "i"(C.value*64) : "memory"); raw[C.value] = t; });
    }
    if (isD) { p1_offsets(IC<0>{}, cv); p1_issue(); }
    if (isD) WAITV(9); else WAITV(0);
    asm volatile("" : "+v"(tagv));
    T = (unsigned)__builtin_amdgcn_readfirstlane(tagv) + 1u;
    asm volatile("" : "+v"(raw[0]), "+v"(raw[1]), "+v"(raw[2]), "+v"(raw[3]), "+v"(raw[4]), "+v"(raw[5]), "+v"(raw[6]), "+v"(raw[7]), "+v"(raw[8]),
                      "+v"(raw[9]), "+v"(raw[10]), "+v"(raw[11]), "+v"(raw[12]), "+v"(raw[13]), "+v"(raw[14]), "+v"(raw[15]), "+v"(raw[16]), "+v"(raw[17]));
    float s1 = 0.f, s2 = 0.f, s3 = 0.f, s4 = 0.f;
    unsigned ones = 0x3f803f80u; OPAQUE(ones);
    #pragma unroll
    for (int c = 0; c < 18; c++) {
      s1 = dot2(raw[c][0], ones, s1); s2 = dot2(raw[c][0], raw[c][0], s2); s3 = dot2(raw[c][1], ones, s3); s4 = dot2(raw[c][1], raw[c][1], s4);
      s1 = dot2(raw[c][2], ones, s1); s2 = dot2(raw[c][2], raw[c][2], s2); s3 = dot2(raw[c][3], ones, s3); s4 = dot2(raw[c][3], raw[c][3], s4);
    }
    s1 += s3; s2 += s4;
    s1 += dpp_f<0xB1>(s1); s1 += dpp_f<0x4E>(s1);
    s2 += dpp_f<0xB1>(s2); s2 += dpp_f<0x4E>(s2);
    const float mean = s1*(1.f/576.f), var = fmaxf(s2*(1.f/576.f) - mean*mean, 0.f);
    const float rs = __builtin_amdgcn_rsqf(var + 1e-5f), sh = -mean*rs;
    const f32x2 rs2 = {rs, rs}, sh2 = {sh, sh};
    int xw = XCH_OFF + w*18432 + (bq >> 1)*1024 + (32*(bq & 1) + ((16*mt + a) ^ (2*bq)))*16;
    int xr0 = XCH_OFF + w*18432 + (32*(lane >> 5) + ((lane & 31) ^ (2*(lane >> 5))))*16;
    int xr1 = XCH_OFF + w*18432 + 1024 + (32*(lane >> 5) + ((lane & 31) ^ (4 + 2*(lane >> 5))))*16;
    OPAQUE(xw); OPAQUE(xr0); OPAQUE(xr1);
    u32x4 xo[18];
    #pragma unroll
    for (int c = 0; c < 18; c++)
      #pragma unroll
      for (int d = 0; d < 4; d++) xo[c][d] = pack2(__builtin_elementwise_fma(f32x2{bf_lo(raw[c][d]), bf_hi(raw[c][d])}, rs2, sh2));
    #pragma unroll
    for (int c = 0; c < 9; c++) *(LAS u32x4*)(lds + xw + c*2048) = xo[c];
    WAITL(0);
    if (wave >= 6) {
      const u32x4 s_a = make_srd(w_in, 576*9216), s_b = make_srd(wqkv, 576*3456);
      SETM0(149504);
      const int vl = lane*16;
      #pragma unroll
      for (int k = 0; k < 11; k++) DMA(vl, s_a, b*20736 + ((w - 2)*11 + k)*1024, 0);
      #pragma unroll
      for (int k = 0; k < 4; k++) DMA(vl, s_b, b*7776 + ((w - 2)*4 + k)*1024, 0);
    }
    BAR();
    if (isD) p1_issue();
    if (wave < 4) {
      #pragma unroll
      for (int c = 0; c < 9; c++) { xn[2*c] = *(LAS bf16x8*)(lds + xr0 + c*2048); xn[2*c + 1] = *(LAS bf16x8*)(lds + xr1 + c*2048); }
      WAITL(0);
    }
    BAR();
    #pragma unroll
    for (int c = 0; c < 9; c++) *(LAS u32x4*)(lds + xw + c*2048) = xo[9 + c];
    if (isD) WAITV(9);
    WAITL(0);
    BAR();
    if (isD) { p1_issue(); p1_issue(); p1_offsets(IC<1>{}, cq); }
    if (wave < 4) {
      #pragma unroll
      for (int c = 0; c < 9; c++) { xn[18 + 2*c] = *(LAS bf16x8*)(lds + xr0 + c*2048); xn[18 + 2*c + 1] = *(LAS bf16x8*)(lds + xr1 + c*2048); }
    }
  }

  unsigned z1p[9][4];
  f32x4 acc[9][2];

  if (wave < 4) {
    constexpr int D = 3, NB = 4;
    int wl = (8*(lane >> 5) + 2*((lane >> 2) & 3))*288 + ((lane >> 4) & 1)*32 + (lane & 3)*8;
    int slot_off = 0;
    f32x16 a32[5];
    auto body = [&](auto NTC) {
      constexpr int NT = NTC.value, FS = 4*NT;
      bf16x8 wf[NB];
      int wc = wl + slot_off, wn;
      OPAQUE(wc);
      auto rdw = [&](auto G, int base) {
        constexpr int i = G.value % FS, k16 = i / NT, n = i % NT;
        LAS char* p = lds + base + (k16 >> 1)*9216 + (k16 & 1)*16*288 + n*64;
        s16x4 lo = __builtin_amdgcn_ds_read_tr16_b64_v4i16((LAS s16x4*)p);
        s16x4 hi = __builtin_amdgcn_ds_read_tr16_b64_v4i16((LAS s16x4*)(p + 288));
        wf[G.value % NB] = __builtin_shufflevector(lo, hi, 0, 1, 2, 3, 4, 5, 6, 7);
      };
      #pragma unroll
      for (int n = 0; n < NT; n++)
        #pragma unroll
        for (int e = 0; e < 16; e++) a32[n][e] = 0.f;
      static_for<0, D>([&](auto G) { rdw(G, wc); });
      SB();
            static_for<0, 9*FS>([&](auto G) {
        constexpr int st = G.value / FS, i = G.value % FS, k16 = i / NT, n = i % NT;
        if constexpr (i == 0) { slot_off = (slot_off >= 4*P1_STAGE) ? 0 : slot_off + P1_STAGE; wn = wl + slot_off; OPAQUE(wn); }
        if constexpr (i == FS - D) { SB(); if constexpr (NT == 5) PIN5(a32); else asm volatile("" : "+v"(a32[0]), "+v"(a32[1]), "+v"(a32[2]), "+v"(a32[3]));
          BAR(); SB(); }
        a32[n] = MFMA32(wf[G.value % NB], xn[4*st + k16], a32[n]);
        if constexpr (G.value + D < 9*FS) { if constexpr (i + D < FS) rdw(IC<G.value + D>{}, wc); else rdw(IC<G.value + D>{}, wn); }
        SG(0x8, 1); SG(0x100, 2);
        SB();
        if constexpr (i == FS - 1) wc = wn;
      });
      if constexpr (NT == 5) PIN5(a32); else asm volatile("" : "+v"(a32[0]), "+v"(a32[1]), "+v"(a32[2]), "+v"(a32[3]));
    };
    #pragma unroll 1
    for (int rep = 0; rep < 2; rep++) {
      asm volatile("" : "+s"(rep));
      body(IC<5>{});
      if (rep == 0) {
        {
      int l2 = lane; OPAQUE(l2);
      const int sr = l2 & 31, h4 = (l2 >> 5)*4;
      auto pk4 = [&](int n, int rq) { return u32x2{pack2(f32x2{a32[n][4*rq], a32[n][4*rq + 1]}), pack2(f32x2{a32[n][4*rq + 2], a32[n][4*rq + 3]})}; };
        f32x2 q1 = {0.f, 0.f}, q2 = {0.f, 0.f};
        LAS char* p = lds + ZB_OFF + w*IMG_W + sr*IMG_PITCH + h4*2;
        #pragma unroll
        for (int n = 0; n < 5; n++)
          #pragma unroll
          for (int rq = 0; rq < 4; rq++) if (n < 4 || rq < 2) {
            const f32x2 ya = gelu2(f32x2{a32[n][4*rq], a32[n][4*rq + 1]}), yb = gelu2(f32x2{a32[n][4*rq + 2], a32[n][4*rq + 3]});
            q1 += ya; q2 = __builtin_elementwise_fma(ya, ya, q2);
            q1 += yb; q2 = __builtin_elementwise_fma(yb, yb, q2);
            *(LAS u32x2*)(p + n*64 + rq*16) = u32x2{pack2(ya), pack2(yb)};
          }
        float t1 = q1[0] + q1[1], t2 = q2[0] + q2[1];
        t1 += __shfl_xor(t1, 32); t2 += __shfl_xor(t2, 32);
        const u32x4 rec = {__builtin_bit_cast(unsigned, t1), T, __builtin_bit_cast(unsigned, t2), T};
        if (l2 < 32) __builtin_amdgcn_raw_buffer_store_b128(rec, MKRSRC(ws + WS_STAT, 2048*256), ((pi*16 + pj)*128 + 32*w + sr)*16, 0, AUX_SC1);
        WAITL(0);
        }
        body(IC<4>{});
        {
      int l2 = lane; OPAQUE(l2);
      const int sr = l2 & 31, h4 = (l2 >> 5)*4;
      auto pk4 = [&](int n, int rq) { return u32x2{pack2(f32x2{a32[n][4*rq], a32[n][4*rq + 1]}), pack2(f32x2{a32[n][4*rq + 2], a32[n][4*rq + 3]})}; };
        LAS char* p = lds + QB_OFF + (16 + 32*w + sr)*QB_PITCH + h4*2;
        #pragma unroll
        for (int n = 0; n < 4; n++)
          #pragma unroll
          for (int rq = 0; rq < 4; rq++) if (n < 3 || rq < 3 || h4 == 0) *(LAS u32x2*)(p + n*64 + rq*16) = pk4(n, rq);
        WAITL(0);
        }
      }
    }
    {
      int l2 = lane; OPAQUE(l2);
      const int sr = l2 & 31, h4 = (l2 >> 5)*4;
      LAS char* p = lds + Z1B_OFF + w*IMG_W + sr*IMG_PITCH + h4*2;
      #pragma unroll
      for (int n = 0; n < 5; n++)
        #pragma unroll
        for (int rq = 0; rq < 4; rq++) if (n < 4 || rq < 2)
          *(LAS u32x2*)(p + n*64 + rq*16) = u32x2{pack2(f32x2{a32[n][4*rq], a32[n][4*rq + 1]}), pack2(f32x2{a32[n][4*rq + 2], a32[n][4*rq + 3]})};
      WAITL(0);
    }
    BAR();
  } else {
    const int g = lane >> 4, fl = lane & 15;
    __builtin_amdgcn_s_setprio(3);
    if (isD) {
      WAITV(18); BAR();
      #pragma unroll 1
      for (int q = 1; q < 6; q++) { p1_issue(); WAITV(18); BAR(); }
      #pragma unroll
      for (int t = 0; t < 9; t++) { const int t_ = cv[t]; cv[t] = cq[t]; cq[t] = t_; }
      csrd = make_srd(wqkv, 576*3456); p1so = 32*(w & 1)*3456 + ((72*pj) & ~15); p1step = 64*3456;
      #pragma unroll 1
      for (int q = 6; q < 15; q++) { p1_issue(); WAITV(18); BAR(); }
      #pragma unroll
      for (int t = 0; t < 9; t++) cv[t] = cq[t];
      csrd = make_srd(w_in, 576*9216); p1so = 32*(w & 1)*9216 + 144*pj*2; p1step = 64*9216;
      #pragma unroll 1
      for (int q = 15; q < 24; q++) {
        if (q == 19 && wave == 4 && pi < 15) {
          const RSRC r_halo = MKRSRC(ws + WS_HALO + (size_t)(pi*16 + pj)*4096, 4096);
          #pragma unroll
          for (int c = 0; c < 4; c++) { int ch = c*64 + lane; if (ch > 247) ch = 247;
            u32x4 v = *(LAS u32x4*)(lds + QB_OFF + 128*QB_PITCH + ch*16);
            __builtin_amdgcn_raw_buffer_store_b128(v, r_halo, ch*16, 0, AUX_SC1); }
        }
        p1_issue(); WAITV(18); BAR();
        if (q == 20 && wave == 4 && lane == 0 && pi < 15) set_flag(ws, FL_HALO, pi, pj, T);
      }
      u32x4 hv[4]; unsigned hf = T;
      const bool hal = (wave == 5 && pi > 0);
      gu32* fp = (gu32*)(ws + WS_FLAGS + (size_t)(FL_HALO*16 + (pi > 0 ? pi - 1 : 0))*256) + pj;
      if (hal) asm volatile("global_load_dword %0, %1, %2 sc1" : "=v"(hf) : "v"(0), "s"(fp) : "memory");
      if (hal) WAITV(10); else WAITV(9);
      BAR();
      if (hal) {
        asm volatile("s_waitcnt vmcnt(0)" : "+v"(hf) :: "memory");
        if (__builtin_amdgcn_readfirstlane(hf) != T)
          for (int spin = 0; spin < SPIN_MAX; ++spin) { if (__builtin_amdgcn_readfirstlane(__hip_atomic_load(fp, RLX_AGENT)) == T) break; __builtin_amdgcn_s_sleep(1); }
        const u32x4 s_hal = make_srd(ws + WS_HALO + (size_t)((pi - 1)*16 + pj)*4096, 4096);
        static_for<0, 4>([&](auto C) { int ch = C.value*64 + lane; if (ch > 247) ch = 247; u32x4 t;
          asm volatile("buffer_load_dwordx4 %0, %1, %2, 0 offen sc1" : "=v"(t) : "v"(ch*16), "s"(s_hal) : "memory"); hv[C.value] = t; });
      } else WAITV(0);
      BAR();
      if (wave == 5) {
        if (pi > 0) asm volatile("s_waitcnt vmcnt(0)" : "+v"(hv[0]), "+v"(hv[1]), "+v"(hv[2]), "+v"(hv[3]) :: "memory");
        #pragma unroll
        for (int c = 0; c < 4; c++) { int ch = c*64 + lane; if (ch > 247) ch = 247; *(LAS u32x4*)(lds + QB_OFF + ch*16) = (pi > 0) ? hv[c] : u32x4{0, 0, 0, 0}; }
        WAITL(0);
      }
      BAR();
    } else {
      const int gA = 2*(w - 2);
      {
        const u32x4 s_a = make_srd(w_sp, 2048*4096), s_b = make_srd(w_out, 2304*1152), s_c = make_srd(wo, 576*1152);
        SETM0(149504);
        const int vl = lane*16;
        auto pf = [&](int k) {
          if (k < 16) DMA(vl, s_a, b*32768 + ((w - 2)*16 + k)*1024, 0);
          else if (k < 22) DMA(vl, s_b, b*10368 + ((w - 2)*6 + k - 16)*1024, 0);
          else if (k < 24) DMA(vl, s_c, b*2592 + ((w - 2)*2 + k - 22)*1024, 0);
        };
        #pragma unroll 1
        for (int q = 0; q < 16; q++) {
          if (q >= 0) { for (int k = (q - 0)*2; k < (q - 0 + 1)*2; k++) pf(k); }
          BAR();
        }
      }
      LAS f32x2* st = (LAS f32x2*)(lds + MISC_OFF + (w - 2)*512);
      {
        const u32x4 s_st = make_srd(ws + WS_STAT, 2048*256);
        const int vo = (pi*16*128 + 32*gA + lane)*16;
        u32x4 rec[16];
        static_for<0, 16>([&](auto C) { u32x4 t; asm volatile("buffer_load_dwordx4 %0, %1, %2, %3 offen sc1" : "=v"(t) : "v"(vo), "s"(s_st), "s"(C.value*2048) : "memory"); rec[C.value] = t; });
        WAITV(16);
        BAR();
        BAR();
        asm volatile("s_waitcnt vmcnt(0)" : "+v"(rec[0]), "+v"(rec[1]), "+v"(rec[2]), "+v"(rec[3]), "+v"(rec[4]), "+v"(rec[5]), "+v"(rec[6]), "+v"(rec[7]),
                          "+v"(rec[8]), "+v"(rec[9]), "+v"(rec[10]), "+v"(rec[11]), "+v"(rec[12]), "+v"(rec[13]), "+v"(rec[14]), "+v"(rec[15]) :: "memory");
        float t1 = 0.f, t2 = 0.f; bool ok = true;
        #pragma unroll
        for (int c = 0; c < 16; c++) { t1 += asf(rec[c][0]); t2 += asf(rec[c][2]); ok = ok && rec[c][1] == T && rec[c][3] == T; }
        if (!__all(ok)) {
          const RSRC r_stat = MKRSRC(ws + WS_STAT, 2048*256);
          for (int spin = 0; spin < SPIN_MAX; ++spin) {
            t1 = 0.f; t2 = 0.f; ok = true;
            #pragma unroll
            for (int c = 0; c < 16; c++) {
              u32x4 v = __builtin_amdgcn_raw_buffer_load_b128(r_stat, vo + c*2048, 0, AUX_SC1);
              t1 += asf(v[0]); t2 += asf(v[2]); ok = ok && v[1] == T && v[3] == T;
            }
            if (__all(ok)) break;
            __builtin_amdgcn_s_sleep(1);
          }
        }
        const float mean = t1*(1.f/2304.f), var = fmaxf(t2*(1.f/2304.f) - mean*mean, 0.f);
        const float rs = __builtin_amdgcn_rsqf(var + 1e-5f);
        st[lane] = f32x2{rs, -mean*rs};
        WAITL(0);
        BAR();
      }
      const RSRC r_z2n = MKRSRC(ws + WS_Z2N, 16*ZJS);
      #pragma unroll 1
      for (int k = 0; k < 2; k++) {
        f32x2 r2[4], h2[4];
        #pragma unroll
        for (int d = 0; d < 4; d++) { f32x2 u0 = st[32*k + 8*g + 2*d], u1 = st[32*k + 8*g + 2*d + 1]; r2[d] = f32x2{u0[0], u1[0]}; h2[d] = f32x2{u0[1], u1[1]}; }
        LAS char* p = lds + ZB_OFF + (gA + k)*IMG_W + (8*g + ((lane >> 2) & 3))*IMG_PITCH + (lane & 3)*8;
        int zo = lane*16 + pj*ZJS + (4*pi + gA + k)*9216;
        #pragma unroll 1
        for (int j3 = 0; j3 < 3; j3++, p += 96, zo += 3072) {
          u32x4 v[3];
          #pragma unroll
          for (int u = 0; u < 3; u++) {
            s16x4 lo = __builtin_amdgcn_ds_read_tr16_b64_v4i16((LAS s16x4*)(p + u*32));
            s16x4 hi = __builtin_amdgcn_ds_read_tr16_b64_v4i16((LAS s16x4*)(p + u*32 + 4*IMG_PITCH));
            v[u] = __builtin_bit_cast(u32x4, __builtin_shufflevector(lo, hi, 0, 1, 2, 3, 4, 5, 6, 7));
          }
          #pragma unroll
          for (int u = 0; u < 3; u++) {
            u32x4 o;
            #pragma unroll
            for (int d = 0; d < 4; d++) o[d] = pack2(f32x2{opq(__builtin_fmaf(bf_lo(v[u][d]), r2[d][0], h2[d][0])), opq(__builtin_fmaf(bf_hi(v[u][d]), r2[d][1], h2[d][1]))});
            __builtin_amdgcn_raw_buffer_store_b128(o, r_z2n, zo + u*1024, 0, AUX_SC1);
          }
          BAR();
        }
      }
      BAR();
      BAR();
    }
    BAR();
    if (!isD) { WAITV(0); if (lane == 0) set_flag(ws, FL_Z, pj, 2*pi + (w - 2), T); }
    {
      #pragma unroll
      for (int m = 0; m < 2; m++) {
        LAS char* zx = lds + Z1B_OFF + w*IMG_W + (8*(fl >> 2) + 4*m + (fl & 3))*IMG_PITCH + g*8;
        #pragma unroll
        for (int n = 0; n < 9; n++) { u32x2 v = *(LAS u32x2*)(zx + n*32); z1p[n][2*m] = v[0]; z1p[n][2*m + 1] = v[1]; }
      }
    }
  }

  const u32x4 s_z2n = make_srd(ws + WS_Z2N + (size_t)pj*ZJS, 64*9*1024);
  const u32x4 s_wsp = make_srd(w_sp + (size_t)128*pi*2048, 128*4096);
  int vw[4];
  const int vlin = lane*16;
  auto p2_issue = [&](int s, auto ZR) {
    const int base = (s & 3)*P2_STAGE;
    if constexpr (ZR.value) {
      static_for<0, 9>([&](auto K) { SETM0(base + w*9216 + K.value*1024); DMAS(vlin, s_z2n, ((2*s + w)*9 + K.value)*1024, 0); });
    } else {
      static_for<0, 8>([&](auto K) { constexpr int k = K.value; const int wr = 2*(w - 2) + (k >> 2);
        SETM0(base + 18432 + wr*4096 + (k & 3)*1024); DMA(vw[k & 3], s_wsp, wr*32*4096 + s*128, 0); });
    }
  };
  if (wave >= 4) {
    { int l2 = lane; OPAQUE(l2);
      const int a = l2 >> 3, bb = l2 & 7;
      #pragma unroll
      for (int m = 0; m < 2; m++)
        #pragma unroll
        for (int hf = 0; hf < 2; hf++) { int rho = 8*hf + a; vw[2*m + hf] = (8*(rho >> 2) + 4*m + (rho & 3))*4096 + ((bb ^ ((rho >> 1) & 7))*16); } }
    if (w >= 2) {
      #pragma unroll 1
      for (int s = 0; s < 2; s++) p2_issue(s, IC<0>{});
    }
  }
  {
      int t2 = tid; OPAQUE(t2);
      const int u = t2 & 1, hh = (t2 >> 7) & 1, tl = ((t2 >> 1) & 63) + 64*(t2 >> 8);
      LAS char* rowp = lds + QB_OFF + (16 + tl)*QB_PITCH + 8*(pj & 1) + 36*hh;
      const int o8 = hh*4, o4 = hh ? 0 : 32;
      u32x2 qv[4]; unsigned q0;
      #pragma unroll
      for (int d = 0; d < 4; d++) qv[d] = *(LAS u32x2*)(rowp + o8 + d*8);
      q0 = *(LAS unsigned*)(rowp + o4);
      float lg[4];
      #pragma unroll
      for (int ww = 0; ww < 4; ww++) {
        LAS char* kr = rowp - (2*(4*u + ww))*QB_PITCH + 80;
        float sa = dot2(q0, *(LAS unsigned*)(kr + o4), 0.f), sb = 0.f;
        #pragma unroll
        for (int d = 0; d < 4; d++) { u32x2 kv = *(LAS u32x2*)(kr + o8 + d*8); sa = dot2(qv[d][0], kv[0], sa); sb = dot2(qv[d][1], kv[1], sb); }
        float s = (sa + sb)*0.23570226039551584f;
        if (128*pi + tl - 2*(4*u + ww) < 0) s = -1e9f;
        lg[ww] = s;
      }
      float l8[8];
      #pragma unroll
      for (int ww = 0; ww < 4; ww++) { float o = dpp_f<0xB1>(lg[ww]); l8[ww] = u ? o : lg[ww]; l8[4 + ww] = u ? lg[ww] : o; }
      float mx = l8[0];
      #pragma unroll
      for (int ww = 1; ww < 8; ww++) mx = fmaxf(mx, l8[ww]);
      float sum = 0.f;
      #pragma unroll
      for (int ww = 0; ww < 8; ww++) { l8[ww] = __builtin_amdgcn_exp2f((l8[ww] - mx)*1.4426950408889634f); sum += l8[ww]; }
      const float inv = __builtin_amdgcn_rcpf(sum);
      float olo[5], ohi[5];
      #pragma unroll
      for (int d = 0; d < 5; d++) { olo[d] = 0.f; ohi[d] = 0.f; }
      #pragma unroll
      for (int ww = 0; ww < 8; ww++) {
        const unsigned pb = pack_bf16(l8[ww]*inv, 0.f);
        const unsigned ph = pb << 16;
        LAS char* vr = rowp - (2*ww)*QB_PITCH + 160;
        u32x2 va = *(LAS u32x2*)(vr + o8 + 16*u), vb = *(LAS u32x2*)(vr + o8 + 16*u + 8); unsigned vc = *(LAS unsigned*)(vr + o4);
        olo[0] = dot2(va[0], pb, olo[0]); ohi[0] = dot2(va[0], ph, ohi[0]); olo[1] = dot2(va[1], pb, olo[1]); ohi[1] = dot2(va[1], ph, ohi[1]);
        olo[2] = dot2(vb[0], pb, olo[2]); ohi[2] = dot2(vb[0], ph, ohi[2]); olo[3] = dot2(vb[1], pb, olo[3]); ohi[3] = dot2(vb[1], ph, ohi[3]);
        olo[4] = dot2(vc, pb, olo[4]); ohi[4] = dot2(vc, ph, ohi[4]);
      }
      *(LAS u32x2*)(rowp + o8 + 16*u) = u32x2{pack_bf16(olo[0], ohi[0]), pack_bf16(olo[1], ohi[1])};
      *(LAS u32x2*)(rowp + o8 + 16*u + 8) = u32x2{pack_bf16(olo[2], ohi[2]), pack_bf16(olo[3], ohi[3])};
      if (u) *(LAS unsigned*)(rowp + o4) = pack_bf16(olo[4], ohi[4]);
      }
  WAITL(0);
  BAR();
  if (wave >= 4 && w < 2) { wait_flags(ws, FL_Z, pj, 32, T, lane); p2_issue(0, IC<1>{}); p2_issue(1, IC<1>{}); }
  if (wave >= 4) {
    #pragma unroll
    for (int n = 0; n < 3; n++) {
      #pragma unroll
      for (int d = 0; d < 4; d++) z1p[n][d] = pack2(gelu2(f32x2{bf_lo(z1p[n][d]), bf_hi(z1p[n][d])}));
      asm volatile("" : "+v"(z1p[n][0]), "+v"(z1p[n][1]), "+v"(z1p[n][2]), "+v"(z1p[n][3]));
    }
  }
  if (wave < 4) {
    const RSRC r_o = MKRSRC(ws + WS_O, 128*OTS);
    if (tid < 252) {
      const int r0 = tid / 9, p = tid % 9;
      const int col = 36*pj + 4*p, c32 = col & 31;
      const int cpart = ((col >> 5)*64 + 16*((c32 & 15) >> 2))*16 + (c32 >> 4)*8;
      LAS char* src = lds + QB_OFF + (16 + r0)*QB_PITCH + 8*(pj & 1) + 8*p;
      #pragma unroll
      for (int it = 0; it < 5; it++) {
        const int tl = r0 + 28*it;
        if (tl < 128) {
          u32x2 v = *(LAS u32x2*)(src + it*28*QB_PITCH);
          const int r32 = tl & 31, tt = 8*pi + 2*(tl >> 5) + ((r32 >> 2) & 1), rho = 4*(r32 >> 3) + (r32 & 3);
          __builtin_amdgcn_raw_buffer_store_b64(v, r_o, tt*OTS + rho*16 + cpart, 0, AUX_SC1);
        }
      }
    }
  }
  WAITL(0);
  BAR();
  const int p3cnt = (xcd < 2) ? 30 : 29;
  const int p3T = xcd*29 + (xcd < 2 ? xcd : 2) + rr;
  const bool p3act = rr < p3cnt;
  const int rg = p3T / 9, pc = p3T % 9;

  if (wave >= 4) {
    __builtin_amdgcn_s_setprio(0);
    auto p2_helper = [&](auto ZR) {
      constexpr int CNT = ZR.value ? 9 : 8;
      p2_issue(2, ZR); p2_issue(3, ZR);
      WAITV(3*CNT);
      BAR();
      auto piece = [&](int s, auto K) {
        constexpr int k = K.value; const int base = (s & 3)*P2_STAGE;
        if constexpr (ZR.value) { SETM0(base + w*9216 + k*1024); DMAS(vlin, s_z2n, ((2*s + w)*9 + k)*1024, 0); }
        else if constexpr (k < 8) { const int wr = 2*(w - 2) + (k >> 2); SETM0(base + 18432 + wr*4096 + (k & 3)*1024); DMA(vw[k & 3], s_wsp, wr*32*4096 + s*128, 0); }
      };
      auto step = [&](int s, auto PR) {
        WAITV(2*CNT);
        BAR();
        static_for<0, 9>([&](auto K) {
          piece(s + 4, K);
          if constexpr (PR.value >= 0 && K.value == 2) {
            constexpr int pr = PR.value;
            unsigned rr2 = pack2(f32x2{opq(gelu_f(opq(bf_lo(z1p[pr / 4][pr % 4])))), opq(gelu_f(opq(bf_hi(z1p[pr / 4][pr % 4]))))});
            asm volatile("" : "+v"(rr2));
            z1p[pr / 4][pr % 4] = rr2;
          }
        });
      };
      static_for<0, 24>([&](auto S) {
        step(S.value, IC<12 + S.value>{});
        if constexpr (S.value == 4) { if (wave == 4 && lane == 0) set_flag(ws, FL_O, pi, pj, T); }
      });
      #pragma unroll 1
      for (int s = 24; s < 28; s++) step(s, IC<-1>{});
      WAITV(2*CNT); BAR();
      WAITV(CNT); BAR();
      #pragma unroll
      for (int n = 0; n < 9; n++) *(LAS u32x4*)(lds + w*9216 + n*1024 + lane*16) = u32x4{z1p[n][0], z1p[n][1], z1p[n][2], z1p[n][3]};
      WAITV(0); WAITL(0); BAR();
      BAR();
    };
    if (w < 2) p2_helper(IC<1>{}); else p2_helper(IC<0>{});
    if (p3act) {
      const u32x4 s_o = make_srd(ws + WS_O, 128*OTS), s_h = make_srd(ws + WS_H, 128*HTS);
      const u32x4 s_wo = make_srd(wo, 576*1152), s_wout = make_srd(w_out, 2304*1152);
      int vW0, vW1, vA[5];
      int l2 = lane; OPAQUE(l2);
      { const int r = l2 >> 3;
        vW0 = r*1152 + (((l2 & 7) ^ (((r >> 1) & 3) << 1))*16);
        vW1 = (r + 8)*1152 + (((l2 & 7) ^ ((((r + 8) >> 1) & 3) << 1))*16);
        #pragma unroll
        for (int m5 = 0; m5 < 5; m5++) { int tt = 5*rg + m5; if (tt > 127) tt = 127; vA[m5] = l2*16 + tt*OTS; } }
      int slot = 2;
      auto p3_w = [&](int sl, u32x4 sw, int R0, int R1) {
        const int base = sl*P3_STAGE + w*9216, s0 = R0*1152 + pc*128, s1 = R1*1152 + pc*128;
        SETM0(base + 5120); DMA(vW0, sw, s0, 0); SETM0(base + 6144); DMA(vW1, sw, s0, 0);
        SETM0(base + 7168); DMA(vW0, sw, s1, 0); SETM0(base + 8192); DMA(vW1, sw, s1, 0);
      };
      auto p3_a = [&](int sl, u32x4 sa, int ks) {
        const int base = sl*P3_STAGE + w*9216;
        static_for<0, 5>([&](auto M) { SETM0(base + M.value*1024); DMAS(vA[M.value], sa, ks*1024, 0); });
      };
      auto adv = [&]() { slot = (slot == 4) ? 0 : slot + 1; };
      auto p3_wo = [&](int u) { const int ks = 3*u + w; if (w < 3) { p3_w(slot, s_wo, 32*ks, 32*ks + 16); p3_a(slot, s_o, ks); } adv(); };
      auto g3_w = [&](int sl, int v) { const int ks = 3*v + w;
        const int R0 = (ks < 64) ? 144*(ks >> 2) + 32*(ks & 3) : 288*(ks - 64) + 128;
        p3_w(sl, s_wout, R0, R0 + ((ks < 64) ? 16 : 144)); };
      auto p3_g3 = [&](int v) { if (w < 3) { g3_w(slot, v); p3_a(slot, s_h, 3*v + w); } adv(); };
      const int p0 = (5*rg) >> 3, p1 = ((5*rg + 4 > 127 ? 127 : 5*rg + 4)) >> 3;
      if (w < 3) { wait_flags(ws, FL_O, p0, 16, T, lane); wait_flags(ws, FL_O, p1, 16, T, lane); }
      #pragma unroll 1
      for (int u = 0; u < 3; u++) p3_wo(u);
      BAR();
      p3_wo(3); p3_wo(4);
      WAITV(27);
      BAR();
      WAITV(18); BAR();
      p3_wo(5); WAITV(18); BAR();
      WAITV(9); BAR();
      WAITV(0); BAR();
      BAR();
      if (w < 3) { g3_w(3, 0); g3_w(4, 1); g3_w(0, 2); g3_w(1, 3); }
      #pragma unroll
      for (int m5 = 0; m5 < 5; m5++) { int tt = 5*rg + m5; if (tt > 127) tt = 127; vA[m5] = l2*16 + tt*HTS; }
      if (w < 3) { wait_flags(ws, FL_H, p0, 16, T, lane); wait_flags(ws, FL_H, p1, 16, T, lane); }
      if (w < 3) { p3_a(3, s_h, w); p3_a(4, s_h, 3 + w); p3_a(0, s_h, 6 + w); p3_a(1, s_h, 9 + w); }
      slot = 2;
      WAITV(10); BAR();
      p3_g3(4); WAITV(14); BAR();
      #pragma unroll 1
      for (int u = 7; u < 26; u++) {
        p3_g3(u - 2);
        WAITV(18); BAR();
      }
      WAITV(9); BAR();
      WAITV(0); BAR();
      BAR();
    }
  } else {
    int l2 = lane; OPAQUE(l2);
    const int g = l2 >> 4, fl = l2 & 15;
    const int zoff = l2*16;
    const int woff0 = 18432 + (4*w + (fl >> 3))*1024 + (fl & 7)*128 + ((g ^ ((fl >> 1) & 7))*16);
    float bs[2];
    #pragma unroll
    for (int m = 0; m < 2; m++) bs[m] = bf_lo(b_sp[128*pi + 32*w + 8*(fl >> 2) + 4*m + (fl & 3)]);
    #pragma unroll
    for (int n = 0; n < 9; n++) { acc[n][0] = f32x4{bs[0], bs[0], bs[0], bs[0]}; acc[n][1] = f32x4{bs[1], bs[1], bs[1], bs[1]}; }
    bf16x8 z0[9], w0[2], z1[9], w1[2];
    auto rd = [&](bf16x8 (&z)[9], bf16x8 (&wv)[2], int s, int kk) {
      const int base = (s & 3)*P2_STAGE;
      LAS char* pw = lds + base + (woff0 ^ (kk*64));
      LAS char* pz = lds + base + kk*9216 + zoff;
      wv[0] = *(LAS bf16x8*)pw; wv[1] = *(LAS bf16x8*)(pw + 2048);
      #pragma unroll
      for (int n = 0; n < 9; n++) z[n] = *(LAS bf16x8*)(pz + n*1024);
    };
    auto mm = [&](bf16x8 (&z)[9], bf16x8 (&wv)[2]) {
      #pragma unroll
      for (int n = 0; n < 9; n++) { acc[n][0] = MFMA(z[n], wv[0], acc[n][0]); acc[n][1] = MFMA(z[n], wv[1], acc[n][1]); }
      #pragma unroll
      for (int q = 0; q < 11; q++) { SG(0x8, 1); SG(0x100, 1); }
      SG(0x8, 7);
    };
    BAR();
    rd(z0, w0, 0, 0);
    CWAITL0(); SB();
    #pragma unroll 1
    for (int s = 0; s < 32; s++) {
      CWAITL0(); SB();
      rd(z1, w1, s, 1);
      mm(z0, w0);
      SB(); CWAITL0(); PIN_ACC(acc);
      if (s == 4) WAITV(0);
      BAR(); SB();
      rd(z0, w0, (s + 1 < 32 ? s + 1 : s), 0);
      mm(z1, w1);
      SB();
    }
    {
      const RSRC r_h = MKRSRC(ws + WS_H, 128*HTS);
      u32x4 z1g[9];
      #pragma unroll
      for (int n = 0; n < 9; n++) z1g[n] = *(LAS u32x4*)(lds + w*9216 + n*1024 + zoff);
      #pragma unroll
      for (int m = 0; m < 2; m++) {
        const int tt = 8*pi + 2*w + m;
        unsigned hp[9][2];
        #pragma unroll
        for (int n = 0; n < 9; n++) {
          hp[n][0] = pack2(f32x2{bf_lo(z1g[n][2*m]), bf_hi(z1g[n][2*m])}*f32x2{acc[n][m][0], acc[n][m][1]});
          hp[n][1] = pack2(f32x2{bf_lo(z1g[n][2*m + 1]), bf_hi(z1g[n][2*m + 1])}*f32x2{acc[n][m][2], acc[n][m][3]});
        }
        #pragma unroll
        for (int q = 0; q < 4; q++)
          __builtin_amdgcn_raw_buffer_store_b128(u32x4{hp[2*q][0], hp[2*q][1], hp[2*q + 1][0], hp[2*q + 1][1]}, r_h, zoff + tt*HTS + (4*pj + q)*1024, 0, AUX_SC1);
        __builtin_amdgcn_raw_buffer_store_b64(u32x2{hp[8][0], hp[8][1]}, r_h, zoff + (pj & 1)*8 + tt*HTS + (64 + (pj >> 1))*1024, 0, AUX_SC1);
      }
    }
    if (p3act) {
      WAITL(0);
      BAR();
      f32x4 aS[5], aC[5];
      u32x2 xv[5];
      const int q4 = (l2 >> 2) & 3, p4 = l2 & 3, r0 = 4*g + q4;
      const int woff = 5120 + r0*128 + (((2*w + (p4 >> 1)) ^ (((r0 >> 1) & 3) << 1))*16) + (p4 & 1)*8;
      bf16x8 hA[3][5], wA[3], hB[3][5], wB[3];
      int slot = 2;
      auto rd3 = [&](bf16x8 (&h)[3][5], bf16x8 (&wv)[3]) {
        LAS char* p = lds + slot*P3_STAGE;
        #pragma unroll
        for (int kk = 0; kk < 3; kk++) {
          s16x4 lo = __builtin_amdgcn_ds_read_tr16_b64_v4i16((LAS s16x4*)(p + kk*9216 + woff));
          s16x4 hi = __builtin_amdgcn_ds_read_tr16_b64_v4i16((LAS s16x4*)(p + kk*9216 + woff + 2048));
          wv[kk] = __builtin_shufflevector(lo, hi, 0, 1, 2, 3, 4, 5, 6, 7);
          #pragma unroll
          for (int m5 = 0; m5 < 5; m5++) h[kk][m5] = *(LAS bf16x8*)(p + kk*9216 + m5*1024 + zoff);
        }
        slot = (slot == 4) ? 0 : slot + 1;
      };
      auto mm3 = [&](bf16x8 (&h)[3][5], bf16x8 (&wv)[3]) {
        #pragma unroll
        for (int kk = 0; kk < 3; kk++)
          #pragma unroll
          for (int m5 = 0; m5 < 5; m5++) aC[m5] = MFMA(wv[kk], h[kk][m5], aC[m5]);
        #pragma unroll
        for (int q = 0; q < 15; q++) { SG(0x8, 1); SG(0x100, 1); }
        SG(0x100, 6);
      };
      #pragma unroll
      for (int m5 = 0; m5 < 5; m5++) {
        int tt = 5*rg + m5; if (tt > 127) tt = 127;
        const int row = 32*(tt >> 1) + 8*(fl >> 2) + 4*(tt & 1) + (fl & 3);
        xv[m5] = *(const u32x2*)(x + (size_t)row*D_ + 64*pc + 16*w + 4*g);
      }
      BAR();
      #pragma unroll 1
      for (int ph = 0; ph < 2; ph++) {
        #pragma unroll
        for (int m5 = 0; m5 < 5; m5++) aC[m5] = f32x4{0,0,0,0};
        rd3(hA, wA); SB();
        const int nit = ph ? 12 : 3;
        #pragma unroll 1
        for (int it = 0; it < nit; it++) {
          CWAITL0(); SB(); rd3(hB, wB); mm3(hA, wA); SB(); PIN5(aC); BAR(); SB();
          CWAITL0(); SB(); rd3(hA, wA); mm3(hB, wB); SB(); PIN5(aC);
          if (ph == 0 && it == 1) { WAITV(0); }
          if (ph == 0 || it < 11) BAR();
          SB();
          if (ph == 0 && it == 1 && tid == 0) set_flag(ws, FL_H, pi, pj, T);
        }
        if (ph == 0) {
          #pragma unroll
          for (int m5 = 0; m5 < 5; m5++) aS[m5] = aC[m5];
          slot = 3;
        }
      }
      #pragma unroll
      for (int m5 = 0; m5 < 5; m5++) {
        const int tt = 5*rg + m5;
        if (tt < 128) {
          const int row = 32*(tt >> 1) + 8*(fl >> 2) + 4*(tt & 1) + (fl & 3);
          const size_t o = (size_t)row*D_ + 64*pc + 16*w + 4*g;
          float r[4];
          #pragma unroll
          for (int e = 0; e < 4; e++) {
            float bv = aC[m5][e], sv = aS[m5][e];
            float sl = bv*__builtin_amdgcn_rcpf(1.f + __builtin_amdgcn_exp2f(-1.4426950408889634f*bv));
            unsigned xw = xv[m5][e >> 1];
            float xx = (e & 1) ? bf_hi(xw) : bf_lo(xw);
            r[e] = __builtin_fmaf(sv, sl, xx);
          }
          *(u32x2*)(out + o) = u32x2{pack_bf16(r[0], r[1]), pack_bf16(r[2], r[3])};
        }
      }
    } else {
      WAITV(0);
      BAR();
      if (tid == 0) set_flag(ws, FL_H, pi, pj, T);
    }
    if (b == 0 && tid == 0) __hip_atomic_store((gu32*)ws, T, RLX_AGENT);
  }
  asm volatile(".Lpf_end:");
}

extern "C" void kernel_launch(void* const* d_in, const int* in_sizes, int n_in, void* d_out, int out_size, void* d_ws, size_t ws_size, hipStream_t stream) {
  (void)hipFuncSetAttribute((const void*)fused_kernel, hipFuncAttributeMaxDynamicSharedMemorySize, LDS_BYTES);
  fused_kernel<<<256, 512, LDS_BYTES, stream>>>((const u16*)d_in[0], (const u16*)d_in[1], (const u16*)d_in[2], (const u16*)d_in[4],
      (const u16*)d_in[5], (const u16*)d_in[6], (const u16*)d_in[7], (u16*)d_out, (char*)d_ws);
}
```

```cpp
#include <hip/hip_runtime.h>
#include <stdint.h>

typedef unsigned short u16;
typedef __attribute__((ext_vector_type(8))) short bf16x8;
typedef __attribute__((ext_vector_type(4))) short s16x4;
typedef __attribute__((ext_vector_type(4))) float f32x4;
typedef __attribute__((ext_vector_type(16))) float f32x16;
typedef __attribute__((ext_vector_type(2))) float f32x2;
typedef __attribute__((ext_vector_type(4))) unsigned u32x4;
typedef __attribute__((ext_vector_type(2))) unsigned u32x2;
typedef __attribute__((ext_vector_type(2))) __bf16 bf16x2;
#define GAS __attribute__((address_space(1)))
#define LAS __attribute__((address_space(3)))
typedef GAS unsigned gu32;
#define RLX_AGENT __ATOMIC_RELAXED, __HIP_MEMORY_SCOPE_AGENT
#define BAR() asm volatile("s_barrier" ::: "memory")
#define SB() __builtin_amdgcn_sched_barrier(0)
#define SG(mask, n) __builtin_amdgcn_sched_group_barrier(mask, n, 0)
#define WAITV(n) asm volatile("s_waitcnt vmcnt(%0)" :: "i"(n) : "memory")
#define WAITL(n) asm volatile("s_waitcnt lgkmcnt(%0)" :: "i"(n) : "memory")
#define CWAITL0() __builtin_amdgcn_s_waitcnt(0xC07F)
#define RSRC __amdgpu_buffer_rsrc_t
#define MKRSRC(p, bytes) __builtin_amdgcn_make_buffer_rsrc((void*)(p), 0, (bytes), 0x00020000)
#define AUX_SC1 16
#define OPAQUE(x) asm volatile("" : "+v"(x))

constexpr int D_ = 576, FH = 2304;
constexpr size_t WS_FLAGS = 4096;
constexpr size_t WS_STAT  = 32768;
constexpr size_t WS_HALO  = WS_STAT + 2048*256;
constexpr int ZJS = 64*9*1024 + 256, HTS = 72*1024 + 256, OTS = 18*1024 + 256;
constexpr size_t WS_Z2N   = WS_HALO + 16*16*4096;
constexpr size_t WS_H     = WS_Z2N + (size_t)16*ZJS;
constexpr size_t WS_O     = WS_H + (size_t)128*HTS;
constexpr int FL_Z = 1, FL_HALO = 2, FL_O = 3, FL_H = 4;
constexpr int LDS_BYTES = 163840;
constexpr int P1_STAGE = 18432, P1_NS = 5;
constexpr int XCH_OFF = 4*P1_STAGE;
constexpr int ZB_OFF = P1_NS*P1_STAGE;
constexpr int QB_PITCH = 248;
constexpr int IMG_PITCH = 296, IMG_W = 32*IMG_PITCH;
constexpr int QB_OFF = ZB_OFF + 4*IMG_W - 16*QB_PITCH;
constexpr int MISC_OFF = QB_OFF + 144*QB_PITCH;
constexpr int Z1B_OFF = 4*P1_STAGE;
constexpr int P2_STAGE = 34816;
constexpr int P3_STAGE = 27648;
constexpr int SPIN_MAX = 1 << 18;

template<int N> struct IC { static constexpr int value = N; };
template<int B, int E, class F> __device__ __forceinline__ void static_for(F&& f) {
  if constexpr (B < E) { f(IC<B>{}); static_for<B + 1, E>(f); }
}

__device__ __forceinline__ unsigned pack2(f32x2 v) { return __builtin_bit_cast(unsigned, __builtin_convertvector(v, bf16x2)); }
__device__ __forceinline__ float opq(float x) { asm("" : "+v"(x)); return x; }
__device__ __forceinline__ unsigned pack_bf16(float a, float b) { return pack2(f32x2{a, b}); }
__device__ __forceinline__ float asf(unsigned u) { return __builtin_bit_cast(float, u); }
__device__ __forceinline__ float bf_lo(unsigned u) { return __builtin_bit_cast(float, u << 16); }
__device__ __forceinline__ float bf_hi(unsigned u) { return __builtin_bit_cast(float, u & 0xffff0000u); }
__device__ __forceinline__ float dot2(unsigned a, unsigned b, float c) {
  return __builtin_amdgcn_fdot2_f32_bf16(__builtin_bit_cast(bf16x2, a), __builtin_bit_cast(bf16x2, b), c, false);
}
__device__ __forceinline__ float gelu_f(float x) {
  const float c1 = -2.302208198f, c2 = -0.1029432397f;
  float t = __builtin_fmaf(x * x, c2, c1);
  float e = __builtin_amdgcn_exp2f(x * t);
  return x * __builtin_amdgcn_rcpf(1.f + e);
}
__device__ __forceinline__ f32x2 gelu2(f32x2 x) {
  const f32x2 c1 = {-2.302208198f, -2.302208198f}, c2 = {-0.1029432397f, -0.1029432397f}, one = {1.f, 1.f};
  f32x2 t = __builtin_elementwise_fma(x * x, c2, c1);
  f32x2 a = x * t;
  f32x2 e = {__builtin_amdgcn_exp2f(a[0]), __builtin_amdgcn_exp2f(a[1])};
  f32x2 d = e + one;
  f32x2 r = {__builtin_amdgcn_rcpf(d[0]), __builtin_amdgcn_rcpf(d[1])};
  return x * r;
}
template<int CTRL> __device__ __forceinline__ float dpp_f(float v) {
  return __builtin_bit_cast(float, __builtin_amdgcn_update_dpp(0, __builtin_bit_cast(int, v), CTRL, 0xf, 0xf, true));
}
__device__ __forceinline__ float row16_allsum(float v) {
  v += dpp_f<0x128>(v); v += dpp_f<0x124>(v); v += dpp_f<0x122>(v); v += dpp_f<0x121>(v); return v;
}
__device__ __forceinline__ bool poll_flags(const char* ws, int group, int idx, int nflags, unsigned T, int lane) {
  gu32* p = (gu32*)(ws + WS_FLAGS + (size_t)(group*16 + idx)*256) + (lane & (nflags - 1));
  unsigned v = __hip_atomic_load(p, RLX_AGENT);
  return __all(v == T);
}
__device__ __forceinline__ void wait_flags(const char* ws, int group, int idx, int nflags, unsigned T, int lane) {
  for (int spin = 0; spin < SPIN_MAX; ++spin) {
    if (poll_flags(ws, group, idx, nflags, T, lane)) break;
    __builtin_amdgcn_s_sleep(1);
  }
}
__device__ __forceinline__ void set_flag(char* ws, int group, int idx, int k, unsigned T) {
  __hip_atomic_store((gu32*)(ws + WS_FLAGS + (size_t)(group*16 + idx)*256) + k, T, RLX_AGENT);
}
__device__ __forceinline__ int kperm(int r) { return 8*(r >> 3) + 4*(r & 1) + ((r >> 1) & 3); }
__device__ __forceinline__ u32x4 make_srd(const void* p, unsigned bytes) {
  unsigned long long a = (unsigned long long)p;
  return u32x4{(unsigned)a, (unsigned)(a >> 32) & 0xffffu, bytes, 0x00020000u};
}
__device__ __forceinline__ void SETM0(int v) { asm volatile("s_mov_b32 m0, %0\n\ts_nop 0" :: "s"(v) : "memory"); }
template<int OFF> __device__ __forceinline__ void dma_o(int voff, u32x4 srd, int soff) {
  asm volatile("buffer_load_dwordx4 %0, %1, %2 offen offset:%3 lds" :: "v"(voff), "s"(srd), "s"(soff), "i"(OFF) : "memory");
}
template<int OFF> __device__ __forceinline__ void dma_s(int voff, u32x4 srd, int soff) {
  asm volatile("buffer_load_dwordx4 %0, %1, %2 offen offset:%3 sc1 lds" :: "v"(voff), "s"(srd), "s"(soff), "i"(OFF) : "memory");
}
#define DMA(voff, srd, soff, OFF) dma_o<OFF>((voff), (srd), (soff))
#define DMAS(voff, srd, soff, OFF) dma_s<OFF>((voff), (srd), (soff))

#define PIN4(a) asm volatile("" : "+v"(a))
#define PIN_ACC(acc) asm volatile("" : "+v"(acc[0][0]), "+v"(acc[0][1]), "+v"(acc[1][0]), "+v"(acc[1][1]), "+v"(acc[2][0]), "+v"(acc[2][1]), \
  "+v"(acc[3][0]), "+v"(acc[3][1]), "+v"(acc[4][0]), "+v"(acc[4][1]), "+v"(acc[5][0]), "+v"(acc[5][1]), "+v"(acc[6][0]), "+v"(acc[6][1]), \
  "+v"(acc[7][0]), "+v"(acc[7][1]), "+v"(acc[8][0]), "+v"(acc[8][1]))
#define PIN5(a) asm volatile("" : "+v"(a[0]), "+v"(a[1]), "+v"(a[2]), "+v"(a[3]), "+v"(a[4]))
#define MFMA32(a, b, c) __builtin_amdgcn_mfma_f32_32x32x16_bf16((a), (b), (c), 0, 0, 0)
#define MFMA(a, b, c) __builtin_amdgcn_mfma_f32_16x16x32_bf16((a), (b), (c), 0, 0, 0)


extern "C" __global__ __launch_bounds__(512, 2) void fused_kernel(const u16* __restrict__ x, const u16* __restrict__ wqkv, const u16* __restrict__ wo,
    const u16* __restrict__ w_in, const u16* __restrict__ w_sp, const u16* __restrict__ b_sp, const u16* __restrict__ w_out,
    u16* __restrict__ out, char* __restrict__ ws)
{
  extern __shared__ __attribute__((aligned(16))) char smem[];
  LAS char* lds = (LAS char*)smem;
  const int tid = threadIdx.x, lane = tid & 63;
  const int wave = __builtin_amdgcn_readfirstlane(tid >> 6);
  const int w = wave & 3;
  const int b = blockIdx.x, xcd = b & 7, rr = b >> 3;
  const int pi = 4*(xcd & 3) + (rr & 3), pj = 8*(xcd >> 2) + (rr >> 2);
  if (wave == 7) {
    unsigned long long pc; unsigned off, sz;
    asm volatile("s_getpc_b64 %0\n.Lpf_here:\n\ts_mov_b32 %1, .Lpf_here-fused_kernel\n\ts_mov_b32 %2, .Lpf_end-fused_kernel" : "=s"(pc), "=s"(off), "=s"(sz));
    pc -= off;
    const u32x4 s_code = u32x4{(unsigned)pc, (unsigned)(pc >> 32) & 0xffffu, sz, 0x00020000u};
    const int per = 2048;
    SETM0(149504); DMA(lane*16, s_code, rr*per, 0); DMA(lane*16, s_code, rr*per, 1024);
  }
  unsigned T;

  int cv[9];
  u32x4 csrd = make_srd(w_in, 576*9216);
  int p1dst = (w & 1)*9216, p1so = 32*(w & 1)*9216 + (FH + 144*pj)*2, p1step = 64*9216;
  int cq[9];
  auto p1_offsets = [&](auto QKV, int (&dst)[9]) {
    int l3 = lane; OPAQUE(l3);
    int r = l3 / 18, ch = l3 % 18;
    #pragma unroll
    for (int t = 0; t < 9; t++) {
      const int kp = kperm(r);
      dst[t] = (QKV.value ? kp*3456 + (ch < 15 ? (ch / 5)*1152 + (ch % 5)*16 : 0) : kp*9216 + ch*16) - (t & 3)*1024;
      r += 3; ch += 10; if (ch >= 18) { ch -= 18; r += 1; }
    }
  };
  auto p1_issue = [&]() {
    SETM0(p1dst); DMA(cv[0], csrd, p1so, 0); DMA(cv[1], csrd, p1so, 1024); DMA(cv[2], csrd, p1so, 2048); DMA(cv[3], csrd, p1so, 3072);
    SETM0(p1dst + 4096); DMA(cv[4], csrd, p1so, 0); DMA(cv[5], csrd, p1so, 1024); DMA(cv[6], csrd, p1so, 2048); DMA(cv[7], csrd, p1so, 3072);
    SETM0(p1dst + 8192); DMA(cv[8], csrd, p1so, 0);
    p1dst = (p1dst >= 4*P1_STAGE) ? p1dst - 4*P1_STAGE : p1dst + P1_STAGE; p1so += p1step;
  };
  const bool isD = (wave == 4 || wave == 5);

  if (wave >= 4) __builtin_amdgcn_s_setprio(3);
  const int mt = wave >> 2;
  bf16x8 xn[36];
  {
    const u32x4 s_x = make_srd(x + (size_t)(128*pi + 32*w)*D_, 32*1152);
    const int a = lane >> 2, bq = lane & 3;
    unsigned tagv;
    asm volatile("global_load_dword %0, %1, %2 sc1" : "=v"(tagv) : "v"(0), "s"(ws) : "memory");
    u32x4 raw[18];
    {
      const int vx = (16*mt + a)*1152 + bq*16;
      static_for<0, 18>([&](auto C) { u32x4 t; asm volatile("buffer_load_dwordx4 %0, %1, %2, 0 offen offset:%3" : "=v"(t) : "v"(vx), "s"(s_x), "i"(C.value*64) : "memory"); raw[C.value] = t; });
    }
    if (isD) { p1_offsets(IC<0>{}, cv); p1_issue(); }
    if (isD) WAITV(9); else WAITV(0);
    asm volatile("" : "+v"(tagv));
    T = (unsigned)__builtin_amdgcn_readfirstlane(tagv) + 1u;
    asm volatile("" : "+v"(raw[0]), "+v"(raw[1]), "+v"(raw[2]), "+v"(raw[3]), "+v"(raw[4]), "+v"(raw[5]), "+v"(raw[6]), "+v"(raw[7]), "+v"(raw[8]),
                      "+v"(raw[9]), "+v"(raw[10]), "+v"(raw[11]), "+v"(raw[12]), "+v"(raw[13]), "+v"(raw[14]), "+v"(raw[15]), "+v"(raw[16]), "+v"(raw[17]));
    float s1 = 0.f, s2 = 0.f, s3 = 0.f, s4 = 0.f;
    unsigned ones = 0x3f803f80u; OPAQUE(ones);
    #pragma unroll
    for (int c = 0; c < 18; c++) {
      s1 = dot2(raw[c][0], ones, s1); s2 = dot2(raw[c][0], raw[c][0], s2); s3 = dot2(raw[c][1], ones, s3); s4 = dot2(raw[c][1], raw[c][1], s4);
      s1 = dot2(raw[c][2], ones, s1); s2 = dot2(raw[c][2], raw[c][2], s2); s3 = dot2(raw[c][3], ones, s3); s4 = dot2(raw[c][3], raw[c][3], s4);
    }
    s1 += s3; s2 += s4;
    s1 += dpp_f<0xB1>(s1); s1 += dpp_f<0x4E>(s1);
    s2 += dpp_f<0xB1>(s2); s2 += dpp_f<0x4E>(s2);
    const float mean = s1*(1.f/576.f), var = fmaxf(s2*(1.f/576.f) - mean*mean, 0.f);
    const float rs = __builtin_amdgcn_rsqf(var + 1e-5f), sh = -mean*rs;
    const f32x2 rs2 = {rs, rs}, sh2 = {sh, sh};
    int xw = XCH_OFF + w*18432 + (bq >> 1)*1024 + (32*(bq & 1) + ((16*mt + a) ^ (2*bq)))*16;
    int xr0 = XCH_OFF + w*18432 + (32*(lane >> 5) + ((lane & 31) ^ (2*(lane >> 5))))*16;
    int xr1 = XCH_OFF + w*18432 + 1024 + (32*(lane >> 5) + ((lane & 31) ^ (4 + 2*(lane >> 5))))*16;
    OPAQUE(xw); OPAQUE(xr0); OPAQUE(xr1);
    u32x4 xo[18];
    #pragma unroll
    for (int c = 0; c < 18; c++)
      #pragma unroll
      for (int d = 0; d < 4; d++) xo[c][d] = pack2(__builtin_elementwise_fma(f32x2{bf_lo(raw[c][d]), bf_hi(raw[c][d])}, rs2, sh2));
    #pragma unroll
    for (int c = 0; c < 9; c++) *(LAS u32x4*)(lds + xw + c*2048) = xo[c];
    WAITL(0);
    if (wave >= 6) {
      const u32x4 s_a = make_srd(w_in, 576*9216), s_b = make_srd(wqkv, 576*3456);
      SETM0(149504);
      const int vl = lane*16;
      #pragma unroll
      for (int k = 0; k < 11; k++) DMA(vl, s_a, b*20736 + ((w - 2)*11 + k)*1024, 0);
      #pragma unroll
      for (int k = 0; k < 4; k++) DMA(vl, s_b, b*7776 + ((w - 2)*4 + k)*1024, 0);
    }
    BAR();
    if (isD) p1_issue();
    if (wave < 4) {
      #pragma unroll
      for (int c = 0; c < 9; c++) { xn[2*c] = *(LAS bf16x8*)(lds + xr0 + c*2048); xn[2*c + 1] = *(LAS bf16x8*)(lds + xr1 + c*2048); }
      WAITL(0);
    }
    BAR();
    #pragma unroll
    for (int c = 0; c < 9; c++) *(LAS u32x4*)(lds + xw + c*2048) = xo[9 + c];
    if (isD) WAITV(9);
    WAITL(0);
    BAR();
    if (isD) { p1_issue(); p1_issue(); p1_offsets(IC<1>{}, cq); }
    if (wave < 4) {
      #pragma unroll
      for (int c = 0; c < 9; c++) { xn[18 + 2*c] = *(LAS bf16x8*)(lds + xr0 + c*2048); xn[18 + 2*c + 1] = *(LAS bf16x8*)(lds + xr1 + c*2048); }
    }
  }

  unsigned z1p[9][4];
  f32x4 acc[9][2];

  if (wave < 4) {
    constexpr int D = 3, NB = 4;
    int wl = (8*(lane >> 5) + 2*((lane >> 2) & 3))*288 + ((lane >> 4) & 1)*32 + (lane & 3)*8;
    int slot_off = 0;
    f32x16 a32[5];
    auto body = [&](auto NTC) {
      constexpr int NT = NTC.value, FS = 4*NT;
      bf16x8 wf[NB];
      int wc = wl + slot_off, wn;
      OPAQUE(wc);
      auto rdw = [&](auto G, int base) {
        constexpr int i = G.value % FS, k16 = i / NT, n = i % NT;
        LAS char* p = lds + base + (k16 >> 1)*9216 + (k16 & 1)*16*288 + n*64;
        s16x4 lo = __builtin_amdgcn_ds_read_tr16_b64_v4i16((LAS s16x4*)p);
        s16x4 hi = __builtin_amdgcn_ds_read_tr16_b64_v4i16((LAS s16x4*)(p + 288));
        wf[G.value % NB] = __builtin_shufflevector(lo, hi, 0, 1, 2, 3, 4, 5, 6, 7);
      };
      #pragma unroll
      for (int n = 0; n < NT; n++)
        #pragma unroll
        for (int e = 0; e < 16; e++) a32[n][e] = 0.f;
      static_for<0, D>([&](auto G) { rdw(G, wc); });
      SB();
            static_for<0, 9*FS>([&](auto G) {
        constexpr int st = G.value / FS, i = G.value % FS, k16 = i / NT, n = i % NT;
        if constexpr (i == 0) { slot_off = (slot_off >= 4*P1_STAGE) ? 0 : slot_off + P1_STAGE; wn = wl + slot_off; OPAQUE(wn); }
        if constexpr (i == FS - D) { SB(); if constexpr (NT == 5) PIN5(a32); else asm volatile("" : "+v"(a32[0]), "+v"(a32[1]), "+v"(a32[2]), "+v"(a32[3]));
          BAR(); SB(); }
        a32[n] = MFMA32(wf[G.value % NB], xn[4*st + k16], a32[n]);
        if constexpr (G.value + D < 9*FS) { if constexpr (i + D < FS) rdw(IC<G.value + D>{}, wc); else rdw(IC<G.value + D>{}, wn); }
        SG(0x8, 1); SG(0x100, 2);
        SB();
        if constexpr (i == FS - 1) wc = wn;
      });
      if constexpr (NT == 5) PIN5(a32); else asm volatile("" : "+v"(a32[0]), "+v"(a32[1]), "+v"(a32[2]), "+v"(a32[3]));
    };
    #pragma unroll 1
    for (int rep = 0; rep < 2; rep++) {
      asm volatile("" : "+s"(rep));
      body(IC<5>{});
      if (rep == 0) {
        {
      int l2 = lane; OPAQUE(l2);
      const int sr = l2 & 31, h4 = (l2 >> 5)*4;
      auto pk4 = [&](int n, int rq) { return u32x2{pack2(f32x2{a32[n][4*rq], a32[n][4*rq + 1]}), pack2(f32x2{a32[n][4*rq + 2], a32[n][4*rq + 3]})}; };
        f32x2 q1 = {0.f, 0.f}, q2 = {0.f, 0.f};
        LAS char* p = lds + ZB_OFF + w*IMG_W + sr*IMG_PITCH + h4*2;
        #pragma unroll
        for (int n = 0; n < 5; n++)
          #pragma unroll
          for (int rq = 0; rq < 4; rq++) if (n < 4 || rq < 2) {
            const f32x2 ya = gelu2(f32x2{a32[n][4*rq], a32[n][4*rq + 1]}), yb = gelu2(f32x2{a32[n][4*rq + 2], a32[n][4*rq + 3]});
            q1 += ya; q2 = __builtin_elementwise_fma(ya, ya, q2);
            q1 += yb; q2 = __builtin_elementwise_fma(yb, yb, q2);
            *(LAS u32x2*)(p + n*64 + rq*16) = u32x2{pack2(ya), pack2(yb)};
          }
        float t1 = q1[0] + q1[1], t2 = q2[0] + q2[1];
        t1 += __shfl_xor(t1, 32); t2 += __shfl_xor(t2, 32);
        const u32x4 rec = {__builtin_bit_cast(unsigned, t1), T, __builtin_bit_cast(unsigned, t2), T};
        if (l2 < 32) __builtin_amdgcn_raw_buffer_store_b128(rec, MKRSRC(ws + WS_STAT, 2048*256), ((pi*16 + pj)*128 + 32*w + sr)*16, 0, AUX_SC1);
        WAITL(0);
        }
        body(IC<4>{});
        {
      int l2 = lane; OPAQUE(l2);
      const int sr = l2 & 31, h4 = (l2 >> 5)*4;
      auto pk4 = [&](int n, int rq) { return u32x2{pack2(f32x2{a32[n][4*rq], a32[n][4*rq + 1]}), pack2(f32x2{a32[n][4*rq + 2], a32[n][4*rq + 3]})}; };
        LAS char* p = lds + QB_OFF + (16 + 32*w + sr)*QB_PITCH + h4*2;
        #pragma unroll
        for (int n = 0; n < 4; n++)
          #pragma unroll
          for (int rq = 0; rq < 4; rq++) if (n < 3 || rq < 3 || h4 == 0) *(LAS u32x2*)(p + n*64 + rq*16) = pk4(n, rq);
        WAITL(0);
        }
      }
    }
    {
      int l2 = lane; OPAQUE(l2);
      const int sr = l2 & 31, h4 = (l2 >> 5)*4;
      LAS char* p = lds + Z1B_OFF + w*IMG_W + sr*IMG_PITCH + h4*2;
      #pragma unroll
      for (int n = 0; n < 5; n++)
        #pragma unroll
        for (int rq = 0; rq < 4; rq++) if (n < 4 || rq < 2)
          *(LAS u32x2*)(p + n*64 + rq*16) = u32x2{pack2(f32x2{a32[n][4*rq], a32[n][4*rq + 1]}), pack2(f32x2{a32[n][4*rq + 2], a32[n][4*rq + 3]})};
      WAITL(0);
    }
    BAR();
  } else {
    const int g = lane >> 4, fl = lane & 15;
    __builtin_amdgcn_s_setprio(3);
    if (isD) {
      WAITV(18); BAR();
      #pragma unroll 1
      for (int q = 1; q < 6; q++) { p1_issue(); WAITV(18); BAR(); }
      #pragma unroll
      for (int t = 0; t < 9; t++) { const int t_ = cv[t]; cv[t] = cq[t]; cq[t] = t_; }
      csrd = make_srd(wqkv, 576*3456); p1so = 32*(w & 1)*3456 + ((72*pj) & ~15); p1step = 64*3456;
      #pragma unroll 1
      for (int q = 6; q < 15; q++) { p1_issue(); WAITV(18); BAR(); }
      #pragma unroll
      for (int t = 0; t < 9; t++) cv[t] = cq[t];
      csrd = make_srd(w_in, 576*9216); p1so = 32*(w & 1)*9216 + 144*pj*2; p1step = 64*9216;
      #pragma unroll 1
      for (int q = 15; q < 24; q++) {
        if (q == 19 && wave == 4 && pi < 15) {
          const RSRC r_halo = MKRSRC(ws + WS_HALO + (size_t)(pi*16 + pj)*4096, 4096);
          #pragma unroll
          for (int c = 0; c < 4; c++) { int ch = c*64 + lane; if (ch > 247) ch = 247;
            u32x4 v = *(LAS u32x4*)(lds + QB_OFF + 128*QB_PITCH + ch*16);
            __builtin_amdgcn_raw_buffer_store_b128(v, r_halo, ch*16, 0, AUX_SC1); }
        }
        p1_issue(); WAITV(18); BAR();
        if (q == 20 && wave == 4 && lane == 0 && pi < 15) set_flag(ws, FL_HALO, pi, pj, T);
      }
      u32x4 hv[4]; unsigned hf = T;
      const bool hal = (wave == 5 && pi > 0);
      gu32* fp = (gu32*)(ws + WS_FLAGS + (size_t)(FL_HALO*16 + (pi > 0 ? pi - 1 : 0))*256) + pj;
      if (hal) asm volatile("global_load_dword %0, %1, %2 sc1" : "=v"(hf) : "v"(0), "s"(fp) : "memory");
      if (hal) WAITV(10); else WAITV(9);
      BAR();
      if (hal) {
        asm volatile("s_waitcnt vmcnt(0)" : "+v"(hf) :: "memory");
        if (__builtin_amdgcn_readfirstlane(hf) != T)
          for (int spin = 0; spin < SPIN_MAX; ++spin) { if (__builtin_amdgcn_readfirstlane(__hip_atomic_load(fp, RLX_AGENT)) == T) break; __builtin_amdgcn_s_sleep(1); }
        const u32x4 s_hal = make_srd(ws + WS_HALO + (size_t)((pi - 1)*16 + pj)*4096, 4096);
        static_for<0, 4>([&](auto C) { int ch = C.value*64 + lane; if (ch > 247) ch = 247; u32x4 t;
          asm volatile("buffer_load_dwordx4 %0, %1, %2, 0 offen sc1" : "=v"(t) : "v"(ch*16), "s"(s_hal) : "memory"); hv[C.value] = t; });
      } else WAITV(0);
      BAR();
      if (wave == 5) {
        if (pi > 0) asm volatile("s_waitcnt vmcnt(0)" : "+v"(hv[0]), "+v"(hv[1]), "+v"(hv[2]), "+v"(hv[3]) :: "memory");
        #pragma unroll
        for (int c = 0; c < 4; c++) { int ch = c*64 + lane; if (ch > 247) ch = 247; *(LAS u32x4*)(lds + QB_OFF + ch*16) = (pi > 0) ? hv[c] : u32x4{0, 0, 0, 0}; }
        WAITL(0);
      }
      BAR();
    } else {
      const int gA = 2*(w - 2);
      {
        const u32x4 s_a = make_srd(w_sp, 2048*4096);
        SETM0(149504);
        const int vl = lane*16;
        #pragma unroll 1
        for (int q = 0; q < 16; q++) { DMA(vl, s_a, b*32768 + ((w - 2)*16 + q)*1024, 0); BAR(); }
      }
      LAS f32x2* st = (LAS f32x2*)(lds + MISC_OFF + (w - 2)*512);
      {
        const u32x4 s_st = make_srd(ws + WS_STAT, 2048*256);
        const int vo = (pi*16*128 + 32*gA + lane)*16;
        u32x4 rec[16];
        static_for<0, 16>([&](auto C) { u32x4 t; asm volatile("buffer_load_dwordx4 %0, %1, %2, %3 offen sc1" : "=v"(t) : "v"(vo), "s"(s_st), "s"(C.value*2048) : "memory"); rec[C.value] = t; });
        WAITV(16);
        BAR();
        BAR();
        asm volatile("s_waitcnt vmcnt(0)" : "+v"(rec[0]), "+v"(rec[1]), "+v"(rec[2]), "+v"(rec[3]), "+v"(rec[4]), "+v"(rec[5]), "+v"(rec[6]), "+v"(rec[7]),
                          "+v"(rec[8]), "+v"(rec[9]), "+v"(rec[10]), "+v"(rec[11]), "+v"(rec[12]), "+v"(rec[13]), "+v"(rec[14]), "+v"(rec[15]) :: "memory");
        float t1 = 0.f, t2 = 0.f; bool ok = true;
        #pragma unroll
        for (int c = 0; c < 16; c++) { t1 += asf(rec[c][0]); t2 += asf(rec[c][2]); ok = ok && rec[c][1] == T && rec[c][3] == T; }
        if (!__all(ok)) {
          const RSRC r_stat = MKRSRC(ws + WS_STAT, 2048*256);
          for (int spin = 0; spin < SPIN_MAX; ++spin) {
            t1 = 0.f; t2 = 0.f; ok = true;
            #pragma unroll
            for (int c = 0; c < 16; c++) {
              u32x4 v = __builtin_amdgcn_raw_buffer_load_b128(r_stat, vo + c*2048, 0, AUX_SC1);
              t1 += asf(v[0]); t2 += asf(v[2]); ok = ok && v[1] == T && v[3] == T;
            }
            if (__all(ok)) break;
            __builtin_amdgcn_s_sleep(1);
          }
        }
        const float mean = t1*(1.f/2304.f), var = fmaxf(t2*(1.f/2304.f) - mean*mean, 0.f);
        const float rs = __builtin_amdgcn_rsqf(var + 1e-5f);
        st[lane] = f32x2{rs, -mean*rs};
        WAITL(0);
        BAR();
      }
      const RSRC r_z2n = MKRSRC(ws + WS_Z2N, 16*ZJS);
      #pragma unroll 1
      for (int k = 0; k < 2; k++) {
        f32x2 r2[4], h2[4];
        #pragma unroll
        for (int d = 0; d < 4; d++) { f32x2 u0 = st[32*k + 8*g + 2*d], u1 = st[32*k + 8*g + 2*d + 1]; r2[d] = f32x2{u0[0], u1[0]}; h2[d] = f32x2{u0[1], u1[1]}; }
        LAS char* p = lds + ZB_OFF + (gA + k)*IMG_W + (8*g + ((lane >> 2) & 3))*IMG_PITCH + (lane & 3)*8;
        int zo = lane*16 + pj*ZJS + (4*pi + gA + k)*9216;
        #pragma unroll 1
        for (int j3 = 0; j3 < 3; j3++, p += 96, zo += 3072) {
          u32x4 v[3];
          #pragma unroll
          for (int u = 0; u < 3; u++) {
            s16x4 lo = __builtin_amdgcn_ds_read_tr16_b64_v4i16((LAS s16x4*)(p + u*32));
            s16x4 hi = __builtin_amdgcn_ds_read_tr16_b64_v4i16((LAS s16x4*)(p + u*32 + 4*IMG_PITCH));
            v[u] = __builtin_bit_cast(u32x4, __builtin_shufflevector(lo, hi, 0, 1, 2, 3, 4, 5, 6, 7));
          }
          #pragma unroll
          for (int u = 0; u < 3; u++) {
            u32x4 o;
            #pragma unroll
            for (int d = 0; d < 4; d++) o[d] = pack2(f32x2{opq(__builtin_fmaf(bf_lo(v[u][d]), r2[d][0], h2[d][0])), opq(__builtin_fmaf(bf_hi(v[u][d]), r2[d][1], h2[d][1]))});
            __builtin_amdgcn_raw_buffer_store_b128(o, r_z2n, zo + u*1024, 0, AUX_SC1);
          }
          BAR();
        }
      }
      BAR();
      BAR();
    }
    BAR();
    if (!isD) { WAITV(0); if (lane == 0) set_flag(ws, FL_Z, pj, 2*pi + (w - 2), T); }
    {
      #pragma unroll
      for (int m = 0; m < 2; m++) {
        LAS char* zx = lds + Z1B_OFF + w*IMG_W + (8*(fl >> 2) + 4*m + (fl & 3))*IMG_PITCH + g*8;
        #pragma unroll
        for (int n = 0; n < 9; n++) { u32x2 v = *(LAS u32x2*)(zx + n*32); z1p[n][2*m] = v[0]; z1p[n][2*m + 1] = v[1]; }
      }
    }
  }

  const u32x4 s_z2n = make_srd(ws + WS_Z2N + (size_t)pj*ZJS, 64*9*1024);
  const u32x4 s_wsp = make_srd(w_sp + (size_t)128*pi*2048, 128*4096);
  int vw[4];
  const int vlin = lane*16;
  auto p2_issue = [&](int s, auto ZR) {
    const int base = (s & 3)*P2_STAGE;
    if constexpr (ZR.value) {
      static_for<0, 9>([&](auto K) { SETM0(base + w*9216 + K.value*1024); DMAS(vlin, s_z2n, ((2*s + w)*9 + K.value)*1024, 0); });
    } else {
      static_for<0, 8>([&](auto K) { constexpr int k = K.value; const int wr = 2*(w - 2) + (k >> 2);
        SETM0(base + 18432 + wr*4096 + (k & 3)*1024); DMA(vw[k & 3], s_wsp, wr*32*4096 + s*128, 0); });
    }
  };
  if (wave >= 4) {
    { int l2 = lane; OPAQUE(l2);
      const int a = l2 >> 3, bb = l2 & 7;
      #pragma unroll
      for (int m = 0; m < 2; m++)
        #pragma unroll
        for (int hf = 0; hf < 2; hf++) { int rho = 8*hf + a; vw[2*m + hf] = (8*(rho >> 2) + 4*m + (rho & 3))*4096 + ((bb ^ ((rho >> 1) & 7))*16); } }
    if (w >= 2) {
      #pragma unroll 1
      for (int s = 0; s < 2; s++) p2_issue(s, IC<0>{});
    }
  }
  {
      int t2 = tid; OPAQUE(t2);
      const int u = t2 & 1, hh = (t2 >> 7) & 1, tl = ((t2 >> 1) & 63) + 64*(t2 >> 8);
      LAS char* rowp = lds + QB_OFF + (16 + tl)*QB_PITCH + 8*(pj & 1) + 36*hh;
      const int o8 = hh*4, o4 = hh ? 0 : 32;
      u32x2 qv[4]; unsigned q0;
      #pragma unroll
      for (int d = 0; d < 4; d++) qv[d] = *(LAS u32x2*)(rowp + o8 + d*8);
      q0 = *(LAS unsigned*)(rowp + o4);
      float lg[4];
      #pragma unroll
      for (int ww = 0; ww < 4; ww++) {
        LAS char* kr = rowp - (2*(4*u + ww))*QB_PITCH + 80;
        float sa = dot2(q0, *(LAS unsigned*)(kr + o4), 0.f), sb = 0.f;
        #pragma unroll
        for (int d = 0; d < 4; d++) { u32x2 kv = *(LAS u32x2*)(kr + o8 + d*8); sa = dot2(qv[d][0], kv[0], sa); sb = dot2(qv[d][1], kv[1], sb); }
        float s = (sa + sb)*0.23570226039551584f;
        if (128*pi + tl - 2*(4*u + ww) < 0) s = -1e9f;
        lg[ww] = s;
      }
      float l8[8];
      #pragma unroll
      for (int ww = 0; ww < 4; ww++) { float o = dpp_f<0xB1>(lg[ww]); l8[ww] = u ? o : lg[ww]; l8[4 + ww] = u ? lg[ww] : o; }
      float mx = l8[0];
      #pragma unroll
      for (int ww = 1; ww < 8; ww++) mx = fmaxf(mx, l8[ww]);
      float sum = 0.f;
      #pragma unroll
      for (int ww = 0; ww < 8; ww++) { l8[ww] = __builtin_amdgcn_exp2f((l8[ww] - mx)*1.4426950408889634f); sum += l8[ww]; }
      const float inv = __builtin_amdgcn_rcpf(sum);
      float olo[5], ohi[5];
      #pragma unroll
      for (int d = 0; d < 5; d++) { olo[d] = 0.f; ohi[d] = 0.f; }
      #pragma unroll
      for (int ww = 0; ww < 8; ww++) {
        const unsigned pb = pack_bf16(l8[ww]*inv, 0.f);
        const unsigned ph = pb << 16;
        LAS char* vr = rowp - (2*ww)*QB_PITCH + 160;
        u32x2 va = *(LAS u32x2*)(vr + o8 + 16*u), vb = *(LAS u32x2*)(vr + o8 + 16*u + 8); unsigned vc = *(LAS unsigned*)(vr + o4);
        olo[0] = dot2(va[0], pb, olo[0]); ohi[0] = dot2(va[0], ph, ohi[0]); olo[1] = dot2(va[1], pb, olo[1]); ohi[1] = dot2(va[1], ph, ohi[1]);
        olo[2] = dot2(vb[0], pb, olo[2]); ohi[2] = dot2(vb[0], ph, ohi[2]); olo[3] = dot2(vb[1], pb, olo[3]); ohi[3] = dot2(vb[1], ph, ohi[3]);
        olo[4] = dot2(vc, pb, olo[4]); ohi[4] = dot2(vc, ph, ohi[4]);
      }
      *(LAS u32x2*)(rowp + o8 + 16*u) = u32x2{pack_bf16(olo[0], ohi[0]), pack_bf16(olo[1], ohi[1])};
      *(LAS u32x2*)(rowp + o8 + 16*u + 8) = u32x2{pack_bf16(olo[2], ohi[2]), pack_bf16(olo[3], ohi[3])};
      if (u) *(LAS unsigned*)(rowp + o4) = pack_bf16(olo[4], ohi[4]);
      }
  WAITL(0);
  BAR();
  if (wave >= 4 && w < 2) { wait_flags(ws, FL_Z, pj, 32, T, lane); p2_issue(0, IC<1>{}); p2_issue(1, IC<1>{}); }
  if (wave >= 4) {
    #pragma unroll
    for (int n = 0; n < 3; n++) {
      #pragma unroll
      for (int d = 0; d < 4; d++) z1p[n][d] = pack2(gelu2(f32x2{bf_lo(z1p[n][d]), bf_hi(z1p[n][d])}));
      asm volatile("" : "+v"(z1p[n][0]), "+v"(z1p[n][1]), "+v"(z1p[n][2]), "+v"(z1p[n][3]));
    }
  }
  if (wave < 4) {
    const RSRC r_o = MKRSRC(ws + WS_O, 128*OTS);
    if (tid < 252) {
      const int r0 = tid / 9, p = tid % 9;
      const int col = 36*pj + 4*p, c32 = col & 31;
      const int cpart = ((col >> 5)*64 + 16*((c32 & 15) >> 2))*16 + (c32 >> 4)*8;
      LAS char* src = lds + QB_OFF + (16 + r0)*QB_PITCH + 8*(pj & 1) + 8*p;
      #pragma unroll
      for (int it = 0; it < 5; it++) {
        const int tl = r0 + 28*it;
        if (tl < 128) {
          u32x2 v = *(LAS u32x2*)(src + it*28*QB_PITCH);
          const int r32 = tl & 31, tt = 8*pi + 2*(tl >> 5) + ((r32 >> 2) & 1), rho = 4*(r32 >> 3) + (r32 & 3);
          __builtin_amdgcn_raw_buffer_store_b64(v, r_o, tt*OTS + rho*16 + cpart, 0, AUX_SC1);
        }
      }
    }
  }
  WAITL(0);
  BAR();
  const int p3cnt = (xcd < 2) ? 30 : 29;
  const int p3T = xcd*29 + (xcd < 2 ? xcd : 2) + rr;
  const bool p3act = rr < p3cnt;
  const int rg = p3T / 9, pc = p3T % 9;

  if (wave >= 4) {
    __builtin_amdgcn_s_setprio(0);
    auto p2_helper = [&](auto ZR) {
      constexpr int CNT = ZR.value ? 9 : 8;
      p2_issue(2, ZR); p2_issue(3, ZR);
      WAITV(3*CNT);
      BAR();
      auto piece = [&](int s, auto K) {
        constexpr int k = K.value; const int base = (s & 3)*P2_STAGE;
        if constexpr (ZR.value) { SETM0(base + w*9216 + k*1024); DMAS(vlin, s_z2n, ((2*s + w)*9 + k)*1024, 0); }
        else if constexpr (k < 8) { const int wr = 2*(w - 2) + (k >> 2); SETM0(base + 18432 + wr*4096 + (k & 3)*1024); DMA(vw[k & 3], s_wsp, wr*32*4096 + s*128, 0); }
      };
      auto step = [&](int s, auto PR) {
        WAITV(2*CNT);
        BAR();
        static_for<0, 9>([&](auto K) {
          piece(s + 4, K);
          if constexpr (PR.value >= 0 && K.value == 2) {
            constexpr int pr = PR.value;
            unsigned rr2 = pack2(f32x2{opq(gelu_f(opq(bf_lo(z1p[pr / 4][pr % 4])))), opq(gelu_f(opq(bf_hi(z1p[pr / 4][pr % 4]))))});
            asm volatile("" : "+v"(rr2));
            z1p[pr / 4][pr % 4] = rr2;
          }
        });
      };
      static_for<0, 24>([&](auto S) {
        step(S.value, IC<12 + S.value>{});
        if constexpr (S.value == 4) { if (wave == 4 && lane == 0) set_flag(ws, FL_O, pi, pj, T); }
      });
      #pragma unroll 1
      for (int s = 24; s < 28; s++) step(s, IC<-1>{});
      WAITV(2*CNT); BAR();
      WAITV(CNT); BAR();
      #pragma unroll
      for (int n = 0; n < 9; n++) *(LAS u32x4*)(lds + w*9216 + n*1024 + lane*16) = u32x4{z1p[n][0], z1p[n][1], z1p[n][2], z1p[n][3]};
      WAITV(0); WAITL(0); BAR();
      BAR();
    };
    if (w < 2) p2_helper(IC<1>{}); else p2_helper(IC<0>{});
    if (p3act) {
      const u32x4 s_o = make_srd(ws + WS_O, 128*OTS), s_h = make_srd(ws + WS_H, 128*HTS);
      const u32x4 s_wo = make_srd(wo, 576*1152), s_wout = make_srd(w_out, 2304*1152);
      int vW0, vW1, vA[5];
      int l2 = lane; OPAQUE(l2);
      { const int r = l2 >> 3;
        vW0 = r*1152 + (((l2 & 7) ^ (((r >> 1) & 3) << 1))*16);
        vW1 = (r + 8)*1152 + (((l2 & 7) ^ ((((r + 8) >> 1) & 3) << 1))*16);
        #pragma unroll
        for (int m5 = 0; m5 < 5; m5++) { int tt = 5*rg + m5; if (tt > 127) tt = 127; vA[m5] = l2*16 + tt*OTS; } }
      int slot = 2;
      auto p3_w = [&](int sl, u32x4 sw, int R0, int R1) {
        const int base = sl*P3_STAGE + w*9216, s0 = R0*1152 + pc*128, s1 = R1*1152 + pc*128;
        SETM0(base + 5120); DMA(vW0, sw, s0, 0); SETM0(base + 6144); DMA(vW1, sw, s0, 0);
        SETM0(base + 7168); DMA(vW0, sw, s1, 0); SETM0(base + 8192); DMA(vW1, sw, s1, 0);
      };
      auto p3_a = [&](int sl, u32x4 sa, int ks) {
        const int base = sl*P3_STAGE + w*9216;
        static_for<0, 5>([&](auto M) { SETM0(base + M.value*1024); DMAS(vA[M.value], sa, ks*1024, 0); });
      };
      auto adv = [&]() { slot = (slot == 4) ? 0 : slot + 1; };
      auto p3_wo = [&](int u) { const int ks = 3*u + w; if (w < 3) { p3_w(slot, s_wo, 32*ks, 32*ks + 16); p3_a(slot, s_o, ks); } adv(); };
      auto g3_w = [&](int sl, int v) { const int ks = 3*v + w;
        const int R0 = (ks < 64) ? 144*(ks >> 2) + 32*(ks & 3) : 288*(ks - 64) + 128;
        p3_w(sl, s_wout, R0, R0 + ((ks < 64) ? 16 : 144)); };
      auto p3_g3 = [&](int v) { if (w < 3) { g3_w(slot, v); p3_a(slot, s_h, 3*v + w); } adv(); };
      const int p0 = (5*rg) >> 3, p1 = ((5*rg + 4 > 127 ? 127 : 5*rg + 4)) >> 3;
      if (w < 3) { wait_flags(ws, FL_O, p0, 16, T, lane); wait_flags(ws, FL_O, p1, 16, T, lane); }
      #pragma unroll 1
      for (int u = 0; u < 3; u++) p3_wo(u);
      BAR();
      p3_wo(3); p3_wo(4);
      WAITV(27);
      BAR();
      WAITV(18); BAR();
      p3_wo(5); WAITV(18); BAR();
      WAITV(9); BAR();
      WAITV(0); BAR();
      BAR();
      if (w < 3) { g3_w(3, 0); g3_w(4, 1); g3_w(0, 2); g3_w(1, 3); }
      #pragma unroll
      for (int m5 = 0; m5 < 5; m5++) { int tt = 5*rg + m5; if (tt > 127) tt = 127; vA[m5] = l2*16 + tt*HTS; }
      if (w < 3) { wait_flags(ws, FL_H, p0, 16, T, lane); wait_flags(ws, FL_H, p1, 16, T, lane); }
      if (w < 3) { p3_a(3, s_h, w); p3_a(4, s_h, 3 + w); p3_a(0, s_h, 6 + w); p3_a(1, s_h, 9 + w); }
      slot = 2;
      WAITV(10); BAR();
      p3_g3(4); WAITV(14); BAR();
      #pragma unroll 1
      for (int u = 7; u < 26; u++) {
        p3_g3(u - 2);
        WAITV(18); BAR();
      }
      WAITV(9); BAR();
      WAITV(0); BAR();
      BAR();
    }
  } else {
    int l2 = lane; OPAQUE(l2);
    const int g = l2 >> 4, fl = l2 & 15;
    const int zoff = l2*16;
    const int woff0 = 18432 + (4*w + (fl >> 3))*1024 + (fl & 7)*128 + ((g ^ ((fl >> 1) & 7))*16);
    float bs[2];
    #pragma unroll
    for (int m = 0; m < 2; m++) bs[m] = bf_lo(b_sp[128*pi + 32*w + 8*(fl >> 2) + 4*m + (fl & 3)]);
    bf16x8 z0[9], w0[2], z1[9], w1[2];
    auto rd = [&](bf16x8 (&z)[9], bf16x8 (&wv)[2], int s, int kk) {
      const int base = (s & 3)*P2_STAGE;
      LAS char* pw = lds + base + (woff0 ^ (kk*64));
      LAS char* pz = lds + base + kk*9216 + zoff;
      wv[0] = *(LAS bf16x8*)pw; wv[1] = *(LAS bf16x8*)(pw + 2048);
      #pragma unroll
      for (int n = 0; n < 9; n++) z[n] = *(LAS bf16x8*)(pz + n*1024);
    };
    auto mm = [&](bf16x8 (&z)[9], bf16x8 (&wv)[2]) {
      #pragma unroll
      for (int n = 0; n < 9; n++) { acc[n][0] = MFMA(z[n], wv[0], acc[n][0]); acc[n][1] = MFMA(z[n], wv[1], acc[n][1]); }
      #pragma unroll
      for (int q = 0; q < 11; q++) { SG(0x8, 1); SG(0x100, 1); }
      SG(0x8, 7);
    };
    asm volatile("" : "+v"(bs[0]), "+v"(bs[1]));
    #pragma unroll
    for (int n = 0; n < 9; n++) { acc[n][0] = f32x4{bs[0], bs[0], bs[0], bs[0]}; acc[n][1] = f32x4{bs[1], bs[1], bs[1], bs[1]}; }
    {
      const u32x4 s_b = make_srd(w_out, 2304*1152), s_c = make_srd(wo, 576*1152);
      SETM0(MISC_OFF + 1024);
      #pragma unroll
      for (int k = 0; k < 3; k++) if (w + 4*k < 11) DMA(zoff, s_b, b*10368 + (w + 4*k)*1024, 0);
      if (w < 3) DMA(zoff, s_c, b*2592 + w*1024, 0);
    }
    BAR();
    rd(z0, w0, 0, 0);
    CWAITL0(); SB();
    #pragma unroll 1
    for (int s = 0; s < 32; s++) {
      CWAITL0(); SB();
      rd(z1, w1, s, 1);
      mm(z0, w0);
      SB(); CWAITL0(); PIN_ACC(acc);
      if (s == 4) WAITV(0);
      BAR(); SB();
      rd(z0, w0, (s + 1 < 32 ? s + 1 : s), 0);
      mm(z1, w1);
      SB();
    }
    {
      const RSRC r_h = MKRSRC(ws + WS_H, 128*HTS);
      u32x4 z1g[9];
      #pragma unroll
      for (int n = 0; n < 9; n++) z1g[n] = *(LAS u32x4*)(lds + w*9216 + n*1024 + zoff);
      #pragma unroll
      for (int m = 0; m < 2; m++) {
        const int tt = 8*pi + 2*w + m;
        unsigned hp[9][2];
        #pragma unroll
        for (int n = 0; n < 9; n++) {
          hp[n][0] = pack2(f32x2{bf_lo(z1g[n][2*m]), bf_hi(z1g[n][2*m])}*f32x2{acc[n][m][0], acc[n][m][1]});
          hp[n][1] = pack2(f32x2{bf_lo(z1g[n][2*m + 1]), bf_hi(z1g[n][2*m + 1])}*f32x2{acc[n][m][2], acc[n][m][3]});
        }
        #pragma unroll
        for (int q = 0; q < 4; q++)
          __builtin_amdgcn_raw_buffer_store_b128(u32x4{hp[2*q][0], hp[2*q][1], hp[2*q + 1][0], hp[2*q + 1][1]}, r_h, zoff + tt*HTS + (4*pj + q)*1024, 0, AUX_SC1);
        __builtin_amdgcn_raw_buffer_store_b64(u32x2{hp[8][0], hp[8][1]}, r_h, zoff + (pj & 1)*8 + tt*HTS + (64 + (pj >> 1))*1024, 0, AUX_SC1);
      }
    }
    if (p3act) {
      WAITL(0);
      BAR();
      f32x4 aS[5], aC[5];
      u32x2 xv[5];
      const int q4 = (l2 >> 2) & 3, p4 = l2 & 3, r0 = 4*g + q4;
      const int woff = 5120 + r0*128 + (((2*w + (p4 >> 1)) ^ (((r0 >> 1) & 3) << 1))*16) + (p4 & 1)*8;
      bf16x8 hA[3][5], wA[3], hB[3][5], wB[3];
      int slot = 2;
      auto rd3 = [&](bf16x8 (&h)[3][5], bf16x8 (&wv)[3]) {
        LAS char* p = lds + slot*P3_STAGE;
        #pragma unroll
        for (int kk = 0; kk < 3; kk++) {
          s16x4 lo = __builtin_amdgcn_ds_read_tr16_b64_v4i16((LAS s16x4*)(p + kk*9216 + woff));
          s16x4 hi = __builtin_amdgcn_ds_read_tr16_b64_v4i16((LAS s16x4*)(p + kk*9216 + woff + 2048));
          wv[kk] = __builtin_shufflevector(lo, hi, 0, 1, 2, 3, 4, 5, 6, 7);
          #pragma unroll
          for (int m5 = 0; m5 < 5; m5++) h[kk][m5] = *(LAS bf16x8*)(p + kk*9216 + m5*1024 + zoff);
        }
        slot = (slot == 4) ? 0 : slot + 1;
      };
      auto mm3 = [&](bf16x8 (&h)[3][5], bf16x8 (&wv)[3]) {
        #pragma unroll
        for (int kk = 0; kk < 3; kk++)
          #pragma unroll
          for (int m5 = 0; m5 < 5; m5++) aC[m5] = MFMA(wv[kk], h[kk][m5], aC[m5]);
        #pragma unroll
        for (int q = 0; q < 15; q++) { SG(0x8, 1); SG(0x100, 1); }
        SG(0x100, 6);
      };
      #pragma unroll
      for (int m5 = 0; m5 < 5; m5++) {
        int tt = 5*rg + m5; if (tt > 127) tt = 127;
        const int row = 32*(tt >> 1) + 8*(fl >> 2) + 4*(tt & 1) + (fl & 3);
        xv[m5] = *(const u32x2*)(x + (size_t)row*D_ + 64*pc + 16*w + 4*g);
      }
      BAR();
      #pragma unroll 1
      for (int ph = 0; ph < 2; ph++) {
        #pragma unroll
        for (int m5 = 0; m5 < 5; m5++) aC[m5] = f32x4{0,0,0,0};
        rd3(hA, wA); SB();
        const int nit = ph ? 12 : 3;
        #pragma unroll 1
        for (int it = 0; it < nit; it++) {
          CWAITL0(); SB(); rd3(hB, wB); mm3(hA, wA); SB(); PIN5(aC); BAR(); SB();
          CWAITL0(); SB(); rd3(hA, wA); mm3(hB, wB); SB(); PIN5(aC);
          if (ph == 0 && it == 1) { WAITV(0); }
          if (ph == 0 || it < 11) BAR();
          SB();
          if (ph == 0 && it == 1 && tid == 0) set_flag(ws, FL_H, pi, pj, T);
        }
        if (ph == 0) {
          #pragma unroll
          for (int m5 = 0; m5 < 5; m5++) aS[m5] = aC[m5];
          slot = 3;
        }
      }
      #pragma unroll
      for (int m5 = 0; m5 < 5; m5++) {
        const int tt = 5*rg + m5;
        if (tt < 128) {
          const int row = 32*(tt >> 1) + 8*(fl >> 2) + 4*(tt & 1) + (fl & 3);
          const size_t o = (size_t)row*D_ + 64*pc + 16*w + 4*g;
          float r[4];
          #pragma unroll
          for (int e = 0; e < 4; e++) {
            float bv = aC[m5][e], sv = aS[m5][e];
            float sl = bv*__builtin_amdgcn_rcpf(1.f + __builtin_amdgcn_exp2f(-1.4426950408889634f*bv));
            unsigned xw = xv[m5][e >> 1];
            float xx = (e & 1) ? bf_hi(xw) : bf_lo(xw);
            r[e] = __builtin_fmaf(sv, sl, xx);
          }
          *(u32x2*)(out + o) = u32x2{pack_bf16(r[0], r[1]), pack_bf16(r[2], r[3])};
        }
      }
    } else {
      WAITV(0);
      BAR();
      if (tid == 0) set_flag(ws, FL_H, pi, pj, T);
    }
    if (b == 0 && tid == 0) __hip_atomic_store((gu32*)ws, T, RLX_AGENT);
  }
  asm volatile(".Lpf_end:");
}

extern "C" void kernel_launch(void* const* d_in, const int* in_sizes, int n_in, void* d_out, int out_size, void* d_ws, size_t ws_size, hipStream_t stream) {
  (void)hipFuncSetAttribute((const void*)fused_kernel, hipFuncAttributeMaxDynamicSharedMemorySize, LDS_BYTES);
  fused_kernel<<<256, 512, LDS_BYTES, stream>>>((const u16*)d_in[0], (const u16*)d_in[1], (const u16*)d_in[2], (const u16*)d_in[4],
      (const u16*)d_in[5], (const u16*)d_in[6], (const u16*)d_in[7], (u16*)d_out, (char*)d_ws);
}
```
